```python
import torch

def _softmax(x, axis=-1):
    return torch.softmax(x, dim=axis)
def _gelu(x):
    return torch.nn.functional.gelu(x, approximate="tanh")
def _silu(x):
    return torch.nn.functional.silu(x)
def _sigmoid(x):
    return torch.sigmoid(x)
def _layernorm(x, axis=-1, eps=1e-5):
    m = x.mean(dim=axis, keepdim=True)
    v = x.var(dim=axis, keepdim=True, unbiased=False)
    return (x - m) / torch.sqrt(v + eps)
def _scatter_add(tgt, idx, src, axis=0):
    return tgt.clone().index_add_(axis, idx, src)
def _segment_sum(src, seg, n):
    out = torch.zeros((n,) + src.shape[1:], dtype=src.dtype)
    return out.index_add_(0, seg, src)
def _pad(x, pw):
    flat = tuple(v for pair in reversed(pw) for v in pair)
    return torch.nn.functional.pad(x, flat)

kernel_name = 'concat_heads__sliding_local_dilated__ssm_conv_h3__prompt'

def setup_inputs():

    _w = (lambda a: (lambda t: t.to(torch.bfloat16) if t.is_floating_point() else t)(torch.as_tensor(a)))
    def _A_setup():
        B, S, D = 1, 2048, 576
        return {
            "x": _w((lambda *s: torch.randn(*s, dtype=torch.bfloat16) * 0.2)(B, S, D)),
            "wqkv": _w((lambda *s: torch.randn(*s, dtype=torch.bfloat16) * 0.2)(D, 3 * D)),
            "wo": _w((lambda *s: torch.randn(*s, dtype=torch.bfloat16) * 0.05)(D, D)),
        }
    _A_in = _A_setup()

    def _B_setup():
        B, S, D = 1, 2048, 576
        ks = 4
        return {
            "q": _w((lambda *s: torch.randn(*s, dtype=torch.bfloat16) * 0.05)(B, S, D)),
            "k": _w((lambda *s: torch.randn(*s, dtype=torch.bfloat16) * 0.8)(B, S, D)),
            "v": _w((lambda *s: torch.randn(*s, dtype=torch.bfloat16) * 0.8)(B, S, D)),
            "shift_k": _w((lambda *s: torch.randn(*s, dtype=torch.bfloat16) * 0.8)(D, ks) * 0.5),
            "ssm_k": _w((lambda *s: torch.randn(*s, dtype=torch.bfloat16) * 3.2)(D, S) * (1.0 / S ** 0.5)),
            "ssm_D": _w((lambda *s: torch.randn(*s, dtype=torch.bfloat16) * 0.8)(D)),
        }
    _B_in = _B_setup()

    return {
        "A_x": _A_in["x"],
        "A_wqkv": _A_in["wqkv"],
        "A_wo": _A_in["wo"],
        "B_q": _B_in["q"],
        "B_k": _B_in["k"],
        "B_v": _B_in["v"],
        "B_shift_k": _B_in["shift_k"],
        "B_ssm_k": _B_in["ssm_k"],
        "B_ssm_D": _B_in["ssm_D"],
    }

def reference(A_x, A_wqkv, A_wo, B_q, B_k, B_v, B_shift_k, B_ssm_k, B_ssm_D):
    def _A_ref(x, wqkv, wo):
        B, S, D = x.shape
        H = 32; Dh = D // H
        W, dil = 8, 2
        qkv = (lambda x, *d: x.permute(*d))((x @ wqkv).reshape(B, S, 3, H, Dh), 2, 0, 3, 1, 4)
        q, k, v = qkv[0], qkv[1], qkv[2]
        scale = Dh ** -0.5
        pos = torch.arange(S)
        off = torch.arange(W) * dil
        idx = pos[:, None] - off[None, :]
        mask = idx >= 0
        idx_c = torch.where(mask, idx, 0).reshape(-1)
        kg = k[:, :, idx_c, :].reshape(B, H, S, W, Dh)
        vg = v[:, :, idx_c, :].reshape(B, H, S, W, Dh)
        logits = torch.einsum("bhsd,bhswd->bhsw", q, kg) * scale
        logits = torch.where(mask[None, None, :, :], logits, -1e9)
        attn = _softmax(logits, axis=-1)
        o = torch.einsum("bhsw,bhswd->bhsd", attn, vg)
        return (lambda x, *d: x.permute(*d))(o, 0, 2, 1, 3).reshape(B, S, D) @ wo

    def _B_ref(q, k, v, shift_k, ssm_k, ssm_D):
        B, S, D = q.shape
        ks = shift_k.shape[-1]
        k_pad = _pad(k, ((0, 0), (ks - 1, 0), (0, 0)))
        k_shift = torch.zeros_like(k)
        for t in range(ks):
            k_shift = k_shift + k_pad[:, t:t + S, :] * shift_k[:, ks - 1 - t][None, None, :]
        kv = k_shift * v
        L = 2 * S
        kv_pad = _pad(kv, ((0, 0), (0, S), (0, 0)))
        kf_pad = _pad(ssm_k, ((0, 0), (0, S)))
        kv_f = (lambda x, axes=(-2,-1): torch.fft.rfft2(x.float(), dim=axes))(kv_pad[:, None, :, :], axes=(1, 2))
        kf_f = (lambda x, axes=(-2,-1): torch.fft.rfft2(x.float(), dim=axes))(kf_pad[:, None, :], axes=(1, 2))
        kf_f = (lambda x, *d: x.permute(*d))(kf_f, 1, 2, 0)
        y_f = kv_f * kf_f[None, :, :, :]
        y = (lambda x, s, axes=(-2,-1): torch.fft.irfft2(x, s=s, dim=axes).to(torch.bfloat16))(y_f, (1, L), axes=(1, 2))[:, 0, :S, :]
        y = y + kv * ssm_D[None, None, :]
        return q * y

    a = _A_ref(A_x, A_wqkv, A_wo)
    b = _B_ref(B_q, B_k, B_v, B_shift_k, B_ssm_k, B_ssm_D)
    return torch.concatenate([a, b], axis=-1)
```

```cpp
#include <hip/hip_runtime.h>
#include <hip/hip_bf16.h>
typedef unsigned short u16;
__device__ __forceinline__ float bf2f(u16 x){ return __uint_as_float(((unsigned)x)<<16); }
__device__ __forceinline__ u16 f2bf(float f){ __hip_bfloat16 b = __float2bfloat16(f); return *(u16*)&b; }
__device__ __forceinline__ float rbf(float f){ return bf2f(f2bf(f)); }
constexpr int S=2048, D=576, H=32, DH=18;

__global__ void k_gemm(const u16* A, const u16* B, u16* C, int M, int N, int K, int ldc){
  int n = blockIdx.x*64 + (threadIdx.x&63); int m = blockIdx.y*4 + (threadIdx.x>>6);
  if(n>=N||m>=M) return;
  float acc=0; for(int k=0;k<K;k++) acc += bf2f(A[m*K+k])*bf2f(B[k*N+n]);
  C[m*ldc+n]=f2bf(acc);
}
__global__ void k_attn(const u16* qkv, u16* o){
  int idx = blockIdx.x*blockDim.x+threadIdx.x; if(idx>=S*H) return;
  int s = idx/H, h = idx%H;
  float scale = 0.23570226039551584f;
  float lg[8]; float mx=-1e30f;
  for(int w=0;w<8;w++){ int p=s-2*w; float a;
    if(p>=0){ a=0; for(int d=0;d<DH;d++) a+= bf2f(qkv[s*1728+h*DH+d])*bf2f(qkv[p*1728+576+h*DH+d]); a = rbf(rbf(a)*scale);} else a = rbf(-1e9f);
    lg[w]=a; mx=fmaxf(mx,a);}
  float sum=0; for(int w=0;w<8;w++){ lg[w]=__expf(lg[w]-mx); sum+=lg[w]; }
  for(int w=0;w<8;w++) lg[w]=rbf(lg[w]/sum);
  for(int d=0;d<DH;d++){ float a=0; for(int w=0;w<8;w++){ int p=s-2*w; if(p<0)p=0; a+= lg[w]*bf2f(qkv[p*1728+1152+h*DH+d]); } o[s*D+h*DH+d]=f2bf(a);}
}
__global__ void k_kv(const u16* k, const u16* v, const u16* sk, u16* kv){
  int idx = blockIdx.x*blockDim.x+threadIdx.x; if(idx>=S*D) return;
  int s=idx/D, d=idx%D; float acc=0;
  for(int t=0;t<4;t++){ int p=s-3+t; float kk = p>=0? bf2f(k[p*D+d]):0.f; acc = rbf(acc + rbf(kk*bf2f(sk[d*4+3-t]))); }
  kv[idx]=f2bf(acc*bf2f(v[idx]));
}
__global__ void k_conv(const u16* kv, const u16* hk, const u16* Dv, const u16* q, u16* out){
  __shared__ float x[S], h[S];
  int d=blockIdx.x;
  for(int i=threadIdx.x;i<S;i+=blockDim.x){ x[i]=bf2f(kv[i*D+d]); h[i]=bf2f(hk[d*S+i]); }
  __syncthreads();
  for(int t=threadIdx.x;t<S;t+=blockDim.x){ float a=0; for(int s=0;s<=t;s++) a+=x[s]*h[t-s];
    float y=rbf(a); y=rbf(y+rbf(x[t]*bf2f(Dv[d]))); out[t*1152+576+d]=f2bf(bf2f(q[t*D+d])*y);}
}
extern "C" void kernel_launch(void* const* d_in, const int* in_sizes, int n_in, void* d_out, int out_size, void* d_ws, size_t ws_size, hipStream_t stream){
  const u16* x=(const u16*)d_in[0]; const u16* wqkv=(const u16*)d_in[1]; const u16* wo=(const u16*)d_in[2];
  const u16* q=(const u16*)d_in[3]; const u16* k=(const u16*)d_in[4]; const u16* v=(const u16*)d_in[5];
  const u16* sk=(const u16*)d_in[6]; const u16* hk=(const u16*)d_in[7]; const u16* Dv=(const u16*)d_in[8];
  u16* out=(u16*)d_out; u16* ws=(u16*)d_ws;
  u16* qkv=ws; u16* o=ws+S*1728; u16* kv=o+S*D;
  k_gemm<<<dim3(27,512),256,0,stream>>>(x,wqkv,qkv,S,1728,576,1728);
  k_attn<<<S*H/256,256,0,stream>>>(qkv,o);
  k_gemm<<<dim3(9,512),256,0,stream>>>(o,wo,out,S,576,576,1152);
  k_kv<<<S*D/256,256,0,stream>>>(k,v,sk,kv);
  k_conv<<<D,256,0,stream>>>(kv,hk,Dv,q,out);
}
```

```cpp
#include <hip/hip_runtime.h>
#include <hip/hip_bf16.h>

typedef unsigned short u16;
typedef unsigned long long u64;
typedef unsigned v4u __attribute__((ext_vector_type(4)));
typedef unsigned v2u __attribute__((ext_vector_type(2)));
typedef short s4 __attribute__((ext_vector_type(4)));
typedef __bf16 b2 __attribute__((ext_vector_type(2)));
typedef __bf16 b8 __attribute__((ext_vector_type(8)));
typedef float f4 __attribute__((ext_vector_type(4)));
typedef float f2 __attribute__((ext_vector_type(2)));
#define LAS __attribute__((address_space(3)))
#define GAS __attribute__((address_space(1)))
#define DEV __device__ __forceinline__
#define SB __builtin_amdgcn_sched_barrier(0)

#define STAMP(i)
#define STAMPDECL
#define STAMPARG
#define STAMPPASS
#define STAMPOUT

struct Params {
  const char *x, *wqkv, *wo, *q, *k, *v, *sk, *hk, *dv;
  char *out, *ws;
};

DEV float lo2f(unsigned d){ return __uint_as_float(d<<16); }
DEV float hi2f(unsigned d){ return __uint_as_float(d&0xffff0000u); }
DEV unsigned pk2(float lo, float hi){ f2 p={lo,hi}; return __builtin_bit_cast(unsigned,__builtin_convertvector(p,b2)); }
DEV v4u ld128(unsigned a){ return *(LAS v4u*)a; }
DEV v2u ld64(unsigned a){ return *(LAS v2u*)a; }
DEV unsigned ld32(unsigned a){ return *(LAS unsigned*)a; }
DEV void st128(unsigned a, v4u v){ *(LAS v4u*)a=v; }
DEV void st64(unsigned a, v2u v){ *(LAS v2u*)a=v; }
DEV void st32(unsigned a, unsigned v){ *(LAS unsigned*)a=v; }
DEV v2u ldtr(unsigned a){ return __builtin_bit_cast(v2u,__builtin_amdgcn_ds_read_tr16_b64_v4i16((LAS s4*)a)); }
DEV f4 mfma(v4u a, v4u b, f4 c){ return __builtin_amdgcn_mfma_f32_16x16x32_bf16(__builtin_bit_cast(b8,a),__builtin_bit_cast(b8,b),c,0,0,0); }
DEV v4u cat(v2u a, v2u b){ return (v4u){a.x,a.y,b.x,b.y}; }
DEV v4u gld128(const char* p){ return *(const GAS v4u*)p; }
DEV void ldsbar(){ asm volatile("s_waitcnt lgkmcnt(0)":::"memory"); __builtin_amdgcn_s_barrier(); asm volatile("":::"memory"); }

constexpr int B_HA=448, B_HB=4576+448, B_X0=9120, B_X1=13344, B_PITCH=17520, B_SCR=8*B_PITCH;

DEV v2u ld64v(unsigned a){ return *(volatile LAS v2u*)a; }
template<int BLO,int BHI> struct BFr { v4u hf[8-BLO], xf[BHI-BLO+1]; };
template<int BLO,int BHI> DEV void b_load(BFr<BLO,BHI>& f, unsigned hb, unsigned xb){
  #pragma unroll
  for(int b=BLO;b<=BHI;b++) f.xf[b-BLO]=cat(ld64v(xb+512*(7-b)), ld64v(xb+512*(7-b)+8));
  #pragma unroll
  for(int a=0;a<8-BLO;a++) f.hf[a]=ld128(hb+512*a);
}
template<int BLO,int BHI> DEV void b_mma(f4 (&acc)[8], const BFr<BLO,BHI>& f){
  #pragma unroll
  for(int b=BLO;b<=BHI;b++){
    #pragma unroll
    for(int a=0;a<8-BLO;a++) if(a+b<8) acc[a+b]=mfma(f.hf[a],f.xf[b-BLO],acc[a+b]);
  }
}
template<int BLO,int BHI,bool LAST=false> DEV void b_pair(f4 (&acc)[8], BFr<BLO,BHI>& f0, unsigned hb, unsigned xb){
  BFr<BLO,BHI> f1;
  b_load<BLO,BHI>(f1,hb-64,xb-64);
  SB; b_mma<BLO,BHI>(acc,f0); SB;
  if(!LAST) b_load<BLO,BHI>(f0,hb-128,xb-128);
  SB; b_mma<BLO,BHI>(acc,f1); SB;
}

DEV void b_prep(int blk, int wave, int lane, v4u kr, v4u vr, v4u halo, float s0,float s1,float s2,float s3){
  const unsigned scr=B_SCR+wave*2112;
  st128(scr+64+lane*16,kr); st128(scr+1088+lane*16,vr);
  if((lane>>2)==blk) st128(scr+(lane&3)*16,halo);
  const int p=lane&3;
  const unsigned ta=scr+((8*(2*(lane>>4)+(p>>1))+((lane&15)>>2))*16)+(p&1)*8;
  v2u k0=ldtr(ta),k1=ldtr(ta+64),k2=ldtr(ta+128),v0=ldtr(ta+1088),v1=ldtr(ta+1088+64);
  float kk[11]={hi2f(k0.x),lo2f(k0.y),hi2f(k0.y),lo2f(k1.x),hi2f(k1.x),lo2f(k1.y),hi2f(k1.y),lo2f(k2.x),hi2f(k2.x),lo2f(k2.y),hi2f(k2.y)};
  float vv[8]={lo2f(v0.x),hi2f(v0.x),lo2f(v0.y),hi2f(v0.y),lo2f(v1.x),hi2f(v1.x),lo2f(v1.y),hi2f(v1.y)};
  float r[8];
  #pragma unroll
  for(int j=0;j<8;j++) r[j]=(kk[j]*s3+kk[j+1]*s2+kk[j+2]*s1+kk[j+3]*s0)*vv[j];
  v4u o={pk2(r[7],r[6]),pk2(r[5],r[4]),pk2(r[3],r[2]),pk2(r[1],r[0])};
  const int t0=512*blk+64*wave+8*(lane>>3);
  const unsigned a=(lane&7)*B_PITCH+(2040-t0)*2;
  st128(a+B_X0,o);
  st32(a+B_X1+4,o.x); st32(a+B_X1+8,o.y); st32(a+B_X1+12,o.z); st32(a+B_X1+16,o.w);
}

template<int JLO,int JHI> DEV void b_zout(f4 (&acc)[8], int wave, int lane, float dd){
  const int tl=64*(lane>>4)+2*(lane&15);
  const unsigned xa=wave*B_PITCH+B_X0+(2046-tl)*2-64-512*7, za=wave*B_PITCH+B_HA+tl*2;
  #pragma unroll
  for(int J=JLO;J<=JHI;J++){
    unsigned x0=ld32(xa+512*(7-J)+64), x1=ld32(xa+512*(7-J));
    st32(za+512*(7-J),    pk2(fmaf(hi2f(x0),dd,acc[J][0]),fmaf(lo2f(x0),dd,acc[J][1])));
    st32(za+512*(7-J)+64, pk2(fmaf(hi2f(x1),dd,acc[J][2]),fmaf(lo2f(x1),dd,acc[J][3])));
  }
}

DEV void b_gate(int pss, int wave, int lane, v4u q, char* outp){
  const int t=512*pss+64*wave+16*(lane>>4)+4*(lane&3);
  const unsigned a=((lane&15)>>2)*B_PITCH+B_HA+(256*(7-(t>>8))+(t&255))*2;
  v2u z0=ldtr(a), z1=ldtr(a+4*B_PITCH);
  v4u o={pk2(lo2f(q.x)*lo2f(z0.x),hi2f(q.x)*hi2f(z0.x)), pk2(lo2f(q.y)*lo2f(z0.y),hi2f(q.y)*hi2f(z0.y)),
         pk2(lo2f(q.z)*lo2f(z1.x),hi2f(q.z)*hi2f(z1.x)), pk2(lo2f(q.w)*lo2f(z1.y),hi2f(q.w)*hi2f(z1.y))};
  *(GAS v4u*)(outp+(size_t)pss*512*2304)=o;
}

DEV void role_b(const Params& p, int cg STAMPARG){
  const int tid=threadIdx.x, lane=tid&63, wave=__builtin_amdgcn_readfirstlane(tid>>6);
  const size_t ro=(size_t)tid*1152+cg*16;
  v4u kr[4],vr[4],qr[4],hr[4],halo={0,0,0,0};
  kr[0]=gld128(p.k+ro); vr[0]=gld128(p.v+ro);
  { const int ht=512*(lane>>2)+64*wave-4+(lane&3);
    if(lane<16 && ht>=0) halo=gld128(p.k+(size_t)ht*1152+cg*16); }
  const v2u skr=*(const GAS v2u*)(p.sk+(cg*8+(lane&7))*8);
  #pragma unroll
  for(int i=0;i<4;i++) hr[i]=gld128(p.hk+(size_t)(cg*8+wave)*4096+i*1024+lane*16);
  SB; __builtin_amdgcn_s_barrier(); SB;
  kr[1]=gld128(p.k+ro+(size_t)512*1152); vr[1]=gld128(p.v+ro+(size_t)512*1152);
  SB;
  const float dd=lo2f(*(const GAS u16*)(p.dv+(cg*8+wave)*2));
  const unsigned cb=wave*B_PITCH;
  if(lane<56) st128(cb+(lane<28? lane*16 : 4576+(lane-28)*16),(v4u){0,0,0,0});
  if(lane<31) st32(cb+(lane<16? B_X0+4096+lane*4 : B_X1+4100+(lane-16)*4),0u);
  const float s0=lo2f(skr.x),s1=hi2f(skr.x),s2=lo2f(skr.y),s3=hi2f(skr.y);
  STAMP(1);
  b_prep(0,wave,lane,kr[0],vr[0],halo,s0,s1,s2,s3);
  #pragma unroll
  for(int i=0;i<4;i++) st128(cb+B_HA+i*1024+lane*16,hr[i]);
  #pragma unroll
  for(int i=0;i<4;i++){
    const unsigned pv=ld32(cb+B_HA+i*1024+lane*16-4);
    v4u s={__builtin_amdgcn_alignbit(hr[i].x,pv,16),__builtin_amdgcn_alignbit(hr[i].y,hr[i].x,16),
           __builtin_amdgcn_alignbit(hr[i].z,hr[i].y,16),__builtin_amdgcn_alignbit(hr[i].w,hr[i].z,16)};
    st128(cb+B_HB+i*1024+lane*16,s);
  }
  const int i16=lane&15, g=lane>>4;
  const unsigned hb0=cb+((i16&1)?B_HA:B_HB)+(32*(i16>>1)+8*g)*2;
  const unsigned xb0=cb+((i16&1)? B_X0+(2046-2*i16+8*g)*2 : B_X1+(2048-2*i16+8*g)*2)-512*7;
  f4 acc[8];
  #pragma unroll
  for(int j=0;j<8;j++) acc[j]=(f4){0,0,0,0};
  unsigned hb=hb0, xb=xb0;
  asm volatile("":"+v"(hb),"+v"(xb));
  SB;
  kr[2]=gld128(p.k+ro+(size_t)2*512*1152); vr[2]=gld128(p.v+ro+(size_t)2*512*1152);
  SB;
  ldsbar();
  STAMP(2);
  {
    BFr<0,1> f; b_load<0,1>(f,hb,xb);
    b_pair<0,1>(acc,f,hb,xb);
    STAMP(6);
    kr[3]=gld128(p.k+ro+(size_t)3*512*1152); vr[3]=gld128(p.v+ro+(size_t)3*512*1152);
    SB;
    STAMP(7);
    b_prep(1,wave,lane,kr[1],vr[1],halo,s0,s1,s2,s3);
    STAMP(8);
    b_pair<0,1>(acc,f,hb-128,xb-128);
    STAMP(9);
    qr[0]=gld128(p.q+ro); qr[1]=gld128(p.q+ro+(size_t)512*1152);
    SB;
    STAMP(10);
    b_prep(2,wave,lane,kr[2],vr[2],halo,s0,s1,s2,s3);
    STAMP(11);
    b_pair<0,1>(acc,f,hb-256,xb-256);
    qr[2]=gld128(p.q+ro+(size_t)2*512*1152); qr[3]=gld128(p.q+ro+(size_t)3*512*1152);
    SB;
    b_pair<0,1,true>(acc,f,hb-384,xb-384);
    STAMP(12);
    b_prep(3,wave,lane,kr[3],vr[3],halo,s0,s1,s2,s3);
    STAMP(13);
  }
  b_zout<0,1>(acc,wave,lane,dd);
  ldsbar();
  STAMP(3);
  char* outp=p.out+(size_t)tid*2304+1152+cg*16;
  {
    BFr<2,7> f; b_load<2,7>(f,hb,xb);
    b_gate(0,wave,lane,qr[0],outp);
    #pragma unroll 1
    for(int r=0;r<6;r+=2){ b_pair<2,7>(acc,f,hb,xb); hb-=128; xb-=128; }
    b_pair<2,7,true>(acc,f,hb,xb);
  }
  STAMP(4);
  b_zout<2,7>(acc,wave,lane,dd);
  ldsbar();
  b_gate(1,wave,lane,qr[1],outp); b_gate(2,wave,lane,qr[2],outp); b_gate(3,wave,lane,qr[3],outp);
  STAMP(5);
}

constexpr int A_TM=90, A_SLOT=41984, A_WOFF=14336, A_IMG=83968, A_IROW=456, A_OST=135040;
constexpr int WS_O=65536, WS_OT=6*72*256;
typedef __amdgpu_buffer_rsrc_t rsrc_t;
DEV void dma16(const v4u& srd, unsigned voff, unsigned soff, unsigned lds){
  asm volatile("s_mov_b32 m0, %3\n\ts_nop 0\n\tbuffer_load_dwordx4 %0, %1, %2 offen lds"::"v"(voff),"s"(srd),"s"(soff),"s"(lds):"memory");
}
DEV v4u mksrd(const char* p, unsigned bytes){
  const u64 a=(u64)p; v4u s={(unsigned)a,(unsigned)(a>>32)&0xffffu,bytes,0x00020000u};
  s.x=__builtin_amdgcn_readfirstlane(s.x); s.y=__builtin_amdgcn_readfirstlane(s.y); return s;
}
DEV float dot2(unsigned a, unsigned b, float c){ return __builtin_amdgcn_fdot2_f32_bf16(__builtin_bit_cast(b2,a),__builtin_bit_cast(b2,b),c,false); }
#define WAITV(n) asm volatile("s_waitcnt vmcnt(" #n ")":::"memory")
#define BAR do{ asm volatile("":::"memory"); __builtin_amdgcn_s_barrier(); asm volatile("":::"memory"); }while(0)

DEV void role_a(const Params& p, int rt, int grp STAMPARG){
  const int tid=threadIdx.x, lane=tid&63, wave=__builtin_amdgcn_readfirstlane(tid>>6);
  const int row0=rt*A_TM, nrow=min(A_TM,2048-row0);
  const int i16=lane&15, g=lane>>4;
  GAS u64* flags=(GAS u64*)(p.ws+rt*256);
  u64 ep=0;
  if(wave>=4){
    const int lw=wave-4;
    const v4u sx=mksrd(p.x,2048*1152), sw=mksrd(p.wqkv,576*3456), so=mksrd(p.wo,576*1152);
    unsigned vo[11];
    #pragma unroll
    for(int i=0;i<11;i++){
      const int pc=lw+4*i;
      if(pc<14){ const int ir=8*pc+(lane>>3); int tok=row0-16+ir; tok=max(tok,max(row0-14,0)); tok=min(tok,row0+nrow-1);
        vo[i]=tok*1152+(((lane&7)^((ir>>1)&7))<<4); }
      else { const int ci=(pc-14)*64+lane, rho=ci/27, c=ci-rho*27, seg=c/9, cc=c-seg*9;
        const int k=(rho&~7)+4*(rho&1)+((rho&7)>>1);
        vo[i]=k*3456+seg*1152+grp*144+cc*16; }
    }
    auto issue=[&](int s, int slot){
      #pragma unroll
      for(int i=0;i<11;i++){
        const int pc=lw+4*i;
        if(i<3 || (i==3 && pc<14)) dma16(sx,vo[i],s*128,slot*A_SLOT+pc*1024);
        else if(i<10 || pc<41) dma16(sw,vo[i],s*64*3456,slot*A_SLOT+pc*1024);
      }
    };
    issue(0,0); issue(1,1); issue(2,2);
    auto issue_wo=[&](int i0,int i1){
      for(int i=i0;i<i1;i++){ const int pc=lw+4*i; if(pc<81){ const int ci=pc*64+lane, rho=ci/9, cc=ci-rho*9; const int k=(rho&~7)+4*(rho&1)+((rho&7)>>1);
        dma16(so,k*1152+grp*144+cc*16,0,pc*1024); } }
    };
    int slot=0;
    #pragma unroll 1
    for(int s=0;s<9;s++){
      if(s==0){ if(lw==0) WAITV(22); else WAITV(20); }
      else if(s<8){ if(lw==0) WAITV(11); else WAITV(10); }
      else WAITV(10);
      BAR;
      if(s>=1 && s<7){ issue(s+2,slot); slot=(slot==2)?0:slot+1; }
      else if(s==7) issue_wo(0,10);
      else if(s==8) issue_wo(10,21);
    }
    STAMP(2);
    BAR;
  } else {
    if(tid==0) ep=__hip_atomic_load(flags+16,__ATOMIC_RELAXED,__HIP_MEMORY_SCOPE_AGENT);
    f4 acc[3][7], ace[3];
    #pragma unroll
    for(int c=0;c<3;c++){ ace[c]=(f4){0,0,0,0};
      #pragma unroll
      for(int m=0;m<7;m++) acc[c][m]=(f4){0,0,0,0}; }
    const int e0=(wave&1)?4:1, o0=(wave&1)?1:4;
    const unsigned xl=i16*128+((g^(i16>>1))<<4);
    const unsigned wl=A_WOFF+(8*g+2*(i16>>2))*432+(lane&3)*8;
    unsigned sb=0;
    #pragma unroll 1
    for(int s=0;s<9;s++){
      ldsbar();
      if(s==0) STAMP(1);
      #pragma unroll
      for(int kk=0;kk<2;kk++){
        const unsigned xa=sb+(xl^(kk*64));
        const unsigned wa=sb+wl+kk*13824+(2+3*wave)*32, we=sb+wl+kk*13824+(wave>>1)*32;
        v4u xf[7], wf[3], wx;
        #pragma unroll
        for(int m=0;m<3;m++) xf[m]=ld128(xa+e0*2048+m*2048);
        wx=cat(ldtr(we),ldtr(we+432));
        #pragma unroll
        for(int c=0;c<3;c++) wf[c]=cat(ldtr(wa+c*32),ldtr(wa+c*32+432));
        xf[3]=ld128(xa);
        #pragma unroll
        for(int m=0;m<3;m++) xf[4+m]=ld128(xa+o0*2048+m*2048);
        #pragma unroll
        for(int m=0;m<3;m++) ace[m]=mfma(wx,xf[m],ace[m]);
        #pragma unroll
        for(int m=0;m<7;m++){
          #pragma unroll
          for(int c=0;c<3;c++) acc[c][m]=mfma(wf[c],xf[m],acc[c][m]);
        }
      }
      sb=(sb==2*A_SLOT)?0:sb+A_SLOT;
    }
    STAMP(2);
    ldsbar();
    const unsigned ia=A_IMG+i16*A_IROW+g*8;
    #pragma unroll
    for(int m=0;m<7;m++){
      const int rtile=(m<3)?e0+m:(m==3?0:o0+m-4);
      #pragma unroll
      for(int c=0;c<3;c++) st64(ia+rtile*16*A_IROW+(2+3*wave+c)*32,(v2u){pk2(acc[c][m][0],acc[c][m][1]),pk2(acc[c][m][2],acc[c][m][3])});
      if(m<3) st64(ia+rtile*16*A_IROW+(wave>>1)*32,(v2u){pk2(ace[m][0],ace[m][1]),pk2(ace[m][2],ace[m][3])});
    }
  }
  ldsbar();
  STAMP(3);
  if(tid<4*A_TM){
    const int h=tid/A_TM, rr=tid-h*A_TM, odd=h&1;
    const unsigned ra=A_IMG+(16+rr)*A_IROW+36*h-4*odd;
    unsigned q[10];
    #pragma unroll
    for(int j=0;j<5;j++){ v2u t=ld64v(ra+8*j); q[2*j]=t.x; q[2*j+1]=t.y; }
    if(odd) q[0]=0; else q[9]=0;
    const unsigned rb=ra-14*A_IROW;
    float lg[8];
    #pragma unroll
    for(int w=0;w<8;w++){
      float a=0;
      #pragma unroll
      for(int j=0;j<5;j++){ v2u t=ld64v(rb+(14-2*w)*A_IROW+144+8*j); a=dot2(q[2*j],t.x,a); a=dot2(q[2*j+1],t.y,a); }
      lg[w]=(row0+rr-2*w>=0)? a*(0.23570226039551584f*1.4426950408889634f) : -1e9f;
    }
    float mx=lg[0];
    #pragma unroll
    for(int w=1;w<8;w++) mx=fmaxf(mx,lg[w]);
    float sum=0;
    #pragma unroll
    for(int w=0;w<8;w++){ lg[w]=__builtin_amdgcn_exp2f(lg[w]-mx); sum+=lg[w]; }
    const float inv=__builtin_amdgcn_rcpf(sum);
    float o[20];
    #pragma unroll
    for(int j=0;j<20;j++) o[j]=0;
    #pragma unroll
    for(int w=0;w<8;w++){
      const unsigned al=pk2(lg[w]*inv,0.f), ah=al<<16;
      #pragma unroll
      for(int j=0;j<5;j++){ v2u t=ld64v(rb+(14-2*w)*A_IROW+288+8*j);
        o[4*j]=dot2(t.x,al,o[4*j]); o[4*j+1]=dot2(t.x,ah,o[4*j+1]); o[4*j+2]=dot2(t.y,al,o[4*j+2]); o[4*j+3]=dot2(t.y,ah,o[4*j+3]); }
    }
    unsigned od[10];
    #pragma unroll
    for(int j=0;j<10;j++) od[j]=pk2(o[2*j],o[2*j+1]);
    const int col0=18*h-2*odd;
    const unsigned oa=A_OST+(rr>>4)*2304+(rr&15)*16;
    unsigned pa[5];
    #pragma unroll
    for(int j=0;j<5;j++){ const int col=col0+4*j; pa[j]=oa+(col>>3)*256+(col&4)*2; }
    st32(pa[0]+4,od[1]); st64(pa[1],(v2u){od[2],od[3]}); st64(pa[2],(v2u){od[4],od[5]}); st64(pa[3],(v2u){od[6],od[7]}); st32(pa[4],od[8]);
    st32(odd? pa[4]+4:pa[0], odd? od[9]:od[0]);
  }
  ldsbar();
  STAMP(4);
  const rsrc_t wsr=__builtin_amdgcn_make_buffer_rsrc(p.ws+WS_O,0,23*WS_OT,0x00020000);
  {
    const int c0=tid, m0=c0/144;
    __builtin_amdgcn_raw_buffer_store_b128(ld128(A_OST+c0*16),wsr,rt*WS_OT+(m0*72+9*grp)*256+(c0-m0*144)*16,0,16);
    const int c1=tid+512, m1=c1/144;
    if(c1<864) __builtin_amdgcn_raw_buffer_store_b128(ld128(A_OST+c1*16),wsr,rt*WS_OT+(m1*72+9*grp)*256+(c1-m1*144)*16,0,16);
  }
  WAITV(0);
  BAR;
  STAMP(5);
  if(wave==0){
    const u64 tag0=ep*6364136223846793005ull+1442695040888963407ull;
    const u64 tag=((u64)(unsigned)__builtin_amdgcn_readfirstlane((unsigned)(tag0>>32))<<32)|(unsigned)__builtin_amdgcn_readfirstlane((unsigned)tag0);
    if(lane==0) __hip_atomic_store(flags+grp,tag,__ATOMIC_RELAXED,__HIP_MEMORY_SCOPE_AGENT);
    for(int spin=0;spin<(1<<22);spin++){
      const u64 f=__hip_atomic_load(flags+(lane&7),__ATOMIC_RELAXED,__HIP_MEMORY_SCOPE_AGENT);
      if(__all(f==tag)) break;
      __builtin_amdgcn_s_sleep(1);
    }
    if(lane==0 && grp==0) __hip_atomic_store(flags+16,tag,__ATOMIC_RELAXED,__HIP_MEMORY_SCOPE_AGENT);
  }
  BAR;
  STAMP(6);
  {
    const int m=(wave<4)?wave:4+((wave-4)>>1), ks0=(wave<4)?0:9*(wave&1);
    const unsigned ob=rt*WS_OT+(m*72+4*ks0)*256+lane*16;
    v4u of[18];
    #pragma unroll
    for(int k=0;k<9;k++) of[k]=__builtin_amdgcn_raw_buffer_load_b128(wsr,ob+k*1024,0,16);
    if(wave<4){
      #pragma unroll
      for(int k=9;k<18;k++) of[k]=__builtin_amdgcn_raw_buffer_load_b128(wsr,ob+k*1024,0,16);
    }
    f4 acc[5];
    #pragma unroll
    for(int c=0;c<5;c++) acc[c]=(f4){0,0,0,0};
    const unsigned wa=(8*g+2*(i16>>2))*144+(lane&3)*8+ks0*4608;
    #pragma unroll
    for(int k=0;k<9;k++){
      #pragma unroll
      for(int c=0;c<5;c++) acc[c]=mfma(cat(ldtr(wa+k*4608+c*32),ldtr(wa+k*4608+c*32+144)),of[k],acc[c]);
    }
    const unsigned red=A_IMG+((wave>>1)&1)*5120+lane*16;
    if(wave>=4 && (wave&1)){
      #pragma unroll
      for(int c=0;c<5;c++) st128(red+c*1024,__builtin_bit_cast(v4u,acc[c]));
    }
    ldsbar();
    STAMP(7);
    if(wave<4){
      #pragma unroll
      for(int k=9;k<18;k++){
        #pragma unroll
        for(int c=0;c<5;c++) acc[c]=mfma(cat(ldtr(wa+k*4608+c*32),ldtr(wa+k*4608+c*32+144)),of[k],acc[c]);
      }
    } else if(!(wave&1)){
      #pragma unroll
      for(int c=0;c<5;c++) acc[c]+=__builtin_bit_cast(f4,ld128(red+c*1024));
    }
    const int rr=m*16+i16;
    if((wave<4 || !(wave&1)) && rr<nrow){
      char* op=p.out+(size_t)(row0+rr)*2304+grp*144+g*8;
      #pragma unroll
      for(int c=0;c<5;c++) if(c<4 || g<2) *(GAS v2u*)(op+c*32)=(v2u){pk2(acc[c][0],acc[c][1]),pk2(acc[c][2],acc[c][3])};
    }
  }
  STAMP(8);
}

__global__ __launch_bounds__(512,2) void fused(Params p){
  extern __shared__ __attribute__((aligned(16))) char smem[];
  STAMPDECL;
  STAMP(0);
  const int b=blockIdx.x, xcd=b&7, j=b>>3, pr=xcd>>1, half=xcd&1;
  const int nA=(pr<3)?24:20;
  if(j>=nA){
    const int jb=j-nA;
    role_b(p, jb<8? xcd*8+jb : 64+half*4+(jb-8) STAMPPASS);
  } else role_a(p, 6*pr+(j>>2), half*4+(j&3) STAMPPASS);
}

constexpr int LDS_BYTES=160*1024;
extern "C" void kernel_launch(void* const* d_in, const int* in_sizes, int n_in, void* d_out, int out_size, void* d_ws, size_t ws_size, hipStream_t stream){
  Params p;
  p.x=(const char*)d_in[0]; p.wqkv=(const char*)d_in[1]; p.wo=(const char*)d_in[2];
  p.q=(const char*)d_in[3]; p.k=(const char*)d_in[4]; p.v=(const char*)d_in[5];
  p.sk=(const char*)d_in[6]; p.hk=(const char*)d_in[7]; p.dv=(const char*)d_in[8];
  p.out=(char*)d_out; p.ws=(char*)d_ws;
  (void)hipFuncSetAttribute((const void*)fused,hipFuncAttributeMaxDynamicSharedMemorySize,LDS_BYTES);
  fused<<<256,512,LDS_BYTES,stream>>>(p);
}
```

```cpp
#define PROBE 0
#define PROBEBLK 8
#include <hip/hip_runtime.h>
#include <hip/hip_bf16.h>

typedef unsigned short u16;
typedef unsigned long long u64;
typedef unsigned v4u __attribute__((ext_vector_type(4)));
typedef unsigned v2u __attribute__((ext_vector_type(2)));
typedef short s4 __attribute__((ext_vector_type(4)));
typedef __bf16 b2 __attribute__((ext_vector_type(2)));
typedef __bf16 b8 __attribute__((ext_vector_type(8)));
typedef float f4 __attribute__((ext_vector_type(4)));
typedef float f2 __attribute__((ext_vector_type(2)));
#define LAS __attribute__((address_space(3)))
#define GAS __attribute__((address_space(1)))
#define DEV __device__ __forceinline__
#define SB __builtin_amdgcn_sched_barrier(0)

#define STAMP(i) do{ if((i)==PROBE || (i)==0) { SB; asm volatile("s_memrealtime %0\n\ts_waitcnt lgkmcnt(0)":"=s"(g_stamp[(i)?1:0])::"memory"); SB; } }while(0)
#define STAMPDECL u64 g_stamp[2]={0,0}
#define STAMPARG , u64 (&g_stamp)[2]
#define STAMPPASS , g_stamp
#define STAMPOUT

struct Params {
  const char *x, *wqkv, *wo, *q, *k, *v, *sk, *hk, *dv;
  char *out, *ws;
};

DEV float lo2f(unsigned d){ return __uint_as_float(d<<16); }
DEV float hi2f(unsigned d){ return __uint_as_float(d&0xffff0000u); }
DEV unsigned pk2(float lo, float hi){ f2 p={lo,hi}; return __builtin_bit_cast(unsigned,__builtin_convertvector(p,b2)); }
DEV v4u ld128(unsigned a){ return *(LAS v4u*)a; }
DEV v2u ld64(unsigned a){ return *(LAS v2u*)a; }
DEV unsigned ld32(unsigned a){ return *(LAS unsigned*)a; }
DEV void st128(unsigned a, v4u v){ *(LAS v4u*)a=v; }
DEV void st64(unsigned a, v2u v){ *(LAS v2u*)a=v; }
DEV void st32(unsigned a, unsigned v){ *(LAS unsigned*)a=v; }
DEV v2u ldtr(unsigned a){ return __builtin_bit_cast(v2u,__builtin_amdgcn_ds_read_tr16_b64_v4i16((LAS s4*)a)); }
DEV f4 mfma(v4u a, v4u b, f4 c){ return __builtin_amdgcn_mfma_f32_16x16x32_bf16(__builtin_bit_cast(b8,a),__builtin_bit_cast(b8,b),c,0,0,0); }
DEV v4u cat(v2u a, v2u b){ return (v4u){a.x,a.y,b.x,b.y}; }
DEV v4u gld128(const char* p){ return *(const GAS v4u*)p; }
DEV void ldsbar(){ asm volatile("s_waitcnt lgkmcnt(0)":::"memory"); __builtin_amdgcn_s_barrier(); asm volatile("":::"memory"); }

constexpr int B_HA=448, B_HB=4576+448, B_X0=9120, B_X1=13344, B_PITCH=17520, B_SCR=8*B_PITCH;

DEV v2u ld64v(unsigned a){ return *(volatile LAS v2u*)a; }
template<int BLO,int BHI> struct BFr { v4u hf[8-BLO], xf[BHI-BLO+1]; };
template<int BLO,int BHI> DEV void b_load(BFr<BLO,BHI>& f, unsigned hb, unsigned xb){
  #pragma unroll
  for(int b=BLO;b<=BHI;b++) f.xf[b-BLO]=cat(ld64v(xb+512*(7-b)), ld64v(xb+512*(7-b)+8));
  #pragma unroll
  for(int a=0;a<8-BLO;a++) f.hf[a]=ld128(hb+512*a);
}
template<int BLO,int BHI> DEV void b_mma(f4 (&acc)[8], const BFr<BLO,BHI>& f){
  #pragma unroll
  for(int b=BLO;b<=BHI;b++){
    #pragma unroll
    for(int a=0;a<8-BLO;a++) if(a+b<8) acc[a+b]=mfma(f.hf[a],f.xf[b-BLO],acc[a+b]);
  }
}
template<int BLO,int BHI,bool LAST=false> DEV void b_pair(f4 (&acc)[8], BFr<BLO,BHI>& f0, unsigned hb, unsigned xb){
  BFr<BLO,BHI> f1;
  b_load<BLO,BHI>(f1,hb-64,xb-64);
  SB; b_mma<BLO,BHI>(acc,f0); SB;
  if(!LAST) b_load<BLO,BHI>(f0,hb-128,xb-128);
  SB; b_mma<BLO,BHI>(acc,f1); SB;
}

DEV void b_prep(int blk, int wave, int lane, v4u kr, v4u vr, v4u halo, float s0,float s1,float s2,float s3){
  const unsigned scr=B_SCR+wave*2112;
  st128(scr+64+lane*16,kr); st128(scr+1088+lane*16,vr);
  if((lane>>2)==blk) st128(scr+(lane&3)*16,halo);
  const int p=lane&3;
  const unsigned ta=scr+((8*(2*(lane>>4)+(p>>1))+((lane&15)>>2))*16)+(p&1)*8;
  v2u k0=ldtr(ta),k1=ldtr(ta+64),k2=ldtr(ta+128),v0=ldtr(ta+1088),v1=ldtr(ta+1088+64);
  float kk[11]={hi2f(k0.x),lo2f(k0.y),hi2f(k0.y),lo2f(k1.x),hi2f(k1.x),lo2f(k1.y),hi2f(k1.y),lo2f(k2.x),hi2f(k2.x),lo2f(k2.y),hi2f(k2.y)};
  float vv[8]={lo2f(v0.x),hi2f(v0.x),lo2f(v0.y),hi2f(v0.y),lo2f(v1.x),hi2f(v1.x),lo2f(v1.y),hi2f(v1.y)};
  float r[8];
  #pragma unroll
  for(int j=0;j<8;j++) r[j]=(kk[j]*s3+kk[j+1]*s2+kk[j+2]*s1+kk[j+3]*s0)*vv[j];
  v4u o={pk2(r[7],r[6]),pk2(r[5],r[4]),pk2(r[3],r[2]),pk2(r[1],r[0])};
  const int t0=512*blk+64*wave+8*(lane>>3);
  const unsigned a=(lane&7)*B_PITCH+(2040-t0)*2;
  st128(a+B_X0,o);
  st32(a+B_X1+4,o.x); st32(a+B_X1+8,o.y); st32(a+B_X1+12,o.z); st32(a+B_X1+16,o.w);
}

template<int JLO,int JHI> DEV void b_zout(f4 (&acc)[8], int wave, int lane, float dd){
  const int tl=64*(lane>>4)+2*(lane&15);
  const unsigned xa=wave*B_PITCH+B_X0+(2046-tl)*2-64-512*7, za=wave*B_PITCH+B_HA+tl*2;
  #pragma unroll
  for(int J=JLO;J<=JHI;J++){
    unsigned x0=ld32(xa+512*(7-J)+64), x1=ld32(xa+512*(7-J));
    st32(za+512*(7-J),    pk2(fmaf(hi2f(x0),dd,acc[J][0]),fmaf(lo2f(x0),dd,acc[J][1])));
    st32(za+512*(7-J)+64, pk2(fmaf(hi2f(x1),dd,acc[J][2]),fmaf(lo2f(x1),dd,acc[J][3])));
  }
}

DEV void b_gate(int pss, int wave, int lane, v4u q, char* outp){
  const int t=512*pss+64*wave+16*(lane>>4)+4*(lane&3);
  const unsigned a=((lane&15)>>2)*B_PITCH+B_HA+(256*(7-(t>>8))+(t&255))*2;
  v2u z0=ldtr(a), z1=ldtr(a+4*B_PITCH);
  v4u o={pk2(lo2f(q.x)*lo2f(z0.x),hi2f(q.x)*hi2f(z0.x)), pk2(lo2f(q.y)*lo2f(z0.y),hi2f(q.y)*hi2f(z0.y)),
         pk2(lo2f(q.z)*lo2f(z1.x),hi2f(q.z)*hi2f(z1.x)), pk2(lo2f(q.w)*lo2f(z1.y),hi2f(q.w)*hi2f(z1.y))};
  *(GAS v4u*)(outp+(size_t)pss*512*2304)=o;
}

DEV void role_b(const Params& p, int cg STAMPARG){
  const int tid=threadIdx.x, lane=tid&63, wave=__builtin_amdgcn_readfirstlane(tid>>6);
  const size_t ro=(size_t)tid*1152+cg*16;
  v4u kr[4],vr[4],qr[4],hr[4],halo={0,0,0,0};
  kr[0]=gld128(p.k+ro); vr[0]=gld128(p.v+ro);
  { const int ht=512*(lane>>2)+64*wave-4+(lane&3);
    if(lane<16 && ht>=0) halo=gld128(p.k+(size_t)ht*1152+cg*16); }
  const v2u skr=*(const GAS v2u*)(p.sk+(cg*8+(lane&7))*8);
  #pragma unroll
  for(int i=0;i<4;i++) hr[i]=gld128(p.hk+(size_t)(cg*8+wave)*4096+i*1024+lane*16);
  SB; __builtin_amdgcn_s_barrier(); SB;
  kr[1]=gld128(p.k+ro+(size_t)512*1152); vr[1]=gld128(p.v+ro+(size_t)512*1152);
  SB;
  const float dd=lo2f(*(const GAS u16*)(p.dv+(cg*8+wave)*2));
  const unsigned cb=wave*B_PITCH;
  if(lane<56) st128(cb+(lane<28? lane*16 : 4576+(lane-28)*16),(v4u){0,0,0,0});
  if(lane<31) st32(cb+(lane<16? B_X0+4096+lane*4 : B_X1+4100+(lane-16)*4),0u);
  const float s0=lo2f(skr.x),s1=hi2f(skr.x),s2=lo2f(skr.y),s3=hi2f(skr.y);
  STAMP(1);
  b_prep(0,wave,lane,kr[0],vr[0],halo,s0,s1,s2,s3);
  #pragma unroll
  for(int i=0;i<4;i++) st128(cb+B_HA+i*1024+lane*16,hr[i]);
  #pragma unroll
  for(int i=0;i<4;i++){
    const unsigned pv=ld32(cb+B_HA+i*1024+lane*16-4);
    v4u s={__builtin_amdgcn_alignbit(hr[i].x,pv,16),__builtin_amdgcn_alignbit(hr[i].y,hr[i].x,16),
           __builtin_amdgcn_alignbit(hr[i].z,hr[i].y,16),__builtin_amdgcn_alignbit(hr[i].w,hr[i].z,16)};
    st128(cb+B_HB+i*1024+lane*16,s);
  }
  const int i16=lane&15, g=lane>>4;
  const unsigned hb0=cb+((i16&1)?B_HA:B_HB)+(32*(i16>>1)+8*g)*2;
  const unsigned xb0=cb+((i16&1)? B_X0+(2046-2*i16+8*g)*2 : B_X1+(2048-2*i16+8*g)*2)-512*7;
  f4 acc[8];
  #pragma unroll
  for(int j=0;j<8;j++) acc[j]=(f4){0,0,0,0};
  unsigned hb=hb0, xb=xb0;
  asm volatile("":"+v"(hb),"+v"(xb));
  SB;
  kr[2]=gld128(p.k+ro+(size_t)2*512*1152); vr[2]=gld128(p.v+ro+(size_t)2*512*1152);
  SB;
  ldsbar();
  STAMP(2);
  {
    BFr<0,1> f; b_load<0,1>(f,hb,xb);
    b_pair<0,1>(acc,f,hb,xb);
    STAMP(6);
    kr[3]=gld128(p.k+ro+(size_t)3*512*1152); vr[3]=gld128(p.v+ro+(size_t)3*512*1152);
    SB;
    STAMP(7);
    b_prep(1,wave,lane,kr[1],vr[1],halo,s0,s1,s2,s3);
    STAMP(8);
    b_pair<0,1>(acc,f,hb-128,xb-128);
    STAMP(9);
    qr[0]=gld128(p.q+ro); qr[1]=gld128(p.q+ro+(size_t)512*1152);
    SB;
    STAMP(10);
    b_prep(2,wave,lane,kr[2],vr[2],halo,s0,s1,s2,s3);
    STAMP(11);
    b_pair<0,1>(acc,f,hb-256,xb-256);
    qr[2]=gld128(p.q+ro+(size_t)2*512*1152); qr[3]=gld128(p.q+ro+(size_t)3*512*1152);
    SB;
    b_pair<0,1,true>(acc,f,hb-384,xb-384);
    STAMP(12);
    b_prep(3,wave,lane,kr[3],vr[3],halo,s0,s1,s2,s3);
    STAMP(13);
  }
  b_zout<0,1>(acc,wave,lane,dd);
  ldsbar();
  STAMP(3);
  char* outp=p.out+(size_t)tid*2304+1152+cg*16;
  {
    BFr<2,7> f; b_load<2,7>(f,hb,xb);
    b_gate(0,wave,lane,qr[0],outp);
    #pragma unroll 1
    for(int r=0;r<6;r+=2){ b_pair<2,7>(acc,f,hb,xb); hb-=128; xb-=128; }
    b_pair<2,7,true>(acc,f,hb,xb);
  }
  STAMP(4);
  b_zout<2,7>(acc,wave,lane,dd);
  ldsbar();
  b_gate(1,wave,lane,qr[1],outp); b_gate(2,wave,lane,qr[2],outp); b_gate(3,wave,lane,qr[3],outp);
  STAMP(5);
}

constexpr int A_TM=90, A_SLOT=41984, A_WOFF=14336, A_IMG=83968, A_IROW=456, A_OST=135040;
constexpr int WS_O=65536, WS_OT=6*72*256;
typedef __amdgpu_buffer_rsrc_t rsrc_t;
DEV void dma16(const v4u& srd, unsigned voff, unsigned soff, unsigned lds){
  asm volatile("s_mov_b32 m0, %3\n\ts_nop 0\n\tbuffer_load_dwordx4 %0, %1, %2 offen lds"::"v"(voff),"s"(srd),"s"(soff),"s"(lds):"memory");
}
DEV v4u mksrd(const char* p, unsigned bytes){
  const u64 a=(u64)p; v4u s={(unsigned)a,(unsigned)(a>>32)&0xffffu,bytes,0x00020000u};
  s.x=__builtin_amdgcn_readfirstlane(s.x); s.y=__builtin_amdgcn_readfirstlane(s.y); return s;
}
DEV float dot2(unsigned a, unsigned b, float c){ return __builtin_amdgcn_fdot2_f32_bf16(__builtin_bit_cast(b2,a),__builtin_bit_cast(b2,b),c,false); }
#define WAITV(n) asm volatile("s_waitcnt vmcnt(" #n ")":::"memory")
#define BAR do{ asm volatile("":::"memory"); __builtin_amdgcn_s_barrier(); asm volatile("":::"memory"); }while(0)

DEV void role_a(const Params& p, int rt, int grp STAMPARG){
  const int tid=threadIdx.x, lane=tid&63, wave=__builtin_amdgcn_readfirstlane(tid>>6);
  const int row0=rt*A_TM, nrow=min(A_TM,2048-row0);
  const int i16=lane&15, g=lane>>4;
  GAS u64* flags=(GAS u64*)(p.ws+rt*256);
  u64 ep=0;
  if(wave>=4){
    const int lw=wave-4;
    const v4u sx=mksrd(p.x,2048*1152), sw=mksrd(p.wqkv,576*3456), so=mksrd(p.wo,576*1152);
    unsigned vo[11];
    #pragma unroll
    for(int i=0;i<11;i++){
      const int pc=lw+4*i;
      if(pc<14){ const int ir=8*pc+(lane>>3); int tok=row0-16+ir; tok=max(tok,max(row0-14,0)); tok=min(tok,row0+nrow-1);
        vo[i]=tok*1152+(((lane&7)^((ir>>1)&7))<<4); }
      else { const int ci=(pc-14)*64+lane, rho=ci/27, c=ci-rho*27, seg=c/9, cc=c-seg*9;
        const int k=(rho&~7)+4*(rho&1)+((rho&7)>>1);
        vo[i]=k*3456+seg*1152+grp*144+cc*16; }
    }
    auto issue=[&](int s, int slot){
      #pragma unroll
      for(int i=0;i<11;i++){
        const int pc=lw+4*i;
        if(i<3 || (i==3 && pc<14)) dma16(sx,vo[i],s*128,slot*A_SLOT+pc*1024);
        else if(i<10 || pc<41) dma16(sw,vo[i],s*64*3456,slot*A_SLOT+pc*1024);
      }
    };
    issue(0,0); issue(1,1); issue(2,2);
    auto issue_wo=[&](int i0,int i1){
      for(int i=i0;i<i1;i++){ const int pc=lw+4*i; if(pc<81){ const int ci=pc*64+lane, rho=ci/9, cc=ci-rho*9; const int k=(rho&~7)+4*(rho&1)+((rho&7)>>1);
        dma16(so,k*1152+grp*144+cc*16,0,pc*1024); } }
    };
    int slot=0;
    #pragma unroll 1
    for(int s=0;s<9;s++){
      if(s==0){ if(lw==0) WAITV(22); else WAITV(20); }
      else if(s<8){ if(lw==0) WAITV(11); else WAITV(10); }
      else WAITV(10);
      BAR;
      if(s>=1 && s<7){ issue(s+2,slot); slot=(slot==2)?0:slot+1; }
      else if(s==7) issue_wo(0,10);
      else if(s==8) issue_wo(10,21);
    }
    STAMP(2);
    BAR;
  } else {
    if(tid==0) ep=__hip_atomic_load(flags+16,__ATOMIC_RELAXED,__HIP_MEMORY_SCOPE_AGENT);
    f4 acc[3][7], ace[3];
    #pragma unroll
    for(int c=0;c<3;c++){ ace[c]=(f4){0,0,0,0};
      #pragma unroll
      for(int m=0;m<7;m++) acc[c][m]=(f4){0,0,0,0}; }
    const int e0=(wave&1)?4:1, o0=(wave&1)?1:4;
    const unsigned xl=i16*128+((g^(i16>>1))<<4);
    const unsigned wl=A_WOFF+(8*g+2*(i16>>2))*432+(lane&3)*8;
    unsigned sb=0;
    #pragma unroll 1
    for(int s=0;s<9;s++){
      ldsbar();
      if(s==0) STAMP(1);
      #pragma unroll
      for(int kk=0;kk<2;kk++){
        const unsigned xa=sb+(xl^(kk*64));
        const unsigned wa=sb+wl+kk*13824+(2+3*wave)*32, we=sb+wl+kk*13824+(wave>>1)*32;
        v4u xf[7], wf[3], wx;
        #pragma unroll
        for(int m=0;m<3;m++) xf[m]=ld128(xa+e0*2048+m*2048);
        wx=cat(ldtr(we),ldtr(we+432));
        #pragma unroll
        for(int c=0;c<3;c++) wf[c]=cat(ldtr(wa+c*32),ldtr(wa+c*32+432));
        xf[3]=ld128(xa);
        #pragma unroll
        for(int m=0;m<3;m++) xf[4+m]=ld128(xa+o0*2048+m*2048);
        #pragma unroll
        for(int m=0;m<3;m++) ace[m]=mfma(wx,xf[m],ace[m]);
        #pragma unroll
        for(int m=0;m<7;m++){
          #pragma unroll
          for(int c=0;c<3;c++) acc[c][m]=mfma(wf[c],xf[m],acc[c][m]);
        }
      }
      sb=(sb==2*A_SLOT)?0:sb+A_SLOT;
    }
    STAMP(2);
    ldsbar();
    const unsigned ia=A_IMG+i16*A_IROW+g*8;
    #pragma unroll
    for(int m=0;m<7;m++){
      const int rtile=(m<3)?e0+m:(m==3?0:o0+m-4);
      #pragma unroll
      for(int c=0;c<3;c++) st64(ia+rtile*16*A_IROW+(2+3*wave+c)*32,(v2u){pk2(acc[c][m][0],acc[c][m][1]),pk2(acc[c][m][2],acc[c][m][3])});
      if(m<3) st64(ia+rtile*16*A_IROW+(wave>>1)*32,(v2u){pk2(ace[m][0],ace[m][1]),pk2(ace[m][2],ace[m][3])});
    }
  }
  ldsbar();
  STAMP(3);
  if(tid<4*A_TM){
    const int h=tid/A_TM, rr=tid-h*A_TM, odd=h&1;
    const unsigned ra=A_IMG+(16+rr)*A_IROW+36*h-4*odd;
    unsigned q[10];
    #pragma unroll
    for(int j=0;j<5;j++){ v2u t=ld64v(ra+8*j); q[2*j]=t.x; q[2*j+1]=t.y; }
    if(odd) q[0]=0; else q[9]=0;
    const unsigned rb=ra-14*A_IROW;
    float lg[8];
    #pragma unroll
    for(int w=0;w<8;w++){
      float a=0;
      #pragma unroll
      for(int j=0;j<5;j++){ v2u t=ld64v(rb+(14-2*w)*A_IROW+144+8*j); a=dot2(q[2*j],t.x,a); a=dot2(q[2*j+1],t.y,a); }
      lg[w]=(row0+rr-2*w>=0)? a*(0.23570226039551584f*1.4426950408889634f) : -1e9f;
    }
    float mx=lg[0];
    #pragma unroll
    for(int w=1;w<8;w++) mx=fmaxf(mx,lg[w]);
    float sum=0;
    #pragma unroll
    for(int w=0;w<8;w++){ lg[w]=__builtin_amdgcn_exp2f(lg[w]-mx); sum+=lg[w]; }
    const float inv=__builtin_amdgcn_rcpf(sum);
    float o[20];
    #pragma unroll
    for(int j=0;j<20;j++) o[j]=0;
    #pragma unroll
    for(int w=0;w<8;w++){
      const unsigned al=pk2(lg[w]*inv,0.f), ah=al<<16;
      #pragma unroll
      for(int j=0;j<5;j++){ v2u t=ld64v(rb+(14-2*w)*A_IROW+288+8*j);
        o[4*j]=dot2(t.x,al,o[4*j]); o[4*j+1]=dot2(t.x,ah,o[4*j+1]); o[4*j+2]=dot2(t.y,al,o[4*j+2]); o[4*j+3]=dot2(t.y,ah,o[4*j+3]); }
    }
    unsigned od[10];
    #pragma unroll
    for(int j=0;j<10;j++) od[j]=pk2(o[2*j],o[2*j+1]);
    const int col0=18*h-2*odd;
    const unsigned oa=A_OST+(rr>>4)*2304+(rr&15)*16;
    unsigned pa[5];
    #pragma unroll
    for(int j=0;j<5;j++){ const int col=col0+4*j; pa[j]=oa+(col>>3)*256+(col&4)*2; }
    st32(pa[0]+4,od[1]); st64(pa[1],(v2u){od[2],od[3]}); st64(pa[2],(v2u){od[4],od[5]}); st64(pa[3],(v2u){od[6],od[7]}); st32(pa[4],od[8]);
    st32(odd? pa[4]+4:pa[0], odd? od[9]:od[0]);
  }
  ldsbar();
  STAMP(4);
  const rsrc_t wsr=__builtin_amdgcn_make_buffer_rsrc(p.ws+WS_O,0,23*WS_OT,0x00020000);
  {
    const int c0=tid, m0=c0/144;
    __builtin_amdgcn_raw_buffer_store_b128(ld128(A_OST+c0*16),wsr,rt*WS_OT+(m0*72+9*grp)*256+(c0-m0*144)*16,0,16);
    const int c1=tid+512, m1=c1/144;
    if(c1<864) __builtin_amdgcn_raw_buffer_store_b128(ld128(A_OST+c1*16),wsr,rt*WS_OT+(m1*72+9*grp)*256+(c1-m1*144)*16,0,16);
  }
  WAITV(0);
  BAR;
  STAMP(5);
  if(wave==0){
    const u64 tag0=ep*6364136223846793005ull+1442695040888963407ull;
    const u64 tag=((u64)(unsigned)__builtin_amdgcn_readfirstlane((unsigned)(tag0>>32))<<32)|(unsigned)__builtin_amdgcn_readfirstlane((unsigned)tag0);
    if(lane==0) __hip_atomic_store(flags+grp,tag,__ATOMIC_RELAXED,__HIP_MEMORY_SCOPE_AGENT);
    for(int spin=0;spin<(1<<22);spin++){
      const u64 f=__hip_atomic_load(flags+(lane&7),__ATOMIC_RELAXED,__HIP_MEMORY_SCOPE_AGENT);
      if(__all(f==tag)) break;
      __builtin_amdgcn_s_sleep(1);
    }
    if(lane==0 && grp==0) __hip_atomic_store(flags+16,tag,__ATOMIC_RELAXED,__HIP_MEMORY_SCOPE_AGENT);
  }
  BAR;
  STAMP(6);
  {
    const int m=(wave<4)?wave:4+((wave-4)>>1), ks0=(wave<4)?0:9*(wave&1);
    const unsigned ob=rt*WS_OT+(m*72+4*ks0)*256+lane*16;
    v4u of[18];
    #pragma unroll
    for(int k=0;k<9;k++) of[k]=__builtin_amdgcn_raw_buffer_load_b128(wsr,ob+k*1024,0,16);
    if(wave<4){
      #pragma unroll
      for(int k=9;k<18;k++) of[k]=__builtin_amdgcn_raw_buffer_load_b128(wsr,ob+k*1024,0,16);
    }
    f4 acc[5];
    #pragma unroll
    for(int c=0;c<5;c++) acc[c]=(f4){0,0,0,0};
    const unsigned wa=(8*g+2*(i16>>2))*144+(lane&3)*8+ks0*4608;
    #pragma unroll
    for(int k=0;k<9;k++){
      #pragma unroll
      for(int c=0;c<5;c++) acc[c]=mfma(cat(ldtr(wa+k*4608+c*32),ldtr(wa+k*4608+c*32+144)),of[k],acc[c]);
    }
    const unsigned red=A_IMG+((wave>>1)&1)*5120+lane*16;
    if(wave>=4 && (wave&1)){
      #pragma unroll
      for(int c=0;c<5;c++) st128(red+c*1024,__builtin_bit_cast(v4u,acc[c]));
    }
    ldsbar();
    STAMP(7);
    if(wave<4){
      #pragma unroll
      for(int k=9;k<18;k++){
        #pragma unroll
        for(int c=0;c<5;c++) acc[c]=mfma(cat(ldtr(wa+k*4608+c*32),ldtr(wa+k*4608+c*32+144)),of[k],acc[c]);
      }
    } else if(!(wave&1)){
      #pragma unroll
      for(int c=0;c<5;c++) acc[c]+=__builtin_bit_cast(f4,ld128(red+c*1024));
    }
    const int rr=m*16+i16;
    if((wave<4 || !(wave&1)) && rr<nrow){
      char* op=p.out+(size_t)(row0+rr)*2304+grp*144+g*8;
      #pragma unroll
      for(int c=0;c<5;c++) if(c<4 || g<2) *(GAS v2u*)(op+c*32)=(v2u){pk2(acc[c][0],acc[c][1]),pk2(acc[c][2],acc[c][3])};
    }
  }
  STAMP(8);
}

__global__ __launch_bounds__(512,2) void fused(Params p){
  extern __shared__ __attribute__((aligned(16))) char smem[];
  STAMPDECL;
  STAMP(0);
  const int b=blockIdx.x, xcd=b&7, j=b>>3, pr=xcd>>1, half=xcd&1;
  const int nA=(pr<3)?24:20;
  if(j>=nA){
    const int jb=j-nA;
    role_b(p, jb<8? xcd*8+jb : 64+half*4+(jb-8) STAMPPASS);
  } else role_a(p, 6*pr+(j>>2), half*4+(j&3) STAMPPASS);
  asm volatile("s_waitcnt vmcnt(0)":::"memory");
  STAMP(15);
  if(blockIdx.x==PROBEBLK && threadIdx.x<64){ u64 d=(PROBE==0)?0:(g_stamp[1]-g_stamp[0]); if(d>5000) d=5000; u64 until=g_stamp[0]+10000+10*d; while(__builtin_amdgcn_s_memrealtime()<until) __builtin_amdgcn_s_sleep(1); }
}

constexpr int LDS_BYTES=160*1024;
extern "C" void kernel_launch(void* const* d_in, const int* in_sizes, int n_in, void* d_out, int out_size, void* d_ws, size_t ws_size, hipStream_t stream){
  Params p;
  p.x=(const char*)d_in[0]; p.wqkv=(const char*)d_in[1]; p.wo=(const char*)d_in[2];
  p.q=(const char*)d_in[3]; p.k=(const char*)d_in[4]; p.v=(const char*)d_in[5];
  p.sk=(const char*)d_in[6]; p.hk=(const char*)d_in[7]; p.dv=(const char*)d_in[8];
  p.out=(char*)d_out; p.ws=(char*)d_ws;
  (void)hipFuncSetAttribute((const void*)fused,hipFuncAttributeMaxDynamicSharedMemorySize,LDS_BYTES);
  fused<<<256,512,LDS_BYTES,stream>>>(p);
}
```

```cpp
#define PROBE 0
#define PROBEBLK 8
#include <hip/hip_runtime.h>
#include <hip/hip_bf16.h>

typedef unsigned short u16;
typedef unsigned long long u64;
typedef unsigned v4u __attribute__((ext_vector_type(4)));
typedef unsigned v2u __attribute__((ext_vector_type(2)));
typedef short s4 __attribute__((ext_vector_type(4)));
typedef __bf16 b2 __attribute__((ext_vector_type(2)));
typedef __bf16 b8 __attribute__((ext_vector_type(8)));
typedef float f4 __attribute__((ext_vector_type(4)));
typedef float f2 __attribute__((ext_vector_type(2)));
#define LAS __attribute__((address_space(3)))
#define GAS __attribute__((address_space(1)))
#define DEV __device__ __forceinline__
#define SB __builtin_amdgcn_sched_barrier(0)

#define STAMP(i) do{ if((i)==PROBE || (i)==0) { SB; asm volatile("s_memrealtime %0\n\ts_waitcnt lgkmcnt(0)":"=s"(g_stamp[(i)?1:0])::"memory"); SB; } }while(0)
#define STAMPDECL u64 g_stamp[2]={0,0}
#define STAMPARG , u64 (&g_stamp)[2]
#define STAMPPASS , g_stamp
#define STAMPOUT

struct Params {
  const char *x, *wqkv, *wo, *q, *k, *v, *sk, *hk, *dv;
  char *out, *ws;
};

DEV float lo2f(unsigned d){ return __uint_as_float(d<<16); }
DEV float hi2f(unsigned d){ return __uint_as_float(d&0xffff0000u); }
DEV unsigned pk2(float lo, float hi){ f2 p={lo,hi}; return __builtin_bit_cast(unsigned,__builtin_convertvector(p,b2)); }
DEV v4u ld128(unsigned a){ return *(LAS v4u*)a; }
DEV v2u ld64(unsigned a){ return *(LAS v2u*)a; }
DEV unsigned ld32(unsigned a){ return *(LAS unsigned*)a; }
DEV void st128(unsigned a, v4u v){ *(LAS v4u*)a=v; }
DEV void st64(unsigned a, v2u v){ *(LAS v2u*)a=v; }
DEV void st32(unsigned a, unsigned v){ *(LAS unsigned*)a=v; }
DEV v2u ldtr(unsigned a){ return __builtin_bit_cast(v2u,__builtin_amdgcn_ds_read_tr16_b64_v4i16((LAS s4*)a)); }
DEV f4 mfma(v4u a, v4u b, f4 c){ return __builtin_amdgcn_mfma_f32_16x16x32_bf16(__builtin_bit_cast(b8,a),__builtin_bit_cast(b8,b),c,0,0,0); }
DEV v4u cat(v2u a, v2u b){ return (v4u){a.x,a.y,b.x,b.y}; }
DEV v4u gld128(const char* p){ return *(const GAS v4u*)p; }
DEV void ldsbar(){ asm volatile("s_waitcnt lgkmcnt(0)":::"memory"); __builtin_amdgcn_s_barrier(); asm volatile("":::"memory"); }

constexpr int B_HA=448, B_HB=4576+448, B_X0=9120, B_X1=13344, B_PITCH=17520, B_SCR=8*B_PITCH;

DEV v2u ld64v(unsigned a){ return *(volatile LAS v2u*)a; }
template<int BLO,int BHI> struct BFr { v4u hf[8-BLO], xf[BHI-BLO+1]; };
template<int BLO,int BHI> DEV void b_load(BFr<BLO,BHI>& f, unsigned hb, unsigned xb){
  #pragma unroll
  for(int b=BLO;b<=BHI;b++) f.xf[b-BLO]=cat(ld64v(xb+512*(7-b)), ld64v(xb+512*(7-b)+8));
  #pragma unroll
  for(int a=0;a<8-BLO;a++) f.hf[a]=ld128(hb+512*a);
}
template<int BLO,int BHI> DEV void b_mma(f4 (&acc)[8], const BFr<BLO,BHI>& f){
  #pragma unroll
  for(int b=BLO;b<=BHI;b++){
    #pragma unroll
    for(int a=0;a<8-BLO;a++) if(a+b<8) acc[a+b]=mfma(f.hf[a],f.xf[b-BLO],acc[a+b]);
  }
}
template<int BLO,int BHI,bool LAST=false> DEV void b_pair(f4 (&acc)[8], BFr<BLO,BHI>& f0, unsigned hb, unsigned xb){
  BFr<BLO,BHI> f1;
  b_load<BLO,BHI>(f1,hb-64,xb-64);
  SB; b_mma<BLO,BHI>(acc,f0); SB;
  if(!LAST) b_load<BLO,BHI>(f0,hb-128,xb-128);
  SB; b_mma<BLO,BHI>(acc,f1); SB;
}

DEV void b_prep(int blk, int wave, int lane, v4u kr, v4u vr, v4u halo, float s0,float s1,float s2,float s3){
  const unsigned scr=B_SCR+wave*2112;
  st128(scr+64+lane*16,kr); st128(scr+1088+lane*16,vr);
  if((lane>>2)==blk) st128(scr+(lane&3)*16,halo);
  const int p=lane&3;
  const unsigned ta=scr+((8*(2*(lane>>4)+(p>>1))+((lane&15)>>2))*16)+(p&1)*8;
  v2u k0=ldtr(ta),k1=ldtr(ta+64),k2=ldtr(ta+128),v0=ldtr(ta+1088),v1=ldtr(ta+1088+64);
  float kk[11]={hi2f(k0.x),lo2f(k0.y),hi2f(k0.y),lo2f(k1.x),hi2f(k1.x),lo2f(k1.y),hi2f(k1.y),lo2f(k2.x),hi2f(k2.x),lo2f(k2.y),hi2f(k2.y)};
  float vv[8]={lo2f(v0.x),hi2f(v0.x),lo2f(v0.y),hi2f(v0.y),lo2f(v1.x),hi2f(v1.x),lo2f(v1.y),hi2f(v1.y)};
  float r[8];
  #pragma unroll
  for(int j=0;j<8;j++) r[j]=(kk[j]*s3+kk[j+1]*s2+kk[j+2]*s1+kk[j+3]*s0)*vv[j];
  v4u o={pk2(r[7],r[6]),pk2(r[5],r[4]),pk2(r[3],r[2]),pk2(r[1],r[0])};
  const int t0=512*blk+64*wave+8*(lane>>3);
  const unsigned a=(lane&7)*B_PITCH+(2040-t0)*2;
  st128(a+B_X0,o);
  st32(a+B_X1+4,o.x); st32(a+B_X1+8,o.y); st32(a+B_X1+12,o.z); st32(a+B_X1+16,o.w);
}

template<int JLO,int JHI> DEV void b_zout(f4 (&acc)[8], int wave, int lane, float dd){
  const int tl=64*(lane>>4)+2*(lane&15);
  const unsigned xa=wave*B_PITCH+B_X0+(2046-tl)*2-64-512*7, za=wave*B_PITCH+B_HA+tl*2;
  #pragma unroll
  for(int J=JLO;J<=JHI;J++){
    unsigned x0=ld32(xa+512*(7-J)+64), x1=ld32(xa+512*(7-J));
    st32(za+512*(7-J),    pk2(fmaf(hi2f(x0),dd,acc[J][0]),fmaf(lo2f(x0),dd,acc[J][1])));
    st32(za+512*(7-J)+64, pk2(fmaf(hi2f(x1),dd,acc[J][2]),fmaf(lo2f(x1),dd,acc[J][3])));
  }
}

DEV void b_gate(int pss, int wave, int lane, v4u q, char* outp){
  const int t=512*pss+64*wave+16*(lane>>4)+4*(lane&3);
  const unsigned a=((lane&15)>>2)*B_PITCH+B_HA+(256*(7-(t>>8))+(t&255))*2;
  v2u z0=ldtr(a), z1=ldtr(a+4*B_PITCH);
  v4u o={pk2(lo2f(q.x)*lo2f(z0.x),hi2f(q.x)*hi2f(z0.x)), pk2(lo2f(q.y)*lo2f(z0.y),hi2f(q.y)*hi2f(z0.y)),
         pk2(lo2f(q.z)*lo2f(z1.x),hi2f(q.z)*hi2f(z1.x)), pk2(lo2f(q.w)*lo2f(z1.y),hi2f(q.w)*hi2f(z1.y))};
  *(GAS v4u*)(outp+(size_t)pss*512*2304)=o;
}

DEV void role_b(const Params& p, int cg STAMPARG){
  const int tid=threadIdx.x, lane=tid&63, wave=__builtin_amdgcn_readfirstlane(tid>>6);
  const size_t ro=(size_t)tid*1152+cg*16;
  v4u kr[4],vr[4],qr[4],hr[4],halo={0,0,0,0};
  kr[0]=gld128(p.k+ro); vr[0]=gld128(p.v+ro);
  { const int ht=512*(lane>>2)+64*wave-4+(lane&3);
    if(lane<16 && ht>=0) halo=gld128(p.k+(size_t)ht*1152+cg*16); }
  const v2u skr=*(const GAS v2u*)(p.sk+(cg*8+(lane&7))*8);
  #pragma unroll
  for(int i=0;i<4;i++) hr[i]=gld128(p.hk+(size_t)(cg*8+wave)*4096+i*1024+lane*16);
  SB; __builtin_amdgcn_s_barrier(); SB;
  kr[1]=gld128(p.k+ro+(size_t)512*1152); vr[1]=gld128(p.v+ro+(size_t)512*1152);
  SB;
  const float dd=lo2f(*(const GAS u16*)(p.dv+(cg*8+wave)*2));
  const unsigned cb=wave*B_PITCH;
  if(lane<56) st128(cb+(lane<28? lane*16 : 4576+(lane-28)*16),(v4u){0,0,0,0});
  if(lane<31) st32(cb+(lane<16? B_X0+4096+lane*4 : B_X1+4100+(lane-16)*4),0u);
  const float s0=lo2f(skr.x),s1=hi2f(skr.x),s2=lo2f(skr.y),s3=hi2f(skr.y);
  STAMP(1);
  b_prep(0,wave,lane,kr[0],vr[0],halo,s0,s1,s2,s3);
  #pragma unroll
  for(int i=0;i<4;i++) st128(cb+B_HA+i*1024+lane*16,hr[i]);
  #pragma unroll
  for(int i=0;i<4;i++){
    const unsigned pv=ld32(cb+B_HA+i*1024+lane*16-4);
    v4u s={__builtin_amdgcn_alignbit(hr[i].x,pv,16),__builtin_amdgcn_alignbit(hr[i].y,hr[i].x,16),
           __builtin_amdgcn_alignbit(hr[i].z,hr[i].y,16),__builtin_amdgcn_alignbit(hr[i].w,hr[i].z,16)};
    st128(cb+B_HB+i*1024+lane*16,s);
  }
  const int i16=lane&15, g=lane>>4;
  const unsigned hb0=cb+((i16&1)?B_HA:B_HB)+(32*(i16>>1)+8*g)*2;
  const unsigned xb0=cb+((i16&1)? B_X0+(2046-2*i16+8*g)*2 : B_X1+(2048-2*i16+8*g)*2)-512*7;
  f4 acc[8];
  #pragma unroll
  for(int j=0;j<8;j++) acc[j]=(f4){0,0,0,0};
  unsigned hb=hb0, xb=xb0;
  asm volatile("":"+v"(hb),"+v"(xb));
  SB;
  kr[2]=gld128(p.k+ro+(size_t)2*512*1152); vr[2]=gld128(p.v+ro+(size_t)2*512*1152);
  SB;
  ldsbar();
  STAMP(2);
  {
    BFr<0,1> f; b_load<0,1>(f,hb,xb);
    b_pair<0,1>(acc,f,hb,xb);
    STAMP(6);
    kr[3]=gld128(p.k+ro+(size_t)3*512*1152); vr[3]=gld128(p.v+ro+(size_t)3*512*1152);
    SB;
    STAMP(7);
    b_prep(1,wave,lane,kr[1],vr[1],halo,s0,s1,s2,s3);
    STAMP(8);
    b_pair<0,1>(acc,f,hb-128,xb-128);
    STAMP(9);
    qr[0]=gld128(p.q+ro); qr[1]=gld128(p.q+ro+(size_t)512*1152);
    SB;
    STAMP(10);
    b_prep(2,wave,lane,kr[2],vr[2],halo,s0,s1,s2,s3);
    STAMP(11);
    b_pair<0,1>(acc,f,hb-256,xb-256);
    qr[2]=gld128(p.q+ro+(size_t)2*512*1152); qr[3]=gld128(p.q+ro+(size_t)3*512*1152);
    SB;
    b_pair<0,1,true>(acc,f,hb-384,xb-384);
    STAMP(12);
    b_prep(3,wave,lane,kr[3],vr[3],halo,s0,s1,s2,s3);
    STAMP(13);
  }
  b_zout<0,1>(acc,wave,lane,dd);
  ldsbar();
  STAMP(3);
  char* outp=p.out+(size_t)tid*2304+1152+cg*16;
  {
    BFr<2,7> f; b_load<2,7>(f,hb,xb);
    b_gate(0,wave,lane,qr[0],outp);
    #pragma unroll 1
    for(int r=0;r<6;r+=2){ b_pair<2,7>(acc,f,hb,xb); hb-=128; xb-=128; }
    b_pair<2,7,true>(acc,f,hb,xb);
  }
  STAMP(4);
  b_zout<2,7>(acc,wave,lane,dd);
  ldsbar();
  b_gate(1,wave,lane,qr[1],outp); b_gate(2,wave,lane,qr[2],outp); b_gate(3,wave,lane,qr[3],outp);
  STAMP(5);
}

constexpr int A_TM=90, A_SLOT=41984, A_WOFF=14336, A_IMG=83968, A_IROW=456, A_OST=135040;
constexpr int WS_O=65536, WS_OT=6*72*256;
typedef __amdgpu_buffer_rsrc_t rsrc_t;
DEV void dma16(const v4u& srd, unsigned voff, unsigned soff, unsigned lds){
  asm volatile("s_mov_b32 m0, %3\n\ts_nop 0\n\tbuffer_load_dwordx4 %0, %1, %2 offen lds"::"v"(voff),"s"(srd),"s"(soff),"s"(lds):"memory");
}
DEV v4u mksrd(const char* p, unsigned bytes){
  const u64 a=(u64)p; v4u s={(unsigned)a,(unsigned)(a>>32)&0xffffu,bytes,0x00020000u};
  s.x=__builtin_amdgcn_readfirstlane(s.x); s.y=__builtin_amdgcn_readfirstlane(s.y); return s;
}
DEV float dot2(unsigned a, unsigned b, float c){ return __builtin_amdgcn_fdot2_f32_bf16(__builtin_bit_cast(b2,a),__builtin_bit_cast(b2,b),c,false); }
#define WAITV(n) asm volatile("s_waitcnt vmcnt(" #n ")":::"memory")
#define BAR do{ asm volatile("":::"memory"); __builtin_amdgcn_s_barrier(); asm volatile("":::"memory"); }while(0)

DEV void role_a(const Params& p, int rt, int grp STAMPARG){
  const int tid=threadIdx.x, lane=tid&63, wave=__builtin_amdgcn_readfirstlane(tid>>6);
  const int row0=rt*A_TM, nrow=min(A_TM,2048-row0);
  const int i16=lane&15, g=lane>>4;
  GAS u64* flags=(GAS u64*)(p.ws+rt*256);
  u64 ep=0;
  if(wave>=4){
    const int lw=wave-4;
    const v4u sx=mksrd(p.x,2048*1152), sw=mksrd(p.wqkv,576*3456), so=mksrd(p.wo,576*1152);
    unsigned vo[11];
    #pragma unroll
    for(int i=0;i<11;i++){
      const int pc=lw+4*i;
      if(pc<14){ const int ir=8*pc+(lane>>3); int tok=row0-16+ir; tok=max(tok,max(row0-14,0)); tok=min(tok,row0+nrow-1);
        vo[i]=tok*1152+(((lane&7)^((ir>>1)&7))<<4); }
      else { const int ci=(pc-14)*64+lane, rho=ci/27, c=ci-rho*27, seg=c/9, cc=c-seg*9;
        const int k=(rho&~7)+4*(rho&1)+((rho&7)>>1);
        vo[i]=k*3456+seg*1152+grp*144+cc*16; }
    }
    auto issue=[&](int s, int slot){
      #pragma unroll
      for(int i=0;i<11;i++){
        const int pc=lw+4*i;
        if(i<3 || (i==3 && pc<14)) dma16(sx,vo[i],s*128,slot*A_SLOT+pc*1024);
        else if(i<10 || pc<41) dma16(sw,vo[i],s*64*3456,slot*A_SLOT+pc*1024);
      }
    };
    issue(0,0); issue(1,1); issue(2,2);
    auto issue_wo=[&](int i0,int i1){
      for(int i=i0;i<i1;i++){ const int pc=lw+4*i; if(pc<81){ const int ci=pc*64+lane, rho=ci/9, cc=ci-rho*9; const int k=(rho&~7)+4*(rho&1)+((rho&7)>>1);
        dma16(so,k*1152+grp*144+cc*16,0,pc*1024); } }
    };
    int slot=0;
    #pragma unroll 1
    for(int s=0;s<9;s++){
      if(s==0){ if(lw==0) WAITV(22); else WAITV(20); }
      else if(s<8){ if(lw==0) WAITV(11); else WAITV(10); }
      else WAITV(10);
      BAR;
      if(s>=1 && s<7){ issue(s+2,slot); slot=(slot==2)?0:slot+1; }
      else if(s==7) issue_wo(0,10);
      else if(s==8) issue_wo(10,21);
    }
    STAMP(2);
    BAR;
  } else {
    if(tid==0) ep=__hip_atomic_load(flags+16,__ATOMIC_RELAXED,__HIP_MEMORY_SCOPE_AGENT);
    f4 acc[3][7], ace[3];
    #pragma unroll
    for(int c=0;c<3;c++){ ace[c]=(f4){0,0,0,0};
      #pragma unroll
      for(int m=0;m<7;m++) acc[c][m]=(f4){0,0,0,0}; }
    const int e0=(wave&1)?4:1, o0=(wave&1)?1:4;
    const unsigned xl=i16*128+((g^(i16>>1))<<4);
    const unsigned wl=A_WOFF+(8*g+2*(i16>>2))*432+(lane&3)*8;
    unsigned sb=0;
    #pragma unroll 1
    for(int s=0;s<9;s++){
      ldsbar();
      if(s==0) STAMP(1);
      #pragma unroll
      for(int kk=0;kk<2;kk++){
        const unsigned xa=sb+(xl^(kk*64));
        const unsigned wa=sb+wl+kk*13824+(2+3*wave)*32, we=sb+wl+kk*13824+(wave>>1)*32;
        v4u xf[7], wf[3], wx;
        #pragma unroll
        for(int m=0;m<3;m++) xf[m]=ld128(xa+e0*2048+m*2048);
        wx=cat(ldtr(we),ldtr(we+432));
        #pragma unroll
        for(int c=0;c<3;c++) wf[c]=cat(ldtr(wa+c*32),ldtr(wa+c*32+432));
        xf[3]=ld128(xa);
        #pragma unroll
        for(int m=0;m<3;m++) xf[4+m]=ld128(xa+o0*2048+m*2048);
        #pragma unroll
        for(int m=0;m<3;m++) ace[m]=mfma(wx,xf[m],ace[m]);
        #pragma unroll
        for(int m=0;m<7;m++){
          #pragma unroll
          for(int c=0;c<3;c++) acc[c][m]=mfma(wf[c],xf[m],acc[c][m]);
        }
      }
      sb=(sb==2*A_SLOT)?0:sb+A_SLOT;
    }
    STAMP(2);
    ldsbar();
    const unsigned ia=A_IMG+i16*A_IROW+g*8;
    #pragma unroll
    for(int m=0;m<7;m++){
      const int rtile=(m<3)?e0+m:(m==3?0:o0+m-4);
      #pragma unroll
      for(int c=0;c<3;c++) st64(ia+rtile*16*A_IROW+(2+3*wave+c)*32,(v2u){pk2(acc[c][m][0],acc[c][m][1]),pk2(acc[c][m][2],acc[c][m][3])});
      if(m<3) st64(ia+rtile*16*A_IROW+(wave>>1)*32,(v2u){pk2(ace[m][0],ace[m][1]),pk2(ace[m][2],ace[m][3])});
    }
  }
  ldsbar();
  STAMP(3);
  if(tid<4*A_TM){
    const int h=tid/A_TM, rr=tid-h*A_TM, odd=h&1;
    const unsigned ra=A_IMG+(16+rr)*A_IROW+36*h-4*odd;
    unsigned q[10];
    #pragma unroll
    for(int j=0;j<5;j++){ v2u t=ld64v(ra+8*j); q[2*j]=t.x; q[2*j+1]=t.y; }
    if(odd) q[0]=0; else q[9]=0;
    const unsigned rb=ra-14*A_IROW;
    float lg[8];
    #pragma unroll
    for(int w=0;w<8;w++){
      float a=0;
      #pragma unroll
      for(int j=0;j<5;j++){ v2u t=ld64v(rb+(14-2*w)*A_IROW+144+8*j); a=dot2(q[2*j],t.x,a); a=dot2(q[2*j+1],t.y,a); }
      lg[w]=(row0+rr-2*w>=0)? a*(0.23570226039551584f*1.4426950408889634f) : -1e9f;
    }
    float mx=lg[0];
    #pragma unroll
    for(int w=1;w<8;w++) mx=fmaxf(mx,lg[w]);
    float sum=0;
    #pragma unroll
    for(int w=0;w<8;w++){ lg[w]=__builtin_amdgcn_exp2f(lg[w]-mx); sum+=lg[w]; }
    const float inv=__builtin_amdgcn_rcpf(sum);
    float o[20];
    #pragma unroll
    for(int j=0;j<20;j++) o[j]=0;
    #pragma unroll
    for(int w=0;w<8;w++){
      const unsigned al=pk2(lg[w]*inv,0.f), ah=al<<16;
      #pragma unroll
      for(int j=0;j<5;j++){ v2u t=ld64v(rb+(14-2*w)*A_IROW+288+8*j);
        o[4*j]=dot2(t.x,al,o[4*j]); o[4*j+1]=dot2(t.x,ah,o[4*j+1]); o[4*j+2]=dot2(t.y,al,o[4*j+2]); o[4*j+3]=dot2(t.y,ah,o[4*j+3]); }
    }
    unsigned od[10];
    #pragma unroll
    for(int j=0;j<10;j++) od[j]=pk2(o[2*j],o[2*j+1]);
    const int col0=18*h-2*odd;
    const unsigned oa=A_OST+(rr>>4)*2304+(rr&15)*16;
    unsigned pa[5];
    #pragma unroll
    for(int j=0;j<5;j++){ const int col=col0+4*j; pa[j]=oa+(col>>3)*256+(col&4)*2; }
    st32(pa[0]+4,od[1]); st64(pa[1],(v2u){od[2],od[3]}); st64(pa[2],(v2u){od[4],od[5]}); st64(pa[3],(v2u){od[6],od[7]}); st32(pa[4],od[8]);
    st32(odd? pa[4]+4:pa[0], odd? od[9]:od[0]);
  }
  ldsbar();
  STAMP(4);
  const rsrc_t wsr=__builtin_amdgcn_make_buffer_rsrc(p.ws+WS_O,0,23*WS_OT,0x00020000);
  {
    const int c0=tid, m0=c0/144;
    __builtin_amdgcn_raw_buffer_store_b128(ld128(A_OST+c0*16),wsr,rt*WS_OT+(m0*72+9*grp)*256+(c0-m0*144)*16,0,16);
    const int c1=tid+512, m1=c1/144;
    if(c1<864) __builtin_amdgcn_raw_buffer_store_b128(ld128(A_OST+c1*16),wsr,rt*WS_OT+(m1*72+9*grp)*256+(c1-m1*144)*16,0,16);
  }
  WAITV(0);
  BAR;
  STAMP(5);
  if(wave==0){
    const u64 tag0=ep*6364136223846793005ull+1442695040888963407ull;
    const u64 tag=((u64)(unsigned)__builtin_amdgcn_readfirstlane((unsigned)(tag0>>32))<<32)|(unsigned)__builtin_amdgcn_readfirstlane((unsigned)tag0);
    if(lane==0) __hip_atomic_store(flags+grp,tag,__ATOMIC_RELAXED,__HIP_MEMORY_SCOPE_AGENT);
    for(int spin=0;spin<(1<<22);spin++){
      const u64 f=__hip_atomic_load(flags+(lane&7),__ATOMIC_RELAXED,__HIP_MEMORY_SCOPE_AGENT);
      if(__all(f==tag)) break;
      __builtin_amdgcn_s_sleep(1);
    }
    if(lane==0 && grp==0) __hip_atomic_store(flags+16,tag,__ATOMIC_RELAXED,__HIP_MEMORY_SCOPE_AGENT);
  }
  BAR;
  STAMP(6);
  {
    const int m=(wave<4)?wave:4+((wave-4)>>1), ks0=(wave<4)?0:9*(wave&1);
    const unsigned ob=rt*WS_OT+(m*72+4*ks0)*256+lane*16;
    v4u of[18];
    #pragma unroll
    for(int k=0;k<9;k++) of[k]=__builtin_amdgcn_raw_buffer_load_b128(wsr,ob+k*1024,0,16);
    if(wave<4){
      #pragma unroll
      for(int k=9;k<18;k++) of[k]=__builtin_amdgcn_raw_buffer_load_b128(wsr,ob+k*1024,0,16);
    }
    f4 acc[5];
    #pragma unroll
    for(int c=0;c<5;c++) acc[c]=(f4){0,0,0,0};
    const unsigned wa=(8*g+2*(i16>>2))*144+(lane&3)*8+ks0*4608;
    #pragma unroll
    for(int k=0;k<9;k++){
      #pragma unroll
      for(int c=0;c<5;c++) acc[c]=mfma(cat(ldtr(wa+k*4608+c*32),ldtr(wa+k*4608+c*32+144)),of[k],acc[c]);
    }
    const unsigned red=A_IMG+((wave>>1)&1)*5120+lane*16;
    if(wave>=4 && (wave&1)){
      #pragma unroll
      for(int c=0;c<5;c++) st128(red+c*1024,__builtin_bit_cast(v4u,acc[c]));
    }
    ldsbar();
    STAMP(7);
    if(wave<4){
      #pragma unroll
      for(int k=9;k<18;k++){
        #pragma unroll
        for(int c=0;c<5;c++) acc[c]=mfma(cat(ldtr(wa+k*4608+c*32),ldtr(wa+k*4608+c*32+144)),of[k],acc[c]);
      }
    } else if(!(wave&1)){
      #pragma unroll
      for(int c=0;c<5;c++) acc[c]+=__builtin_bit_cast(f4,ld128(red+c*1024));
    }
    const int rr=m*16+i16;
    if((wave<4 || !(wave&1)) && rr<nrow){
      char* op=p.out+(size_t)(row0+rr)*2304+grp*144+g*8;
      #pragma unroll
      for(int c=0;c<5;c++) if(c<4 || g<2) *(GAS v2u*)(op+c*32)=(v2u){pk2(acc[c][0],acc[c][1]),pk2(acc[c][2],acc[c][3])};
    }
  }
  STAMP(8);
}

__global__ __launch_bounds__(512,2) void fused(Params p){
  extern __shared__ __attribute__((aligned(16))) char smem[];
  STAMPDECL;
  STAMP(0);
  const int b=blockIdx.x, xcd=b&7, j=b>>3, pr=xcd>>1, half=xcd&1;
  const int nA=(pr<3)?24:20;
  if(j>=nA){
    const int jb=j-nA;
    role_b(p, jb<8? xcd*8+jb : 64+half*4+(jb-8) STAMPPASS);
  } else role_a(p, 6*pr+(j>>2), half*4+(j&3) STAMPPASS);
  asm volatile("s_waitcnt vmcnt(0)":::"memory");
  STAMP(15);
}

__global__ void k_rec(u64* w){ if(threadIdx.x==0) __hip_atomic_store(w,(u64)__builtin_amdgcn_s_memrealtime(),__ATOMIC_RELAXED,__HIP_MEMORY_SCOPE_AGENT); }
__global__ void k_spin(u64* w){ u64 t=__hip_atomic_load(w,__ATOMIC_RELAXED,__HIP_MEMORY_SCOPE_AGENT); u64 now=__builtin_amdgcn_s_memrealtime(); u64 until=t+10000; if(until>now+20000) until=now; while(__builtin_amdgcn_s_memrealtime()<until) __builtin_amdgcn_s_sleep(1); }
constexpr int LDS_BYTES=160*1024;
extern "C" void kernel_launch(void* const* d_in, const int* in_sizes, int n_in, void* d_out, int out_size, void* d_ws, size_t ws_size, hipStream_t stream){
  Params p;
  p.x=(const char*)d_in[0]; p.wqkv=(const char*)d_in[1]; p.wo=(const char*)d_in[2];
  p.q=(const char*)d_in[3]; p.k=(const char*)d_in[4]; p.v=(const char*)d_in[5];
  p.sk=(const char*)d_in[6]; p.hk=(const char*)d_in[7]; p.dv=(const char*)d_in[8];
  p.out=(char*)d_out; p.ws=(char*)d_ws;
  (void)hipFuncSetAttribute((const void*)fused,hipFuncAttributeMaxDynamicSharedMemorySize,LDS_BYTES);
  k_rec<<<1,64,0,stream>>>((u64*)((char*)d_ws+(32<<20)));
  fused<<<256,512,LDS_BYTES,stream>>>(p);
  k_spin<<<1,64,0,stream>>>((u64*)((char*)d_ws+(32<<20)));
}
```

```cpp
#define NOROT
#include <hip/hip_runtime.h>
#include <hip/hip_bf16.h>

typedef unsigned short u16;
typedef unsigned long long u64;
typedef unsigned v4u __attribute__((ext_vector_type(4)));
typedef unsigned v2u __attribute__((ext_vector_type(2)));
typedef short s4 __attribute__((ext_vector_type(4)));
typedef __bf16 b2 __attribute__((ext_vector_type(2)));
typedef __bf16 b8 __attribute__((ext_vector_type(8)));
typedef float f4 __attribute__((ext_vector_type(4)));
typedef float f2 __attribute__((ext_vector_type(2)));
#define LAS __attribute__((address_space(3)))
#define GAS __attribute__((address_space(1)))
#if defined(__HIP_DEVICE_COMPILE__)
#define NOPK __attribute__((target("no-packed-fp32-ops")))
#else
#define NOPK
#endif
#define DEV __device__ __forceinline__ NOPK
#define SB __builtin_amdgcn_sched_barrier(0)
#define SGB(m,n) __builtin_amdgcn_sched_group_barrier(m,n,0)
#pragma clang diagnostic ignored "-Wint-to-pointer-cast"

#define STAMP(i)
#define STAMPDECL
#define STAMPARG
#define STAMPPASS
#define STAMPOUT

#define PNPRE 6
#define PNISS 3
#define PNF 4
struct Params {
  const char *x, *wqkv, *wo, *q, *k, *v, *sk, *hk, *dv;
  char *out, *ws;
};

DEV float lo2f(unsigned d){ return __builtin_bit_cast(float,d<<16); }
DEV float hi2f(unsigned d){ return __builtin_bit_cast(float,d&0xffff0000u); }
DEV unsigned pk2(float lo, float hi){ f2 p={lo,hi}; return __builtin_bit_cast(unsigned,__builtin_convertvector(p,b2)); }
DEV unsigned pk2a(float lo, float hi){ unsigned d; asm("v_cvt_pk_bf16_f32 %0, %1, %2":"=v"(d):"v"(lo),"v"(hi)); return d; }
DEV v4u ld128(unsigned a){ return *(LAS v4u*)a; }
DEV v2u ld64v(unsigned a){ return *(volatile LAS v2u*)a; }
DEV unsigned ld32(unsigned a){ return *(LAS unsigned*)a; }
DEV void st128(unsigned a, v4u v){ *(LAS v4u*)a=v; }
DEV void st64(unsigned a, v2u v){ *(LAS v2u*)a=v; }
DEV void st64v(unsigned a, v2u v){ *(volatile LAS v2u*)a=v; }
DEV void st32(unsigned a, unsigned v){ *(LAS unsigned*)a=v; }
DEV v2u ldtr(unsigned a){ return __builtin_bit_cast(v2u,__builtin_amdgcn_ds_read_tr16_b64_v4i16((LAS s4*)a)); }
DEV f4 mfma(v4u a, v4u b, f4 c){ return __builtin_amdgcn_mfma_f32_16x16x32_bf16(__builtin_bit_cast(b8,a),__builtin_bit_cast(b8,b),c,0,0,0); }
DEV v4u cat(v2u a, v2u b){ return (v4u){a.x,a.y,b.x,b.y}; }
DEV v4u gld128(const char* p){ return *(const GAS v4u*)p; }
DEV void gload(v4u& d, const char* sbase, unsigned voff){ asm volatile("global_load_dwordx4 %0, %1, %2":"=v"(d):"v"(voff),"s"(sbase):"memory"); }
DEV void greload(v4u& d, const char* sbase, unsigned voff){
 asm volatile("global_load_dwordx4 %0, %1, %2":"+v"(d):"v"(voff),"s"(sbase):"memory"); }
#define WAIT1(n,a) asm volatile("s_waitcnt vmcnt(" #n ")":"+v"(a)::"memory")
#define WAIT2(n,a,b) asm volatile("s_waitcnt vmcnt(" #n ")":"+v"(a),"+v"(b)::"memory")
#define WAIT3(n,a,b,c) asm volatile("s_waitcnt vmcnt(" #n ")":"+v"(a),"+v"(b),"+v"(c)::"memory")
#define WAIT4(n,a,b,c,d) asm volatile("s_waitcnt vmcnt(" #n ")":"+v"(a),"+v"(b),"+v"(c),"+v"(d)::"memory")
#define WAITL asm volatile("":::"memory"); __builtin_amdgcn_s_waitcnt(0xC07F); asm volatile("":::"memory")
DEV void ldsbar(){ WAITL; __builtin_amdgcn_s_barrier(); asm volatile("":::"memory"); }
DEV int imin(int a,int b){ return a<b?a:b; }
DEV int imax(int a,int b){ return a>b?a:b; }
#define WAITV(n) asm volatile("s_waitcnt vmcnt(" #n ")":::"memory")
#define BAR do{ asm volatile("":::"memory"); __builtin_amdgcn_s_barrier(); asm volatile("":::"memory"); }while(0)
template<int NR,int NM> DEV void interleave(){
  constexpr int PER=(NR+NM-1)/NM, NG=(NR+PER-1)/PER;
  #pragma unroll
  for(int i=0;i<NG;i++){ SGB(0x8,1); SGB(0x100,PER); }
  if(NM>NG) SGB(0x8,NM-NG);
}

#define AMM(acc,a,b) asm volatile("v_mfma_f32_16x16x32_bf16 %0, %1, %2, %0":"+v"(acc):"v"(a),"v"(b))
#define ARD(d,a,off) asm volatile("ds_read_b128 %0, %1 offset:%2":"=v"(d):"v"(a),"i"(off):"memory")
#define ATR(d,a,off) asm volatile("ds_read_b64_tr_b16 %0, %1 offset:%2":"=v"(d):"v"(a),"i"(off):"memory")
#define AADD(v,s) asm volatile("v_add_u32 %0, %1, %0":"+v"(v):"s"(s))
#define AR64(d,a,off) asm volatile("ds_read_b64 %0, %1 offset:%2":"=v"(d):"v"(a),"i"(off):"memory")
constexpr int B_HA=448, B_HB=4576+448, B_X0=9120, B_X1=13344, B_PITCH=17520, B_SCR=8*B_PITCH;

template<int BLO,int NB> struct PF { v4u h[8-BLO]; v2u x[NB][2]; };
template<int BLO,int NB,int OFF> DEV void p_rd(int i, PF<BLO,NB>& f, unsigned hb, unsigned xb){
  #pragma unroll
  for(int a=0;a<8-BLO;a++) if(i==2+a) ARD(f.h[a],hb,512*a+OFF);
  #pragma unroll
  for(int b=0;b<NB;b++){ const int q=b?8-BLO+2*b:0; if(i==q) AR64(f.x[b][0],xb,512*(7-BLO-b)+OFF); if(i==q+1) AR64(f.x[b][1],xb,512*(7-BLO-b)+8+OFF); }
}
template<int BLO,int NB> DEV void p_mm(int i, f4 (&acc)[8], PF<BLO,NB>& f){
  int n=0;
  #pragma unroll
  for(int b=0;b<NB;b++){
    #pragma unroll
    for(int a=0;a<8-BLO-b;a++){ if(n==i){ v4u x=cat(f.x[b][0],f.x[b][1]); AMM(acc[a+b+BLO],f.h[a],x); } n++; }
  }
}
template<int N,int BLO,int NB> DEV void p_wait(PF<BLO,NB>& f){
  asm volatile("s_waitcnt lgkmcnt(%0)"::"i"(N):"memory");
  #pragma unroll
  for(int a=0;a<8-BLO;a++) asm volatile("":"+v"(f.h[a]));
  #pragma unroll
  for(int b=0;b<NB;b++) asm volatile("":"+v"(f.x[b][0]),"+v"(f.x[b][1]));
}
template<int BLO,int NB,int OFF> DEV void p_block(f4 (&acc)[8], PF<BLO,NB>& fc, PF<BLO,NB>& fn, unsigned hb, unsigned xb){
  constexpr int NR=8-BLO+2*NB, NM=NB*(8-BLO)-NB*(NB-1)/2, PER=(NR+NM-1)/NM;
  constexpr int LAG=(BLO==6)?2:4, I2=(BLO==6)?2:(NB==2? 6-BLO : (NB-2)*(8-BLO)-(NB-2)*(NB-3)/2);
  p_wait<LAG>(fc);
  #pragma unroll
  for(int i=0;i<NM;i++){
    if(i==I2) asm volatile("s_waitcnt lgkmcnt(%0)"::"i"(I2*PER<NR?I2*PER:NR):"memory");
    p_mm<BLO,NB>(i,acc,fc);
    #pragma unroll
    for(int j=0;j<PER;j++) if(i*PER+j<NR) p_rd<BLO,NB,OFF>(i*PER+j,fn,hb,xb);
  }
}
template<int BLO,int NB> DEV void b_seg(f4 (&acc)[8], PF<BLO,NB>& f0, unsigned hb, unsigned xb, int next){
  PF<BLO,NB> f1;
  #pragma unroll 1
  for(int r=0;r<4;r++){
    p_block<BLO,NB,64>(acc,f0,f1,hb,xb);
    const int d=(r==3)? next-384 : 128;
    AADD(hb,d); AADD(xb,d);
    p_block<BLO,NB,0>(acc,f1,f0,hb,xb);
  }
}
template<int JLO,int NJ> DEV void b_zout(f4 (&acc)[8], unsigned za){
  asm volatile("s_nop 7\n\ts_nop 3":::"memory");
  #pragma unroll
  for(int J=JLO;J<JLO+NJ;J++){
    asm volatile("":"+v"(acc[J]));
    st32(za+512*(7-J)+64,pk2a(acc[J][1],acc[J][0])); st32(za+512*(7-J),pk2a(acc[J][3],acc[J][2]));
  }
}
template<int BLO,int NB> DEV void b_stage(f4 (&a0)[8], f4 (&a1)[8], unsigned hb, unsigned xb, unsigned za){
  PF<BLO,NB> f;
  #pragma unroll
  for(int i=2;i<10-BLO;i++) p_rd<BLO,NB,0>(i,f,hb,xb);
  ldsbar();
  p_rd<BLO,NB,0>(0,f,hb,xb); p_rd<BLO,NB,0>(1,f,hb,xb);
  #pragma unroll
  for(int i=10-BLO;i<8-BLO+2*NB;i++) p_rd<BLO,NB,0>(i,f,hb,xb);
  asm volatile("s_waitcnt lgkmcnt(%0)"::"i"(2*NB-2):"memory");
  b_seg<BLO,NB>(a0,f,hb,xb,B_PITCH);
  b_zout<BLO,NB>(a0,za);
  b_seg<BLO,NB>(a1,f,hb+B_PITCH,xb+B_PITCH,0);
  b_zout<BLO,NB>(a1,za+B_PITCH);
  p_wait<0>(f);
}

struct BC { v4u slo,shi,i01,i23; };
DEV void b_prep(unsigned ta, unsigned a, v4u vr, const BC& c){
  const v4u a00=ld128(ta), a01=ld128(ta+512), a10=ld128(ta+64), a11=ld128(ta+576);
  const f4 z={0,0,0,0};
  const f4 v1=mfma(vr,c.i01,z), v2=mfma(vr,c.i23,z);
  const f4 d1=mfma(a01,c.shi,mfma(a00,c.slo,z)), d2=mfma(a11,c.shi,mfma(a10,c.slo,z));
  const v4u o={pk2(d2[3]*v2[3],d2[2]*v2[2]),pk2(d2[1]*v2[1],d2[0]*v2[0]),pk2(d1[3]*v1[3],d1[2]*v1[2]),pk2(d1[1]*v1[1],d1[0]*v1[0])};
  st128(a+B_X0,o);
  st32(a+B_X1+4,o.x); st64(a+B_X1+8,(v2u){o.y,o.z}); st32(a+B_X1+16,o.w);
}
DEV void b_gate(unsigned ga, unsigned gb, v4u q, const float (&dd)[8], char* outp){
  const v2u y0=ldtr(ga), x0=ldtr(ga+B_X0-B_HA), y1=ldtr(gb), x1=ldtr(gb+B_X0-B_HA);
  #define G2(Q,Y,X,i) pk2(lo2f(Q)*__builtin_fmaf(lo2f(X),dd[i],lo2f(Y)), hi2f(Q)*__builtin_fmaf(hi2f(X),dd[i+1],hi2f(Y)))
  const v4u o={G2(q.x,y0.x,x0.x,0),G2(q.y,y0.y,x0.y,2),G2(q.z,y1.x,x1.x,4),G2(q.w,y1.y,x1.y,6)};
  *(GAS v4u*)outp=o;
}

DEV void role_b(const Params& p, int cg STAMPARG){
  const int tid=__builtin_amdgcn_workitem_id_x(), lane=tid&63, wave=__builtin_amdgcn_readfirstlane(tid>>6);
  const int i16=lane&15, g=lane>>4;
  constexpr unsigned BLK=512*1152;
  if(wave<4){
    v4u hr[8];
    #pragma unroll
    for(int i=0;i<8;i++) gload(hr[i],p.hk,(cg*8+2*wave)*4096+i*1024+lane*16);
    const unsigned cb=2*wave*B_PITCH;
    #pragma unroll
    for(int c=0;c<2;c++){
      if(lane<56) st128(cb+c*B_PITCH+(lane<28? lane*16 : 4576+(lane-28)*16),(v4u){0,0,0,0});
      if(lane<31) st32(cb+c*B_PITCH+(lane<16? B_X0+4096+lane*4 : B_X1+4100+(lane-16)*4),0u);
    }
    unsigned hb=cb+((i16&1)?B_HA:B_HB)+(32*(i16>>1)+8*g)*2-448;
    unsigned xb=cb+((i16&1)? B_X0+(2046-2*i16+8*g)*2 : B_X1+(2048-2*i16+8*g)*2)-512*7-448;
    unsigned za=cb+B_HA+(2046-64*g-2*i16)*2-64-512*7;
    asm volatile("":"+v"(hb),"+v"(xb),"+v"(za));
    f4 a0[8],a1[8];
    #pragma unroll
    for(int j=0;j<8;j++){ a0[j]=(f4){0,0,0,0}; a1[j]=(f4){0,0,0,0}; asm volatile("":"+v"(a0[j]),"+v"(a1[j])); }
    #pragma unroll
    for(int c=0;c<2;c++){
      if(c==0) WAIT4(4,hr[0],hr[1],hr[2],hr[3]); else WAIT4(0,hr[4],hr[5],hr[6],hr[7]);
      #pragma unroll
      for(int i=0;i<4;i++){
        const v4u h=hr[4*c+i];
        const unsigned first=i? __builtin_amdgcn_readlane(hr[4*c+i-1].w,63) : 0u;
        const unsigned pv=__builtin_amdgcn_update_dpp(first,h.w,0x138,0xf,0xf,false);
        const v4u s={__builtin_amdgcn_alignbit(h.x,pv,16),__builtin_amdgcn_alignbit(h.y,h.x,16),__builtin_amdgcn_alignbit(h.z,h.y,16),__builtin_amdgcn_alignbit(h.w,h.z,16)};
        st128(cb+c*B_PITCH+B_HA+i*1024+lane*16,h);
        st128(cb+c*B_PITCH+B_HB+i*1024+lane*16,s);
      }
    }
    STAMP(1);
    __builtin_amdgcn_s_setprio(1);
    b_stage<0,2>(a0,a1,hb,xb,za);
    STAMP(3);
    b_stage<2,4>(a0,a1,hb,xb,za);
    STAMP(4);
    b_stage<6,2>(a0,a1,hb,xb,za);
    STAMP(5);
    ldsbar();
  } else {
    const int w=wave-4;
    const int vl=32*(g&1)+8*(i16>>2)+4*(g>>1)+(lane&3);
    const int ql=lane^3;
    const unsigned rk=(w*128+lane)*1152+cg*16, rv=(w*128+vl)*1152+cg*16, rq=(w*128+ql)*1152+cg*16;
    v4u k0[4],k1[4],v0[4],v1[4],q0[4],q1[4],halo; unsigned skv;
    #define LOADKV(b) { gload(k0[b],p.k+(b)*BLK,rk); gload(k1[b],p.k+(b)*BLK+64*1152,rk); gload(v0[b],p.v+(b)*BLK,rv); gload(v1[b],p.v+(b)*BLK+64*1152,rv); }
    #define LOADQ(b) { gload(q0[b],p.q+(b)*BLK,rq); gload(q1[b],p.q+(b)*BLK+64*1152,rq); }
    const int ht=512*((lane>>2)&3)+128*w-4+(lane&3);
    gload(halo,p.k,imax(ht,0)*1152+cg*16);
    asm volatile("global_load_ushort %0, %1, %2":"=v"(skv):"v"(cg*64+(lane&7)*8+g*2),"s"(p.sk):"memory");
    LOADKV(0)
    const v4u dw=*(const __attribute__((address_space(4))) v4u*)(p.dv+cg*16);
    const float dd[8]={lo2f(dw.x),hi2f(dw.x),lo2f(dw.y),hi2f(dw.y),lo2f(dw.z),hi2f(dw.z),lo2f(dw.w),hi2f(dw.w)};
    unsigned sw=B_SCR+w*2112+lane*16, sh=B_SCR+w*2112+(lane&3)*16;
    unsigned ta=B_SCR+w*2112+(4+8*(i16>>2)+(lane&3)-g)*16;
    unsigned xw=(lane&7)*B_PITCH+(2040-1536-64-128*w-32*(i16>>3)-8*g)*2;
    unsigned ga=(i16>>2)*B_PITCH+B_HA+(2044-1536-64-128*w-16*g-4*(lane&3))*2, gx=ga+4*B_PITCH;
    char* outp=p.out+(size_t)(w*128+ql)*2304+1152+cg*16;
    asm volatile("":"+v"(sw),"+v"(sh),"+v"(ta),"+v"(xw),"+v"(ga),"+v"(gx),"+v"(outp));
    BC c;
    const int di=(lane&7)>>1;
    { const unsigned one=0x3f80u<<(16*(lane&1));
      const unsigned e0=(g==(i16>>3))?one:0u, e1=(g==(i16>>3)+2)?one:0u;
      c.i01=(v4u){di==0?e0:0u,di==1?e0:0u,di==2?e0:0u,di==3?e0:0u};
      c.i23=(v4u){di==0?e1:0u,di==1?e1:0u,di==2?e1:0u,di==3?e1:0u};
      asm volatile("":"+v"(c.i01),"+v"(c.i23)); }
    LOADKV(1)
    WAIT2(8,halo,skv);
    if(ht<0) halo=(v4u){0,0,0,0};
    { const unsigned sv=skv<<(16*(lane&1)), s0=(i16<8)?sv:0u, s1=(i16<8)?0u:sv;
      c.slo=(v4u){di==0?s0:0u,di==1?s0:0u,di==2?s0:0u,di==3?s0:0u};
      c.shi=(v4u){di==0?s1:0u,di==1?s1:0u,di==2?s1:0u,di==3?s1:0u}; }
    #define JOB(b,n) { WAIT4(n,k0[b],k1[b],v0[b],v1[b]); __builtin_amdgcn_s_setprio(2); st128(sw+64,k0[b]); st128(sw+1088,k1[b]); if((lane>>2)==(b)) st128(sh,halo); \
        b_prep(ta,xw+1024*(3-(b))+128,v0[b],c); b_prep(ta+1024,xw+1024*(3-(b)),v1[b],c); __builtin_amdgcn_s_setprio(0); }
    #define GATE(b) { b_gate(ga+1024*(3-(b))+128,gx+1024*(3-(b))+128,q0[b],dd,outp+(size_t)(b)*512*2304); b_gate(ga+1024*(3-(b)),gx+1024*(3-(b)),q1[b],dd,outp+(size_t)((b)*512+64)*2304); }
    STAMP(1);
    JOB(0,4)
    ldsbar();
    STAMP(2);
    LOADKV(2) LOADKV(3)
    JOB(1,8) JOB(2,4)
    STAMP(8);
    ldsbar();
    STAMP(3);
    LOADQ(0) LOADQ(1) LOADQ(2) LOADQ(3)
    JOB(3,8)
    WAIT2(6,q0[0],q1[0]);
    GATE(0)
    STAMP(9);
    ldsbar();
    STAMP(4);
    WAIT4(2,q0[1],q1[1],q0[2],q1[2]); WAIT2(2,q0[3],q1[3]);
    GATE(1) GATE(2)
    STAMP(5);
    ldsbar();
    STAMP(6);
    GATE(3)
    STAMP(7);
  }
}

constexpr int A_TM=90, A_SLOT=41984, A_WOFF=14336, A_IMG=83968, A_IROW=464, A_ZERO=136192, A_TAG=A_ZERO+288;
constexpr int WS_O=65536, WS_OM=19*1024, WS_OT=6*WS_OM;
typedef __amdgpu_buffer_rsrc_t rsrc_t;
DEV void dma16(const v4u& srd, unsigned voff, unsigned soff, unsigned lds){
  asm volatile("s_mov_b32 m0, %3\n\ts_nop 0\n\tbuffer_load_dwordx4 %0, %1, %2 offen lds"::"v"(voff),"s"(srd),"s"(soff),"s"(lds):"memory");
}
#define DW1 "s_add_u32 m0, m0, 3456\n\ts_add_u32 %0, %0, 27648\n\tbuffer_load_dwordx4 %1, %2, %0 offen lds\n\t"
DEV void dma_w8(const v4u& srd, unsigned voff, unsigned soff, unsigned lds){
  asm volatile("s_lshr_b64 exec, exec, 10\n\ts_mov_b32 m0, %3\n\ts_nop 0\n\tbuffer_load_dwordx4 %1, %2, %0 offen lds\n\t"
               DW1 DW1 DW1 DW1 DW1 DW1 DW1 "s_mov_b64 exec, -1"
               :"+s"(soff):"v"(voff),"s"(srd),"s"(lds):"memory","scc");
}
DEV v4u mksrd(const char* p, unsigned bytes){
  const u64 a=(u64)p; return (v4u){(unsigned)a,(unsigned)(a>>32)&0xffffu,bytes,0x00020000u};
}
struct AFr { v4u x[7]; v2u w[4][2]; };
struct AOp { v4u x[7], w[4]; };
template<int WO> DEV void a_rd(int i, AFr& f, unsigned xe, unsigned x0, unsigned xo, unsigned wa, unsigned we){
  switch(i){
    case 0: ATR(f.w[3][0],we,WO); break;      case 1: ATR(f.w[3][1],we,WO+432); break;
    case 2: ARD(f.x[0],xe,0); break;          case 3: ARD(f.x[1],xe,2048); break;   case 4: ARD(f.x[2],xe,4096); break;
    case 5: ATR(f.w[0][0],wa,WO); break;      case 6: ATR(f.w[0][1],wa,WO+432); break;
    case 7: ATR(f.w[1][0],wa,WO+32); break;   case 8: ATR(f.w[1][1],wa,WO+464); break;
    case 9: ATR(f.w[2][0],wa,WO+64); break;   case 10: ATR(f.w[2][1],wa,WO+496); break;
    case 11: ARD(f.x[3],x0,0); break;
    case 12: ARD(f.x[4],xo,0); break;         case 13: ARD(f.x[5],xo,2048); break;  case 14: ARD(f.x[6],xo,4096); break;
  }
}
DEV void a_wait(AFr& f, AOp& o){
  asm volatile("s_waitcnt lgkmcnt(0)":"+v"(f.x[0]),"+v"(f.x[1]),"+v"(f.x[2]),"+v"(f.x[3]),"+v"(f.x[4]),"+v"(f.x[5]),"+v"(f.x[6]),
     "+v"(f.w[0][0]),"+v"(f.w[0][1]),"+v"(f.w[1][0]),"+v"(f.w[1][1]),"+v"(f.w[2][0]),"+v"(f.w[2][1]),"+v"(f.w[3][0]),"+v"(f.w[3][1])::"memory");
  #pragma unroll
  for(int m=0;m<7;m++) o.x[m]=f.x[m];
  #pragma unroll
  for(int c=0;c<4;c++) o.w[c]=cat(f.w[c][0],f.w[c][1]);
}
DEV void a_mm(int i, f4 (&acc)[3][7], f4 (&ace)[3], const AOp& f){
  if(i<3) AMM(ace[i],f.w[3],f.x[i]); else AMM(acc[(i-3)%3][(i-3)/3],f.w[(i-3)%3],f.x[(i-3)/3]);
}
DEV float max3(float a,float b,float c){ float d; asm("v_max3_f32 %0, %1, %2, %3":"=v"(d):"v"(a),"v"(b),"v"(c)); return d; }
DEV float max2(float a,float b){ float d; asm("v_max_f32 %0, %1, %2":"=v"(d):"v"(a),"v"(b)); return d; }
DEV void tr4(unsigned (&a)[4]){
  auto r0=__builtin_amdgcn_permlane16_swap(a[0],a[1],false,false);
  auto r1=__builtin_amdgcn_permlane16_swap(a[2],a[3],false,false);
  auto s0=__builtin_amdgcn_permlane32_swap(r0[0],r1[0],false,false);
  auto s1=__builtin_amdgcn_permlane32_swap(r0[1],r1[1],false,false);
  a[0]=s0[0]; a[1]=s1[0]; a[2]=s0[1]; a[3]=s1[1];
}

DEV unsigned sum5(unsigned m, v4u v){ return (m+v.x+v.y)+v.z+v.w; }
DEV unsigned rs4(unsigned a, unsigned b, unsigned c, unsigned d){
  auto x=__builtin_amdgcn_permlane16_swap(a,b,false,false);
  auto y=__builtin_amdgcn_permlane16_swap(c,d,false,false);
  auto z=__builtin_amdgcn_permlane32_swap(x[0]+x[1],y[0]+y[1],false,false);
  return z[0]+z[1];
}
DEV void role_a(const Params& p, int rt, int grp STAMPARG){
  const int tid=__builtin_amdgcn_workitem_id_x(), lane=tid&63, wave=__builtin_amdgcn_readfirstlane(tid>>6);
  const int row0=rt*A_TM, nrow=imin(A_TM,2048-row0);
  const int i16=lane&15, g=lane>>4;
  GAS unsigned* epoch=(GAS unsigned*)(p.ws+rt*256);
  unsigned vl[5]={0,0,0,0,0};
  unsigned ka,kb,qb,va,sel0,sel1,sel2,sel3;
  const int par=i16&1; const bool cur0=(4*g+par<=i16), cur1=(4*g+par+2<=i16);
  #define A_CONSTS { ka=A_IMG+(16*wave+i16)*A_IROW+8*g; kb=(g==0)?ka+32:A_ZERO-144; qb=(g==0)?ka+32+16*A_IROW:A_ZERO; \
    va=A_IMG+(16*wave+4*g+(i16>>2))*A_IROW+288+(lane&3)*8; \
    const unsigned plo=par?0x01000c0cu:0x0c0c0100u, phi=par?0x03020c0cu:0x0c0c0302u, pzz=0x0c0c0c0cu; \
    sel0=cur0?pzz:plo; sel1=cur1?pzz:phi; sel2=cur0?plo:pzz; sel3=cur1?phi:pzz; \
    asm volatile("":"+v"(ka),"+v"(kb),"+v"(qb),"+v"(va),"+v"(sel0),"+v"(sel1),"+v"(sel2),"+v"(sel3)); }
  const rsrc_t wsr=__builtin_amdgcn_make_buffer_rsrc(p.ws+WS_O,0,23*WS_OT,0x00020000);
  if(wave>=4){
    const int lw=wave-4;
    const v4u sx=mksrd(p.x,2048*1152), sw=mksrd(p.wqkv,576*3456), so=mksrd(p.wo,576*1152);
    unsigned vx[4];
    #pragma unroll
    for(int i=0;i<4;i++){
      const int ir=8*(lw+4*i)+(lane>>3); int tok=row0-16+ir; tok=imax(tok,imax(row0-14,0)); tok=imin(tok,row0+nrow-1);
      vx[i]=tok*1152+(((lane&7)^((ir>>1)&7))<<4);
    }
    const int l27=lane>=27? lane-27:lane, seg=(l27>=18)?2:(l27>=9?1:0);
    const unsigned vw=(lane>=27?4*3456:0)+seg*(1152-144)+l27*16+grp*144+lw*3456;
    int st=0;
    unsigned sl=lw*1024;
    #define A_ISSUE() do{ \
      dma16(sx,vx[0],st*128,sl); dma16(sx,vx[1],st*128,sl+4096); dma16(sx,vx[2],st*128,sl+8192); if(lw<2) dma16(sx,vx[3],st*128,sl+12288); \
      dma_w8(sw,vw,st*64*3456,sl+A_WOFF-lw*160); \
      st=(st==8)?0:st+1; sl=(sl>=2*A_SLOT)?sl-2*A_SLOT:sl+A_SLOT; }while(0)
    A_ISSUE(); A_ISSUE();
    if(lw<2) WAITV(12); else WAITV(11);
    BAR;
    A_ISSUE();
    A_CONSTS
    unsigned vo[3];
    #pragma unroll
    for(int i=0;i<3;i++){
      const int ci=(i<2? lw+4*i : 8)*64+lane, rho=ci/9, cc=ci-rho*9;
      vo[i]=(32*(rho>>5)+16*(rho&1)+4*((rho>>3)&3)+((rho>>1)&3))*1152+grp*144+cc*16;
    }
    #pragma unroll
    for(int i=0;i<5;i++){
      const int ci=imin((lw&1)+2*i,8)*64+lane, rho=ci/9, cc=ci-rho*9;
      vl[i]=(72*(rho>>3)+64+4*(rho&1)+((rho>>1)&3))*1152+grp*144+cc*16;
    }
    asm volatile("":"+v"(vo[0]),"+v"(vo[1]),"+v"(vo[2]),"+v"(vl[0]),"+v"(vl[1]),"+v"(vl[2]),"+v"(vl[3]),"+v"(vl[4]));
    #define A_ISSUE_WO(P0,P1) do{ _Pragma("unroll 1") for(int P=(P0);P<(P1);P++){ \
        dma16(so,vo[0],P*72*1152,P*9216+lw*1024); dma16(so,vo[1],P*72*1152,P*9216+lw*1024+4096); \
        if((P&3)==lw) dma16(so,vo[2],P*72*1152,P*9216+8192); } }while(0)
    #pragma unroll 1
    for(int n=1;n<8;n++){
      if(lw<2) WAITV(12); else WAITV(11);
      BAR;
      if(n<7) A_ISSUE();
    }
    A_ISSUE_WO(0,3);
    if(lw<3) WAITV(7); else WAITV(6);
    BAR;
    A_ISSUE_WO(3,6);
    BAR;
    A_ISSUE_WO(6,8);
    STAMP(2);
  } else {
    unsigned ep;
    asm volatile("global_load_dword %0, %1, %2 sc1":"=v"(ep):"v"(0),"s"(epoch):"memory");
    A_CONSTS
    f4 acc[3][7], ace[3];
    #pragma unroll
    for(int c=0;c<3;c++){ ace[c]=(f4){0,0,0,0};
      #pragma unroll
      for(int m=0;m<7;m++) acc[c][m]=(f4){0,0,0,0}; }
    const int e0=(wave&1)?4:1, o0=(wave&1)?1:4;
    const unsigned xl=i16*128+((g^(i16>>1))<<4);
    const unsigned wl=A_WOFF+(8*g+2*(i16>>2))*432+(lane&3)*8;
    unsigned ae0=xl+e0*2048, az0=xl, ao0=xl+o0*2048, ae1=ae0^64, az1=az0^64, ao1=ao0^64, wa=wl+(2+3*wave)*32, we=wl+(wave>>1)*32;
    const unsigned ia=A_IMG+i16*A_IROW+g*8;
    unsigned ie=ia+e0*16*A_IROW+(2+3*wave)*32, iz=ia+(2+3*wave)*32, io=ia+o0*16*A_IROW+(2+3*wave)*32, ix=ia+e0*16*A_IROW+(wave>>1)*32;
    asm volatile("":"+v"(ae0),"+v"(az0),"+v"(ao0),"+v"(ae1),"+v"(az1),"+v"(ao1),"+v"(wa),"+v"(we),"+v"(ie),"+v"(iz),"+v"(io),"+v"(ix));
    #pragma unroll
    for(int c=0;c<3;c++){ asm volatile("":"+v"(ace[c]));
      #pragma unroll
      for(int m=0;m<7;m++) asm volatile("":"+v"(acc[c][m])); }
    if(tid<72) st32(A_ZERO+tid*4,0u);
    WAIT1(0,ep);
    if(tid==0) st32(A_TAG,ep*2891336453u+1013904223u);
    int slot=0;
    AFr f0,f1; AOp g0,g1;
    ldsbar();
    STAMP(1);
    #pragma unroll
    for(int i=0;i<15;i++) a_rd<0>(i,f0,ae0,az0,ao0,wa,we);
    a_wait(f0,g0);
    #pragma unroll 1
    for(int s=0;s<8;s++){
      const int dl=(slot==2)?-2*A_SLOT:A_SLOT; slot=(slot==2)?0:slot+1;
      #pragma unroll
      for(int i=0;i<24;i++){
        a_mm(i,acc,ace,g0);
        if(i<15) a_rd<13824>(i,f1,ae1,az1,ao1,wa,we);
        if(i==15) AADD(ae0,dl); if(i==16) AADD(az0,dl); if(i==17) AADD(ao0,dl); if(i==18) AADD(wa,dl); if(i==19) AADD(we,dl);
      }
      a_wait(f1,g1);
      BAR;
      #pragma unroll
      for(int i=0;i<24;i++){
        a_mm(i,acc,ace,g1);
        if(i<15) a_rd<0>(i,f0,ae0,az0,ao0,wa,we);
        if(i==15) AADD(ae1,dl); if(i==16) AADD(az1,dl); if(i==17) AADD(ao1,dl);
      }
      a_wait(f0,g0);
    }
    #pragma unroll
    for(int i=0;i<24;i++){
      a_mm(i,acc,ace,g0);
      if(i<15) a_rd<13824>(i,f1,ae1,az1,ao1,wa,we);
    }
    a_wait(f1,g1);
    BAR;
    SB;
    #pragma unroll
    for(int m=0;m<3;m++) ace[m]=mfma(g1.w[3],g1.x[m],ace[m]);
    #pragma unroll
    for(int m=0;m<7;m++){
      #pragma unroll
      for(int c=0;c<3;c++) acc[c][m]=mfma(g1.w[c],g1.x[m],acc[c][m]);
    }
    #pragma unroll
    for(int m=0;m<3;m++) st64(ix+m*16*A_IROW,(v2u){pk2(ace[m][0],ace[m][1]),pk2(ace[m][2],ace[m][3])});
    #pragma unroll
    for(int m=0;m<7;m++){
      const unsigned ib=(m<3)?ie+m*16*A_IROW:(m==3?iz:io+(m-4)*16*A_IROW);
      #pragma unroll
      for(int c=0;c<3;c++) st64v(ib+c*32,(v2u){pk2(acc[c][m][0],acc[c][m][1]),pk2(acc[c][m][2],acc[c][m][3])});
    }
    SGB(0x8,3);
    #pragma unroll
    for(int i=0;i<21;i++){ SGB(0x8,1); SGB(0x2,2); SGB(0x200,1); }
    SB;
    STAMP(2);
  }
  ldsbar();
  STAMP(3);
  const unsigned tag=ld32(A_TAG);
  if(wave>=6){
    const v4u so=mksrd(p.wo,576*1152);
    #pragma unroll
    for(int i=0;i<5;i++) if(i<4 || wave==6) dma16(so,vl[i],0,8*9216+(wave&1)*1024+i*2048);
    WAITV(0);
  } else {
    v4u qf[4],kp[4],kc[4],vf[5];
    #pragma unroll
    for(int h=0;h<4;h++){
      const int hb=36*h-4*(h&1);
      qf[h]=cat(ld64v(ka+16*A_IROW+hb),ld64v(qb+hb));
      kp[h]=cat(ld64v(ka+144+hb),ld64v(kb+144+hb));
      kc[h]=cat(ld64v(ka+16*A_IROW+144+hb),ld64v(qb+144+hb));
    }
    #pragma unroll
    for(int c=0;c<5;c++) vf[c]=cat(ldtr(va+32*c),ldtr(va+16*A_IROW+32*c));
    SB;
    f4 sp[4],sc[4];
    #pragma unroll
    for(int h=0;h<4;h++){
      v4u q=qf[h];
      if(h&1) q.x=(g==0)?0u:q.x; else q.w=0u;
      sp[h]=mfma(kp[h],q,(f4){0,0,0,0}); sc[h]=mfma(kc[h],q,(f4){0,0,0,0});
    }
    SB;
    STAMP(13);
    unsigned a0[4],a1[4];
    #pragma unroll
    for(int h=0;h<4;h++){
      const float c0=sc[h][0],c1=sc[h][1],c2=sc[h][2],c3=sc[h][3],p0=sp[h][0],p1=sp[h][1],p2=sp[h][2],p3=sp[h][3];
      const float ca=par?c1:c0, pa=par?p1:p0, cb=par?c3:c2, pb=par?p3:p2;
      a0[h]=__builtin_bit_cast(unsigned,cur0?ca:pa);
      a1[h]=__builtin_bit_cast(unsigned,cur1?cb:pb);
    }
    if(row0+16*wave==0){
      const unsigned mk=__builtin_bit_cast(unsigned,-1e9f/0.23570226039551584f);
      #pragma unroll
      for(int h=0;h<4;h++){ a0[h]=cur0?a0[h]:mk; a1[h]=cur1?a1[h]:mk; }
    }
    tr4(a0); tr4(a1);
    float l[8];
    #pragma unroll
    for(int i=0;i<4;i++){ l[2*i]=__builtin_bit_cast(float,a0[i]); l[2*i+1]=__builtin_bit_cast(float,a1[i]); }
    constexpr float SC=0.23570226039551584f*1.4426950408889634f;
    const float mx=-SC*max3(max3(l[0],l[1],l[2]),max3(l[3],l[4],l[5]),max2(l[6],l[7]));
    #pragma unroll
    for(int i=0;i<8;i++) l[i]=__builtin_amdgcn_exp2f(__builtin_fmaf(l[i],SC,mx));
    const float inv=__builtin_amdgcn_rcpf(((l[0]+l[1])+(l[2]+l[3]))+((l[4]+l[5])+(l[6]+l[7])));
    unsigned pp[4];
    #pragma unroll
    for(int i=0;i<4;i++) pp[i]=pk2(l[2*i]*inv,l[2*i+1]*inv);
    tr4(pp);
    v4u pf[4];
    #pragma unroll
    for(int h=0;h<4;h++) pf[h]=(v4u){__builtin_amdgcn_perm(0u,pp[h],sel0),__builtin_amdgcn_perm(0u,pp[h],sel1),__builtin_amdgcn_perm(0u,pp[h],sel2),__builtin_amdgcn_perm(0u,pp[h],sel3)};
    const f4 z={0,0,0,0};
    const f4 o0=mfma(vf[0],pf[0],z), a1_=mfma(vf[1],pf[0],z), b1=mfma(vf[1],pf[1],z), a2=mfma(vf[2],pf[1],z),
             b2_=mfma(vf[2],pf[2],z), a3=mfma(vf[3],pf[2],z), b3=mfma(vf[3],pf[3],z), o4=mfma(vf[4],pf[3],z);
    const v4u t01={pk2(o0[0],o0[1]),pk2(o0[2],o0[3]), (g==0)?pk2(a1_[0],a1_[1]):pk2(b1[0],b1[1]), pk2(b1[2],b1[3])};
    const v4u t23={(g==0)?pk2(a2[0],a2[1]):pk2(b2_[0],b2_[1]), (g==0)?pk2(a2[2],a2[3]):pk2(b2_[2],b2_[3]),
                   (g<2)?pk2(a3[0],a3[1]):pk2(b3[0],b3[1]), (g==0)?pk2(a3[2],a3[3]):pk2(b3[2],b3[3])};
    const unsigned u0=pk2(o4[0],o4[1]), u1=pk2(o4[2],o4[3]);
    auto r0=__builtin_amdgcn_permlane16_swap(u0,u0,false,false);
    auto r1=__builtin_amdgcn_permlane16_swap(u1,u1,false,false);
    STAMP(12);
    const unsigned ob=rt*WS_OT+wave*WS_OM;
    __builtin_amdgcn_raw_buffer_store_b128(t01,wsr,ob+2*grp*1024+lane*16,0,16);
    __builtin_amdgcn_raw_buffer_store_b128(t23,wsr,ob+2*grp*1024+1024+lane*16,0,16);
    const v4u t4={(unsigned)r0[0],(unsigned)r1[0],(unsigned)r0[1],(unsigned)r1[1]};
    unsigned hs=sum5(sum5(0,t01),t23);
    { auto x=__builtin_amdgcn_permlane16_swap(hs,hs,false,false); hs=x[0]+x[1];
      auto y=__builtin_amdgcn_permlane32_swap(hs,hs,false,false); hs=y[0]+y[1]; }
    if(g==0){
      __builtin_amdgcn_raw_buffer_store_b128(t4,wsr,ob+16*1024+grp*256+lane*16,0,16);
      __builtin_amdgcn_raw_buffer_store_b64((v2u){sum5(hs,t4)+tag,tag},wsr,ob+18*1024+grp*128+lane*8,0,16);
    }
    WAITV(4);
  }
  STAMP(4);
  BAR;
  STAMP(5);
  {
    const int m=(wave<4)?wave:4+((wave-4)>>1);
    const unsigned wa=(8*g+2*(i16>>2))*144+(lane&3)*8;
    const unsigned ob=rt*WS_OT+m*WS_OM+lane*16, rb=rt*WS_OT+m*WS_OM+18*1024+lane*8;
    const unsigned red=A_IMG+((wave>>1)&1)*5120+lane*16, rf=A_ZERO+272+((wave>>1)&1)*4;
    int l9=lane/9, c9=lane-l9*9;
    asm volatile("":"+v"(l9),"+v"(c9));
    f4 acc[5];
    #define OLD(ks) __builtin_amdgcn_raw_buffer_load_b128(wsr,ob,(ks)*1024,16)
    #define RLD(st) __builtin_amdgcn_raw_buffer_load_b64(wsr,rb,(st)*512,16)
    #define WFR(a,ks,c) cat(ldtr((a)+(ks)*4608+(c)*32),ldtr((a)+(ks)*4608+(c)*32+144))
    #define MEMF asm volatile("":::"memory")
    if(wave<4){
      constexpr int NPRE=PNPRE, NW=9, NISS=PNISS, NF=PNF;
      #pragma unroll 1
      for(int tries=0;tries<64;tries++){
        v4u wf[NPRE][5], of[18]; v2u r0,r1;
        #pragma unroll
        for(int k=0;k<NPRE;k++){
          #pragma unroll
          for(int c=0;c<5;c++) wf[k][c]=WFR(wa,k,c);
          if(k==NISS){ SB; MEMF; r0=RLD(0); r1=RLD(1); SB;
            #pragma unroll
            for(int j=0;j<NF;j++){ of[j]=OLD(j); SB; } }
        }
        SB;
        #pragma unroll 1
        for(int spin=0;spin<(1<<20);spin++){
          if(__builtin_amdgcn_ballot_w64(r0.y!=tag || r1.y!=tag)==0) break;
          MEMF; r0=RLD(0); r1=RLD(1); SB;
          #pragma unroll
          for(int j=0;j<NF;j++){ of[j]=OLD(j); SB; }
        }
        #pragma unroll
        for(int j=NF;j<NW;j++){ of[j]=OLD(j); SB; }
        STAMP(6);
        #pragma unroll
        for(int c=0;c<5;c++) acc[c]=(f4){0,0,0,0};
        unsigned hs[10];
        #pragma unroll
        for(int k=0;k<18;k++){
          #pragma unroll
          for(int c=0;c<5;c++){
            acc[c]=mfma(wf[k%NPRE][c],of[k],acc[c]);
            if(k+NPRE<18){ wf[k%NPRE][c]=WFR(wa,k+NPRE,c); SGB(0x8,1); SGB(0x100,2); }
          }
          if(k>=16) hs[k-8]=sum5(tag,of[k]); else hs[k>>1]=sum5((k&1)?hs[k>>1]:0u,of[k]);
          if(k==7) hs[0]=rs4(hs[0],hs[1],hs[2],hs[3]);
          if(k==15) hs[4]=rs4(hs[4],hs[5],hs[6],hs[7]);
          SB;
          if(k+NW<18){ of[k+NW]=OLD(k+NW); SB; }
        }
        if(__builtin_amdgcn_ballot_w64(hs[0]+hs[8]!=r0.x || hs[4]+hs[9]!=r1.x)==0) break;
      }
      STAMP(7);
    } else {
      const int hf=wave&1;
      const unsigned wb=wa+hf*8*4608, wc=wa+(16+hf)*4608;
      #pragma unroll 1
      for(int tries=0;tries<64;tries++){
        v4u wf[9][5], of[9]; v2u r0;
        #pragma unroll
        for(int k=0;k<8;k++){
          #pragma unroll
          for(int c=0;c<5;c++) wf[k][c]=WFR(wb,k,c);
          if(k==PNISS){ SB; MEMF; r0=RLD(hf); SB;
            #pragma unroll
            for(int j=0;j<PNF;j++){ of[j]=OLD(8*hf+j); SB; } }
        }
        #pragma unroll
        for(int c=0;c<5;c++) wf[8][c]=WFR(wc,0,c);
        SB;
        #pragma unroll 1
        for(int spin=0;spin<(1<<20);spin++){
          if(__builtin_amdgcn_ballot_w64(r0.y!=tag)==0) break;
          MEMF; r0=RLD(hf); SB;
          #pragma unroll
          for(int j=0;j<PNF;j++){ of[j]=OLD(8*hf+j); SB; }
        }
        #pragma unroll
        for(int j=PNF;j<9;j++){ of[j]=OLD(j<8?8*hf+j:16+hf); SB; }
        __builtin_amdgcn_s_setprio(1);
        STAMP(6);
        #pragma unroll
        for(int c=0;c<5;c++) acc[c]=(f4){0,0,0,0};
        unsigned hs[5];
        #pragma unroll
        for(int k=0;k<9;k++){
          #pragma unroll
          for(int c=0;c<5;c++) acc[c]=mfma(wf[k][c],of[k],acc[c]);
          hs[k>>1]=sum5((k==8)?tag:((k&1)?hs[k>>1]:0u),of[k]);
          SB;
        }
        if(__builtin_amdgcn_ballot_w64(rs4(hs[0],hs[1],hs[2],hs[3])+hs[4]!=r0.x)==0) break;
      }
      if(hf){
        #pragma unroll
        for(int c=0;c<5;c++) st128(red+c*1024,__builtin_bit_cast(v4u,acc[c]));
        WAITL;
        *(volatile LAS unsigned*)rf=1u;
      } else {
        for(int spin=0;spin<(1<<24);spin++) if(__builtin_amdgcn_readfirstlane(*(volatile LAS unsigned*)rf)) break;
        asm volatile("":::"memory");
        #pragma unroll
        for(int c=0;c<5;c++) acc[c]+=__builtin_bit_cast(f4,ld128(red+c*1024));
      }
      STAMP(7);
    }
    if(wave<4 || !(wave&1)){
      const unsigned tb=A_IMG+10240+wave*2560;
      #pragma unroll
      for(int c=0;c<5;c++) st64(tb+i16*160+g*8+c*32,(v2u){pk2(acc[c][0],acc[c][1]),pk2(acc[c][2],acc[c][3])});
      #pragma unroll
      for(int i=0;i<3;i++){
        const int r=7*i+l9;
        if(lane<63 && r<16 && m*16+r<nrow) *(GAS v4u*)(p.out+(size_t)(row0+m*16+r)*2304+grp*144+c9*16)=ld128(tb+r*160+c9*16);
      }
    }
    if(tid==0 && grp==0) __hip_atomic_store(epoch,tag,__ATOMIC_RELAXED,__HIP_MEMORY_SCOPE_AGENT);
  }
  STAMP(8);
}

NOPK __global__ __launch_bounds__(512,2) void fused(Params p){
  STAMPDECL;
  STAMP(0);
  const int b=__builtin_amdgcn_workgroup_id_x(), xcd=b&7, j=b>>3, pr=xcd>>1, half=xcd&1;
  const int nA=(pr<3)?24:20;
  if(j>=nA){
    const int jb=j-nA;
    role_b(p, jb<8? xcd*8+jb : 64+half*4+(jb-8) STAMPPASS);
  } else
    role_a(p, 6*pr+(j>>2), half*4+(j&3) STAMPPASS);
}

constexpr int LDS_BYTES=160*1024;
extern "C" void kernel_launch(void* const* d_in, const int* in_sizes, int n_in, void* d_out, int out_size, void* d_ws, size_t ws_size, hipStream_t stream){
  Params p;
  p.x=(const char*)d_in[0]; p.wqkv=(const char*)d_in[1]; p.wo=(const char*)d_in[2];
  p.q=(const char*)d_in[3]; p.k=(const char*)d_in[4]; p.v=(const char*)d_in[5];
  p.sk=(const char*)d_in[6]; p.hk=(const char*)d_in[7]; p.dv=(const char*)d_in[8];
  p.out=(char*)d_out; p.ws=(char*)d_ws;
  (void)hipFuncSetAttribute((const void*)fused,hipFuncAttributeMaxDynamicSharedMemorySize,LDS_BYTES);
  fused<<<256,512,LDS_BYTES,stream>>>(p);
}
```

```cpp
#include <hip/hip_runtime.h>
#include <hip/hip_bf16.h>

typedef unsigned short u16;
typedef unsigned long long u64;
typedef unsigned v4u __attribute__((ext_vector_type(4)));
typedef unsigned v2u __attribute__((ext_vector_type(2)));
typedef short s4 __attribute__((ext_vector_type(4)));
typedef __bf16 b2 __attribute__((ext_vector_type(2)));
typedef __bf16 b8 __attribute__((ext_vector_type(8)));
typedef float f4 __attribute__((ext_vector_type(4)));
typedef float f2 __attribute__((ext_vector_type(2)));
#define LAS __attribute__((address_space(3)))
#define GAS __attribute__((address_space(1)))
#if defined(__HIP_DEVICE_COMPILE__)
#define NOPK __attribute__((target("no-packed-fp32-ops")))
#else
#define NOPK
#endif
#define DEV __device__ __forceinline__ NOPK
#define SB __builtin_amdgcn_sched_barrier(0)
#define SGB(m,n) __builtin_amdgcn_sched_group_barrier(m,n,0)
#pragma clang diagnostic ignored "-Wint-to-pointer-cast"

#define STAMP(i)
#define STAMPDECL
#define STAMPARG
#define STAMPPASS
#define STAMPOUT

#define PNPRE 6
#define PNISS 3
#define PNF 4
#define ALOOK 2
#define NOAT
#define ROT 9
#define BV 1
#define CPFN 32
#define NTB " nt"
#define NTQ ""
#define NTX ""
#define NTW ""
#define NTO ""
#define BSTAMP(i)
#define NSTAMP(i) STAMP(i)
#define BSLP 16
struct Params {
  const char *x, *wqkv, *wo, *q, *k, *v, *sk, *hk, *dv;
  char *out, *ws;
};

DEV float lo2f(unsigned d){ return __builtin_bit_cast(float,d<<16); }
DEV float hi2f(unsigned d){ return __builtin_bit_cast(float,d&0xffff0000u); }
DEV unsigned pk2(float lo, float hi){ f2 p={lo,hi}; return __builtin_bit_cast(unsigned,__builtin_convertvector(p,b2)); }
DEV unsigned pk2a(float lo, float hi){ unsigned d; asm("v_cvt_pk_bf16_f32 %0, %1, %2":"=v"(d):"v"(lo),"v"(hi)); return d; }
DEV v4u ld128(unsigned a){ return *(LAS v4u*)a; }
DEV v2u ld64v(unsigned a){ return *(volatile LAS v2u*)a; }
DEV unsigned ld32(unsigned a){ return *(LAS unsigned*)a; }
DEV void st128(unsigned a, v4u v){ *(LAS v4u*)a=v; }
DEV void st64(unsigned a, v2u v){ *(LAS v2u*)a=v; }
DEV void st64v(unsigned a, v2u v){ *(volatile LAS v2u*)a=v; }
DEV void st32(unsigned a, unsigned v){ *(LAS unsigned*)a=v; }
DEV v2u ldtr(unsigned a){ return __builtin_bit_cast(v2u,__builtin_amdgcn_ds_read_tr16_b64_v4i16((LAS s4*)a)); }
DEV f4 mfma(v4u a, v4u b, f4 c){ return __builtin_amdgcn_mfma_f32_16x16x32_bf16(__builtin_bit_cast(b8,a),__builtin_bit_cast(b8,b),c,0,0,0); }
DEV v4u cat(v2u a, v2u b){ return (v4u){a.x,a.y,b.x,b.y}; }
DEV v4u gld128(const char* p){ return *(const GAS v4u*)p; }
DEV void gloadq(v4u& d, const char* sbase, unsigned voff){ asm volatile("global_load_dwordx4 %0, %1, %2" NTQ:"=v"(d):"v"(voff),"s"(sbase):"memory"); }
DEV void gload(v4u& d, const char* sbase, unsigned voff){ asm volatile("global_load_dwordx4 %0, %1, %2" NTB:"=v"(d):"v"(voff),"s"(sbase):"memory"); }
DEV void greload(v4u& d, const char* sbase, unsigned voff){
 asm volatile("global_load_dwordx4 %0, %1, %2":"+v"(d):"v"(voff),"s"(sbase):"memory"); }
#define WAIT1(n,a) asm volatile("s_waitcnt vmcnt(" #n ")":"+v"(a)::"memory")
#define WAIT2(n,a,b) asm volatile("s_waitcnt vmcnt(" #n ")":"+v"(a),"+v"(b)::"memory")
#define WAIT3(n,a,b,c) asm volatile("s_waitcnt vmcnt(" #n ")":"+v"(a),"+v"(b),"+v"(c)::"memory")
#define WAIT4(n,a,b,c,d) asm volatile("s_waitcnt vmcnt(" #n ")":"+v"(a),"+v"(b),"+v"(c),"+v"(d)::"memory")
#define WAITL asm volatile("":::"memory"); __builtin_amdgcn_s_waitcnt(0xC07F); asm volatile("":::"memory")
DEV void ldsbar(){ WAITL; __builtin_amdgcn_s_barrier(); asm volatile("":::"memory"); }
DEV int imin(int a,int b){ return a<b?a:b; }
DEV int imax(int a,int b){ return a>b?a:b; }
#define WAITV(n) asm volatile("s_waitcnt vmcnt(" #n ")":::"memory")
#define BAR do{ asm volatile("":::"memory"); __builtin_amdgcn_s_barrier(); asm volatile("":::"memory"); }while(0)
template<int NR,int NM> DEV void interleave(){
  constexpr int PER=(NR+NM-1)/NM, NG=(NR+PER-1)/PER;
  #pragma unroll
  for(int i=0;i<NG;i++){ SGB(0x8,1); SGB(0x100,PER); }
  if(NM>NG) SGB(0x8,NM-NG);
}

#define AMM(acc,a,b) asm volatile("v_mfma_f32_16x16x32_bf16 %0, %1, %2, %0":"+v"(acc):"v"(a),"v"(b))
#define ARD(d,a,off) asm volatile("ds_read_b128 %0, %1 offset:%2":"=v"(d):"v"(a),"i"(off):"memory")
#define ATR(d,a,off) asm volatile("ds_read_b64_tr_b16 %0, %1 offset:%2":"=v"(d):"v"(a),"i"(off):"memory")
#define AADD(v,s) asm volatile("v_add_u32 %0, %1, %0":"+v"(v):"s"(s))
#define AR64(d,a,off) asm volatile("ds_read_b64 %0, %1 offset:%2":"=v"(d):"v"(a),"i"(off):"memory")
constexpr int B_HA=448, B_HB=4576+448, B_X0=9120, B_X1=13344, B_ST=17520, B_PITCH=19696, B_SCR=8*B_PITCH;

template<int BLO,int NB> struct PF { v4u h[8-BLO]; v2u x[NB][2]; };
template<int BLO,int NB,int OFF> DEV void p_rd(int i, PF<BLO,NB>& f, unsigned hb, unsigned xb){
  #pragma unroll
  for(int a=0;a<8-BLO;a++) if(i==2+a) ARD(f.h[a],hb,512*a+OFF);
  #pragma unroll
  for(int b=0;b<NB;b++){ const int q=b?8-BLO+2*b:0; if(i==q) AR64(f.x[b][0],xb,512*(7-BLO-b)+OFF); if(i==q+1) AR64(f.x[b][1],xb,512*(7-BLO-b)+8+OFF); }
}
template<int BLO,int NB> DEV void p_mm(int i, f4 (&acc)[8], PF<BLO,NB>& f){
  int n=0;
  #pragma unroll
  for(int b=0;b<NB;b++){
    #pragma unroll
    for(int a=0;a<8-BLO-b;a++){ if(n==i){ v4u x=cat(f.x[b][0],f.x[b][1]); AMM(acc[a+b+BLO],f.h[a],x); } n++; }
  }
}
template<int N,int BLO,int NB> DEV void p_wait(PF<BLO,NB>& f){
  asm volatile("s_waitcnt lgkmcnt(%0)"::"i"(N):"memory");
  #pragma unroll
  for(int a=0;a<8-BLO;a++) asm volatile("":"+v"(f.h[a]));
  #pragma unroll
  for(int b=0;b<NB;b++) asm volatile("":"+v"(f.x[b][0]),"+v"(f.x[b][1]));
}
template<int BLO,int NB,int OFF> DEV void p_block(f4 (&acc)[8], PF<BLO,NB>& fc, PF<BLO,NB>& fn, unsigned hb, unsigned xb){
  constexpr int NR=8-BLO+2*NB, NM=NB*(8-BLO)-NB*(NB-1)/2, EX=(NR>NM)?NR-NM:0;
  constexpr int I2=(NB==2? 6-BLO : (NB-2)*(8-BLO)-(NB-2)*(NB-3)/2);
  p_wait<4>(fc);
  #pragma unroll
  for(int i=0;i<NM;i++){
    if(i==I2) asm volatile("s_waitcnt lgkmcnt(%0)"::"i"(I2+EX<NR?I2+EX:NR):"memory");
    p_mm<BLO,NB>(i,acc,fc);
    if(i<EX){ p_rd<BLO,NB,OFF>(2*i,fn,hb,xb); p_rd<BLO,NB,OFF>(2*i+1,fn,hb,xb); }
    else if(i+EX<NR) p_rd<BLO,NB,OFF>(i+EX,fn,hb,xb);
  }
}
template<int BLO,int NB> DEV void b_seg(f4 (&acc)[8], PF<BLO,NB>& f0, unsigned hb, unsigned xb, int next){
  PF<BLO,NB> f1;
  #pragma unroll 1
  for(int r=0;r<4;r++){
    p_block<BLO,NB,64>(acc,f0,f1,hb,xb);
    const int d=(r==3)? next-384 : 128;
    AADD(hb,d); AADD(xb,d);
    p_block<BLO,NB,0>(acc,f1,f0,hb,xb);
  }
}
template<int JLO,int NJ> DEV void b_zout(f4 (&acc)[8], unsigned za){
  asm volatile("s_nop 7\n\ts_nop 3":::"memory");
  #pragma unroll
  for(int J=JLO;J<JLO+NJ;J++){
    asm volatile("":"+v"(acc[J]));
    st32(za+512*(7-J)+64,pk2a(acc[J][1],acc[J][0])); st32(za+512*(7-J),pk2a(acc[J][3],acc[J][2]));
  }
}
template<int BLO,int NB> DEV void b_stage(f4 (&a0)[8], f4 (&a1)[8], unsigned hb, unsigned xb, unsigned za){
  PF<BLO,NB> f;
  #pragma unroll
  for(int i=2;i<10-BLO;i++) p_rd<BLO,NB,0>(i,f,hb,xb);
  ldsbar();
  p_rd<BLO,NB,0>(0,f,hb,xb); p_rd<BLO,NB,0>(1,f,hb,xb);
  #pragma unroll
  for(int i=10-BLO;i<8-BLO+2*NB;i++) p_rd<BLO,NB,0>(i,f,hb,xb);
  asm volatile("s_waitcnt lgkmcnt(%0)"::"i"(2*NB-2):"memory");
  b_seg<BLO,NB>(a0,f,hb,xb,B_PITCH);
  b_zout<BLO,NB>(a0,za);
  b_seg<BLO,NB>(a1,f,hb+B_PITCH,xb+B_PITCH,0);
  b_zout<BLO,NB>(a1,za+B_PITCH);
  p_wait<0>(f);
}

DEV unsigned sum5(unsigned m, v4u v){ return (m+v.x+v.y)+v.z+v.w; }
DEV void b_prep2(unsigned ta, unsigned tv, v4u sl0, v4u sh0, v4u sl1, v4u sh1, v4u i01, v4u i23, v4u& oa, v4u& ob){
  v4u a[2][4], vr[2];
  #pragma unroll
  for(int t=0;t<2;t++){ vr[t]=ld128(tv+t*B_PITCH); a[t][0]=ld128(ta+t*B_PITCH); a[t][2]=ld128(ta+t*B_PITCH+64); }
  #pragma unroll
  for(int t=0;t<2;t++){ a[t][1]=ld128(ta+t*B_PITCH+512); a[t][3]=ld128(ta+t*B_PITCH+576); }
  SB;
  const f4 z={0,0,0,0};
  f4 v1[2],v2[2],d1[2],d2[2];
  #pragma unroll
  for(int t=0;t<2;t++){ v1[t]=mfma(vr[t],i01,z); v2[t]=mfma(vr[t],i23,z); d1[t]=mfma(a[t][0],t?sl1:sl0,z); d2[t]=mfma(a[t][2],t?sl1:sl0,z); }
  #pragma unroll
  for(int t=0;t<2;t++){ d1[t]=mfma(a[t][1],t?sh1:sh0,d1[t]); d2[t]=mfma(a[t][3],t?sh1:sh0,d2[t]); }
  SB;
  #pragma unroll
  for(int t=0;t<2;t++){
    const v4u o={pk2(d2[t][3]*v2[t][3],d2[t][2]*v2[t][2]),pk2(d2[t][1]*v2[t][1],d2[t][0]*v2[t][0]),pk2(d1[t][3]*v1[t][3],d1[t][2]*v1[t][2]),pk2(d1[t][1]*v1[t][1],d1[t][0]*v1[t][0])};
    if(t) ob=o; else oa=o;
  }
}
DEV void b_put(unsigned a, v4u o){
  st128(a+B_X0,o);
  st32(a+B_X1+4,o.x); st64(a+B_X1+8,(v2u){o.y,o.z}); st32(a+B_X1+16,o.w);
}
DEV v4u b_diag(int di, unsigned e){ return (v4u){di==0?e:0u,di==1?e:0u,di==2?e:0u,di==3?e:0u}; }
constexpr int WS_B=4<<20, WS_BT=1280;
DEV void b_gate(unsigned ga, unsigned gb, v4u q, const float (&dd)[8], char* outp){
  const v2u y0=ldtr(ga), x0=ldtr(ga+B_X0-B_HA), y1=ldtr(gb), x1=ldtr(gb+B_X0-B_HA);
  #define G2(Q,Y,X,i) pk2(lo2f(Q)*__builtin_fmaf(lo2f(X),dd[i],lo2f(Y)), hi2f(Q)*__builtin_fmaf(hi2f(X),dd[i+1],hi2f(Y)))
  const v4u o={G2(q.x,y0.x,x0.x,0),G2(q.y,y0.y,x0.y,2),G2(q.z,y1.x,x1.x,4),G2(q.w,y1.y,x1.y,6)};
  *(GAS v4u*)outp=o;
}

DEV void role_b(const Params& p, int cg STAMPARG){
  const int tid=__builtin_amdgcn_workitem_id_x(), lane=tid&63, wave=__builtin_amdgcn_readfirstlane(tid>>6);
  const int i16=lane&15, g=lane>>4;
  constexpr unsigned BLK=512*1152;
  const int w=wave&3, ql=lane^3;
  const unsigned rq=(w*128+ql)*1152+cg*16;
  unsigned ga,gx; char* outp; float dd[8];
  #define GCONST { const v4u dw=*(const __attribute__((address_space(4))) v4u*)(p.dv+cg*16); \
    dd[0]=lo2f(dw.x); dd[1]=hi2f(dw.x); dd[2]=lo2f(dw.y); dd[3]=hi2f(dw.y); dd[4]=lo2f(dw.z); dd[5]=hi2f(dw.z); dd[6]=lo2f(dw.w); dd[7]=hi2f(dw.w); \
    ga=(i16>>2)*B_PITCH+B_HA+(2044-1536-64-128*w-16*g-4*(lane&3))*2; gx=ga+4*B_PITCH; outp=p.out+(size_t)(w*128+ql)*2304+1152+cg*16; \
    asm volatile("":"+v"(ga),"+v"(gx),"+v"(outp)); }
  #define GATE(b,Q0,Q1) { b_gate(ga+1024*(3-(b))+128,gx+1024*(3-(b))+128,Q0,dd,outp+(size_t)(b)*512*2304); b_gate(ga+1024*(3-(b)),gx+1024*(3-(b)),Q1,dd,outp+(size_t)((b)*512+64)*2304); }
  if(wave<4){
    v4u hr[8];
    BAR;
    #pragma unroll
    for(int i=0;i<8;i++) gload(hr[i],p.hk,(cg*8+2*wave)*4096+i*1024+lane*16);
    const unsigned cb=2*wave*B_PITCH;
    #pragma unroll
    for(int c=0;c<2;c++){
      if(lane<56) st128(cb+c*B_PITCH+(lane<28? lane*16 : 4576+(lane-28)*16),(v4u){0,0,0,0});
      if(lane<31) st32(cb+c*B_PITCH+(lane<16? B_X0+4096+lane*4 : B_X1+4100+(lane-16)*4),0u);
    }
    unsigned hb=cb+((i16&1)?B_HA:B_HB)+(32*(i16>>1)+8*g)*2-448;
    unsigned xb=cb+((i16&1)? B_X0+(2046-2*i16+8*g)*2 : B_X1+(2048-2*i16+8*g)*2)-512*7-448;
    unsigned za=cb+B_HA+(2046-64*g-2*i16)*2-64-512*7;
    asm volatile("":"+v"(hb),"+v"(xb),"+v"(za));
    GCONST
    f4 a0[8],a1[8];
    #pragma unroll
    for(int j=0;j<8;j++){ a0[j]=(f4){0,0,0,0}; a1[j]=(f4){0,0,0,0}; asm volatile("":"+v"(a0[j]),"+v"(a1[j])); }
    #pragma unroll
    for(int c=0;c<2;c++){
      if(c==0) WAIT4(4,hr[0],hr[1],hr[2],hr[3]); else WAIT4(0,hr[4],hr[5],hr[6],hr[7]);
      #pragma unroll
      for(int i=0;i<4;i++){
        const v4u h=hr[4*c+i];
        const unsigned first=i? __builtin_amdgcn_readlane(hr[4*c+i-1].w,63) : 0u;
        const unsigned pv=__builtin_amdgcn_update_dpp(first,h.w,0x138,0xf,0xf,false);
        const v4u s={__builtin_amdgcn_alignbit(h.x,pv,16),__builtin_amdgcn_alignbit(h.y,h.x,16),__builtin_amdgcn_alignbit(h.z,h.y,16),__builtin_amdgcn_alignbit(h.w,h.z,16)};
        st128(cb+c*B_PITCH+B_HA+i*1024+lane*16,h);
        st128(cb+c*B_PITCH+B_HB+i*1024+lane*16,s);
      }
    }
    STAMP(1);
    __builtin_amdgcn_s_setprio(1);
    b_stage<0,2>(a0,a1,hb,xb,za);
    STAMP(3);
    b_stage<2,2>(a0,a1,hb,xb,za);
    STAMP(4);
    v4u qa,qb;
    gloadq(qa,p.q+3*BLK,rq); gloadq(qb,p.q+3*BLK+64*1152,rq);
    b_stage<4,4>(a0,a1,hb,xb,za);
    STAMP(5);
    WAIT2(0,qa,qb);
    ldsbar();
    GATE(3,qa,qb)
    STAMP(7);
  } else {
    const int j=cg&7, cp=(cg&~7)+2*w;
    const unsigned rk=(j*64+w*8+(lane>>3))*1152+(cg>>3)*128+(lane&7)*16;
    v4u k0[4],k1[4],v0[4],v1[4],q0[3],q1[3],halo; unsigned sk0,sk1,ep;
    #define LOADKV(b) { gload(k0[b],p.k+(b)*BLK,rk); gload(k1[b],p.k+(b)*BLK+32*1152,rk); gload(v0[b],p.v+(b)*BLK,rk); gload(v1[b],p.v+(b)*BLK+32*1152,rk); }
    #define LOADQ(b) { gloadq(q0[b],p.q+(b)*BLK,rq); gloadq(q1[b],p.q+(b)*BLK+64*1152,rq); }
    unsigned junk=0;
    #define TOUCH(P,O) asm volatile("global_load_dword %0, %1, %2":"+v"(junk):"v"(O),"s"(P):"memory")
    asm volatile("global_load_ushort %0, %2, %3\n\tglobal_load_ushort %1, %2, %3 offset:64":"=&v"(sk0),"=&v"(sk1):"v"(cp*64+(lane&7)*8+g*2),"s"(p.sk):"memory");
    GAS unsigned* epoch=(GAS unsigned*)(p.ws+(32+(cg>>3))*256);
    asm volatile("global_load_dword %0, %1, %2 sc1":"=v"(ep):"v"(0),"s"(epoch):"memory");
    LOADKV(0)
    const int ht=512*((lane>>3)&3)+64*j-4+w;
    gload(halo,p.k,imax(ht,0)*1152+(cg>>3)*128+(lane&7)*16);
    const unsigned po=(cp*32+j)*WS_BT, co=(cg*32+2*w)*WS_BT;
    st32(B_SCR+4*w,0u);
    ldsbar();
    TOUCH(p.ws+WS_B,co+imin(lane,19)*128);
    LOADKV(1)
    STAMP(10);
    GCONST
    #define KO(sl) ((sl)?B_ST+64:B_X1)
    #define VO(sl) ((sl)?B_ST+1088:B_X0)
    unsigned sw=(lane&7)*B_PITCH+(w*8+(lane>>3))*16, sh=(lane&7)*B_PITCH+w*16;
    unsigned ta=2*w*B_PITCH+(4+8*(i16>>2)+(lane&3)-g)*16, tv=2*w*B_PITCH+(32*(g&1)+8*(i16>>2)+4*(g>>1)+(lane&3))*16;
    unsigned xw=(lane&7)*B_PITCH+(2040-1536-64-128*w-32*(i16>>3)-8*g)*2;
    const int di=(lane&7)>>1;
    const unsigned one=0x3f80u<<(16*(lane&1));
    v4u i01=b_diag(di,(g==(i16>>3))?one:0u), i23=b_diag(di,(g==(i16>>3)+2)?one:0u);
    asm volatile("":"+v"(sw),"+v"(sh),"+v"(ta),"+v"(tv),"+v"(xw),"+v"(i01),"+v"(i23));
    const __amdgpu_buffer_rsrc_t xr=__builtin_amdgcn_make_buffer_rsrc(p.ws+WS_B,0,72*32*WS_BT,0x00020000);
    WAIT3(10,sk0,sk1,ep);
    unsigned tag=ep*2891336453u+1013904223u;
    sk0<<=16*(lane&1); sk1<<=16*(lane&1);
    v4u sl0=b_diag(di,(i16<8)?sk0:0u), sh0=b_diag(di,(i16<8)?0u:sk0), sl1=b_diag(di,(i16<8)?sk1:0u), sh1=b_diag(di,(i16<8)?0u:sk1);
    asm volatile("":"+v"(tag),"+v"(sl0),"+v"(sh0),"+v"(sl1),"+v"(sh1));
    WAIT1(5,halo);
    STAMP(11);
    if(ht<0) halo=(v4u){0,0,0,0};
    #define STAGE(b,sl,n) { WAIT4(n,k0[b],k1[b],v0[b],v1[b]); \
        st128(sw+KO(sl),k0[b]); st128(sw+KO(sl)+512,k1[b]); st128(sw+VO(sl),v0[b]); st128(sw+VO(sl)+512,v1[b]); \
        if((lane>>3)==(b)) st128(sh+KO(sl)-64,halo); }
    #define SYNC(n) { WAITL; *(volatile LAS unsigned*)(B_SCR+4*w)=(n); \
        _Pragma("unroll 1") for(int spin=0;spin<(1<<24);spin++){ if(__builtin_amdgcn_ballot_w64(*(volatile LAS unsigned*)(B_SCR+4*(lane&3))<(n))==0) break; __builtin_amdgcn_s_sleep(1); } asm volatile("":::"memory"); }
    #define PROD(b,sl) { __builtin_amdgcn_s_setprio(2); \
        v4u oa,ob; b_prep2(ta+KO(sl)-64,tv+VO(sl),sl0,sh0,sl1,sh1,i01,i23,oa,ob); __builtin_amdgcn_s_setprio(0); \
        __builtin_amdgcn_raw_buffer_store_b128(oa,xr,lane*16+po+(b)*8*WS_BT,0,16); __builtin_amdgcn_raw_buffer_store_b32(sum5(tag,oa),xr,1024+lane*4,po+(b)*8*WS_BT,16); \
        __builtin_amdgcn_raw_buffer_store_b128(ob,xr,lane*16+po+(32+(b)*8)*WS_BT,0,16); __builtin_amdgcn_raw_buffer_store_b32(sum5(tag,ob),xr,1024+lane*4,po+(32+(b)*8)*WS_BT,16); }
    #define RECV(b) { v4u oa,ob; _Pragma("unroll 1") for(int spin=0;spin<(1<<20);spin++){ asm volatile("":::"memory"); \
          oa=__builtin_amdgcn_raw_buffer_load_b128(xr,lane*16,co+(b)*8*WS_BT,16); ob=__builtin_amdgcn_raw_buffer_load_b128(xr,lane*16+WS_BT,co+(b)*8*WS_BT,16); \
          const unsigned ca=__builtin_amdgcn_raw_buffer_load_b32(xr,1024+lane*4,co+(b)*8*WS_BT,16), cb=__builtin_amdgcn_raw_buffer_load_b32(xr,1024+lane*4+WS_BT,co+(b)*8*WS_BT,16); \
          if(__builtin_amdgcn_ballot_w64(sum5(tag,oa)!=ca || sum5(tag,ob)!=cb)==0) break; __builtin_amdgcn_s_sleep(4); } \
        b_put(xw+1024*(3-(b))+128,oa); b_put(xw+1024*(3-(b)),ob); }
    STAGE(0,1,5)
    SYNC(1)
    PROD(0,1)
    STAMP(12);
    __builtin_amdgcn_s_sleep(BSLP);
    RECV(0)
    STAMP(1);
    ldsbar();
    STAMP(2);
    LOADKV(2) LOADKV(3)
    STAGE(1,0,8)
    SYNC(2)
    PROD(1,0)
    SYNC(3)
    STAGE(2,1,8)
    STAGE(3,0,4)
    RECV(1)
    STAMP(13);
    TOUCH(p.q+w*BLK,(lane*8+j)*1152+cg*16);
    SYNC(4)
    PROD(2,1)
    STAMP(8);
    ldsbar();
    STAMP(3);
    PROD(3,0)
    LOADQ(0) LOADQ(1)
    SYNC(5)
    RECV(2) RECV(3)
    asm volatile(""::"v"(junk));
    LOADQ(2)
    STAMP(9);
    ldsbar();
    STAMP(4);
    if(tid==256 && j==0) __hip_atomic_store(epoch,tag,__ATOMIC_RELAXED,__HIP_MEMORY_SCOPE_AGENT);
    WAIT4(2,q0[0],q1[0],q0[1],q1[1]);
    GATE(0,q0[0],q1[0]) GATE(1,q0[1],q1[1])
    STAMP(5);
    WAIT2(4,q0[2],q1[2]);
    ldsbar();
    STAMP(6);
    GATE(2,q0[2],q1[2])
    STAMP(7);
  }
}

constexpr int A_TM=90, A_SLOT=41984, A_WOFF=14336, A_IMG=83968, A_IROW=464, A_ZERO=136192, A_TAG=A_ZERO+288;
constexpr int WS_O=65536, WS_OM=19*1024, WS_OT=6*WS_OM;
typedef __amdgpu_buffer_rsrc_t rsrc_t;
DEV void dma16o(const v4u& srd, unsigned voff, unsigned soff, unsigned lds){
  asm volatile("s_mov_b32 m0, %3\n\ts_nop 0\n\tbuffer_load_dwordx4 %0, %1, %2 offen" NTO " lds"::"v"(voff),"s"(srd),"s"(soff),"s"(lds):"memory");
}
DEV void dma16(const v4u& srd, unsigned voff, unsigned soff, unsigned lds){
  asm volatile("s_mov_b32 m0, %3\n\ts_nop 0\n\tbuffer_load_dwordx4 %0, %1, %2 offen" NTX " lds"::"v"(voff),"s"(srd),"s"(soff),"s"(lds):"memory");
}
#define DW1 "s_add_u32 m0, m0, 3456\n\ts_add_u32 %0, %0, 27648\n\tbuffer_load_dwordx4 %1, %2, %0 offen" NTW " lds\n\t"
DEV void dma_w8(const v4u& srd, unsigned voff, unsigned soff, unsigned lds){
  asm volatile("s_lshr_b64 exec, exec, 10\n\ts_mov_b32 m0, %3\n\ts_nop 0\n\tbuffer_load_dwordx4 %1, %2, %0 offen" NTW " lds\n\t"
               DW1 DW1 DW1 DW1 DW1 DW1 DW1 "s_mov_b64 exec, -1"
               :"+s"(soff):"v"(voff),"s"(srd),"s"(lds):"memory","scc");
}
DEV v4u mksrd(const char* p, unsigned bytes){
  const u64 a=(u64)p; return (v4u){(unsigned)a,(unsigned)(a>>32)&0xffffu,bytes,0x00020000u};
}
struct AFr { v4u x[7]; v2u w[4][2]; };
struct AOp { v4u x[7], w[4]; };
template<int WO> DEV void a_rd(int i, AFr& f, unsigned xe, unsigned x0, unsigned xo, unsigned wa, unsigned we){
  switch(i){
    case 0: ATR(f.w[3][0],we,WO); break;      case 1: ATR(f.w[3][1],we,WO+432); break;
    case 2: ARD(f.x[0],xe,0); break;          case 3: ARD(f.x[1],xe,2048); break;   case 4: ARD(f.x[2],xe,4096); break;
    case 5: ATR(f.w[0][0],wa,WO); break;      case 6: ATR(f.w[0][1],wa,WO+432); break;
    case 7: ATR(f.w[1][0],wa,WO+32); break;   case 8: ATR(f.w[1][1],wa,WO+464); break;
    case 9: ATR(f.w[2][0],wa,WO+64); break;   case 10: ATR(f.w[2][1],wa,WO+496); break;
    case 11: ARD(f.x[3],x0,0); break;
    case 12: ARD(f.x[4],xo,0); break;         case 13: ARD(f.x[5],xo,2048); break;  case 14: ARD(f.x[6],xo,4096); break;
  }
}
DEV void a_wait(AFr& f, AOp& o){
  asm volatile("s_waitcnt lgkmcnt(0)":"+v"(f.x[0]),"+v"(f.x[1]),"+v"(f.x[2]),"+v"(f.x[3]),"+v"(f.x[4]),"+v"(f.x[5]),"+v"(f.x[6]),
     "+v"(f.w[0][0]),"+v"(f.w[0][1]),"+v"(f.w[1][0]),"+v"(f.w[1][1]),"+v"(f.w[2][0]),"+v"(f.w[2][1]),"+v"(f.w[3][0]),"+v"(f.w[3][1])::"memory");
  #pragma unroll
  for(int m=0;m<7;m++) o.x[m]=f.x[m];
  #pragma unroll
  for(int c=0;c<4;c++) o.w[c]=cat(f.w[c][0],f.w[c][1]);
}
DEV void a_mm(int i, f4 (&acc)[3][7], f4 (&ace)[3], const AOp& f){
  if(i<3) AMM(ace[i],f.w[3],f.x[i]); else AMM(acc[(i-3)%3][(i-3)/3],f.w[(i-3)%3],f.x[(i-3)/3]);
}
DEV float max3(float a,float b,float c){ float d; asm("v_max3_f32 %0, %1, %2, %3":"=v"(d):"v"(a),"v"(b),"v"(c)); return d; }
DEV float max2(float a,float b){ float d; asm("v_max_f32 %0, %1, %2":"=v"(d):"v"(a),"v"(b)); return d; }
DEV void tr4(unsigned (&a)[4]){
  auto r0=__builtin_amdgcn_permlane16_swap(a[0],a[1],false,false);
  auto r1=__builtin_amdgcn_permlane16_swap(a[2],a[3],false,false);
  auto s0=__builtin_amdgcn_permlane32_swap(r0[0],r1[0],false,false);
  auto s1=__builtin_amdgcn_permlane32_swap(r0[1],r1[1],false,false);
  a[0]=s0[0]; a[1]=s1[0]; a[2]=s0[1]; a[3]=s1[1];
}

DEV unsigned rs4(unsigned a, unsigned b, unsigned c, unsigned d){
  auto x=__builtin_amdgcn_permlane16_swap(a,b,false,false);
  auto y=__builtin_amdgcn_permlane16_swap(c,d,false,false);
  auto z=__builtin_amdgcn_permlane32_swap(x[0]+x[1],y[0]+y[1],false,false);
  return z[0]+z[1];
}
DEV void role_a(const Params& p, int rt, int grp, int rot STAMPARG){
  const int tid=__builtin_amdgcn_workitem_id_x(), lane=tid&63, wave=__builtin_amdgcn_readfirstlane(tid>>6);
  const int row0=rt*A_TM, nrow=imin(A_TM,2048-row0);
  const int i16=lane&15, g=lane>>4;
  GAS unsigned* epoch=(GAS unsigned*)(p.ws+rt*256);
  unsigned vl[5]={0,0,0,0,0};
  unsigned ka,kb,qb,va,sel0,sel1,sel2,sel3;
  const int par=i16&1; const bool cur0=(4*g+par<=i16), cur1=(4*g+par+2<=i16);
  #define A_CONSTS { ka=A_IMG+(16*wave+i16)*A_IROW+8*g; kb=(g==0)?ka+32:A_ZERO-144; qb=(g==0)?ka+32+16*A_IROW:A_ZERO; \
    va=A_IMG+(16*wave+4*g+(i16>>2))*A_IROW+288+(lane&3)*8; \
    const unsigned plo=par?0x01000c0cu:0x0c0c0100u, phi=par?0x03020c0cu:0x0c0c0302u, pzz=0x0c0c0c0cu; \
    sel0=cur0?pzz:plo; sel1=cur1?pzz:phi; sel2=cur0?plo:pzz; sel3=cur1?phi:pzz; \
    asm volatile("":"+v"(ka),"+v"(kb),"+v"(qb),"+v"(va),"+v"(sel0),"+v"(sel1),"+v"(sel2),"+v"(sel3)); }
  const rsrc_t wsr=__builtin_amdgcn_make_buffer_rsrc(p.ws+WS_O,0,23*WS_OT,0x00020000);
  if(wave>=4){
    const int lw=wave-4;
    const v4u sx=mksrd(p.x,2048*1152), sw=mksrd(p.wqkv,576*3456), so=mksrd(p.wo,576*1152);
    int st=rot;
    unsigned sl=lw*1024;
    const int l27=lane>=27? lane-27:lane, seg=(l27>=18)?2:(l27>=9?1:0);
    const unsigned vw=(lane>=27?4*3456:0)+seg*(1152-144)+l27*16+grp*144+lw*3456;
    dma_w8(sw,vw,st*64*3456,sl+A_WOFF-lw*160);
    unsigned vx[4];
    #pragma unroll
    for(int i=0;i<4;i++){
      const int ir=8*(lw+4*i)+(lane>>3); int tok=row0-16+ir; tok=imax(tok,imax(row0-14,0)); tok=imin(tok,row0+nrow-1);
      vx[i]=tok*1152+(((lane&7)^((ir>>1)&7))<<4);
    }
    #define A_ISSUEX() do{ dma16(sx,vx[0],st*128,sl); dma16(sx,vx[1],st*128,sl+4096); dma16(sx,vx[2],st*128,sl+8192); if(lw<2) dma16(sx,vx[3],st*128,sl+12288); \
      st=(st==8)?0:st+1; sl=(sl>=2*A_SLOT)?sl-2*A_SLOT:sl+A_SLOT; }while(0)
    #define A_ISSUE() do{ dma_w8(sw,vw,st*64*3456,sl+A_WOFF-lw*160); A_ISSUEX(); }while(0)
    A_ISSUEX();
    STAMP(10);
    A_ISSUE();
    STAMP(11);
    if(lw<2) WAITV(12); else WAITV(11);
    BAR;
    A_ISSUE();
    A_CONSTS
    unsigned vo[3];
    #pragma unroll
    for(int i=0;i<3;i++){
      const int ci=(i<2? lw+4*i : 8)*64+lane, rho=ci/9, cc=ci-rho*9;
      vo[i]=(32*(rho>>5)+16*(rho&1)+4*((rho>>3)&3)+((rho>>1)&3))*1152+grp*144+cc*16;
    }
    #pragma unroll
    for(int i=0;i<5;i++){
      const int ci=imin((lw&1)+2*i,8)*64+lane, rho=ci/9, cc=ci-rho*9;
      vl[i]=(72*(rho>>3)+64+4*(rho&1)+((rho>>1)&3))*1152+grp*144+cc*16;
    }
    asm volatile("":"+v"(vo[0]),"+v"(vo[1]),"+v"(vo[2]),"+v"(vl[0]),"+v"(vl[1]),"+v"(vl[2]),"+v"(vl[3]),"+v"(vl[4]));
    #define A_ISSUE_WO(P0,P1) do{ _Pragma("unroll 1") for(int P=(P0);P<(P1);P++){ \
        dma16o(so,vo[0],P*72*1152,P*9216+lw*1024); dma16o(so,vo[1],P*72*1152,P*9216+lw*1024+4096); \
        if((P&3)==lw) dma16o(so,vo[2],P*72*1152,P*9216+8192); } }while(0)
    #pragma unroll 1
    for(int n=1;n<8;n++){
      if(lw<2) WAITV(12); else WAITV(11);
      BAR;
      if(n<7) A_ISSUE();
    }
    A_ISSUE_WO(0,3);
    if(lw<3) WAITV(7); else WAITV(6);
    BAR;
    A_ISSUE_WO(3,6);
    BAR;
    A_ISSUE_WO(6,8);
    STAMP(2);
  } else {
    unsigned ep;
    asm volatile("global_load_dword %0, %1, %2 sc1":"=v"(ep):"v"(0),"s"(epoch):"memory");
    A_CONSTS
    f4 acc[3][7], ace[3];
    #pragma unroll
    for(int c=0;c<3;c++){ ace[c]=(f4){0,0,0,0};
      #pragma unroll
      for(int m=0;m<7;m++) acc[c][m]=(f4){0,0,0,0}; }
    const int e0=(wave&1)?4:1, o0=(wave&1)?1:4;
    const unsigned xl=i16*128+((g^(i16>>1))<<4);
    const unsigned wl=A_WOFF+(8*g+2*(i16>>2))*432+(lane&3)*8;
    unsigned ae0=xl+e0*2048, az0=xl, ao0=xl+o0*2048, ae1=ae0^64, az1=az0^64, ao1=ao0^64, wa=wl+(2+3*wave)*32, we=wl+(wave>>1)*32;
    const unsigned ia=A_IMG+i16*A_IROW+g*8;
    unsigned ie=ia+e0*16*A_IROW+(2+3*wave)*32, iz=ia+(2+3*wave)*32, io=ia+o0*16*A_IROW+(2+3*wave)*32, ix=ia+e0*16*A_IROW+(wave>>1)*32;
    asm volatile("":"+v"(ae0),"+v"(az0),"+v"(ao0),"+v"(ae1),"+v"(az1),"+v"(ao1),"+v"(wa),"+v"(we),"+v"(ie),"+v"(iz),"+v"(io),"+v"(ix));
    #pragma unroll
    for(int c=0;c<3;c++){ asm volatile("":"+v"(ace[c]));
      #pragma unroll
      for(int m=0;m<7;m++) asm volatile("":"+v"(acc[c][m])); }
    if(tid<72) st32(A_ZERO+tid*4,0u);
    WAIT1(0,ep);
    if(tid==0) st32(A_TAG,ep*2891336453u+1013904223u);
    int slot=0;
    AFr f0,f1; AOp g0,g1;
    ldsbar();
    STAMP(1);
    #pragma unroll
    for(int i=0;i<15;i++) a_rd<0>(i,f0,ae0,az0,ao0,wa,we);
    a_wait(f0,g0);
    #pragma unroll 1
    for(int s=0;s<8;s++){
      const int dl=(slot==2)?-2*A_SLOT:A_SLOT; slot=(slot==2)?0:slot+1;
      #pragma unroll
      for(int i=0;i<24;i++){
        a_mm(i,acc,ace,g0);
        if(i<15) a_rd<13824>(i,f1,ae1,az1,ao1,wa,we);
        if(i==15) AADD(ae0,dl); if(i==16) AADD(az0,dl); if(i==17) AADD(ao0,dl); if(i==18) AADD(wa,dl); if(i==19) AADD(we,dl);
      }
      a_wait(f1,g1);
      BAR;
      #pragma unroll
      for(int i=0;i<24;i++){
        a_mm(i,acc,ace,g1);
        if(i<15) a_rd<0>(i,f0,ae0,az0,ao0,wa,we);
        if(i==15) AADD(ae1,dl); if(i==16) AADD(az1,dl); if(i==17) AADD(ao1,dl);
      }
      a_wait(f0,g0);
    }
    #pragma unroll
    for(int i=0;i<24;i++){
      a_mm(i,acc,ace,g0);
      if(i<15) a_rd<13824>(i,f1,ae1,az1,ao1,wa,we);
    }
    a_wait(f1,g1);
    BAR;
    SB;
    #pragma unroll
    for(int m=0;m<3;m++) ace[m]=mfma(g1.w[3],g1.x[m],ace[m]);
    #pragma unroll
    for(int m=0;m<7;m++){
      #pragma unroll
      for(int c=0;c<3;c++) acc[c][m]=mfma(g1.w[c],g1.x[m],acc[c][m]);
    }
    #pragma unroll
    for(int m=0;m<3;m++) st64(ix+m*16*A_IROW,(v2u){pk2(ace[m][0],ace[m][1]),pk2(ace[m][2],ace[m][3])});
    #pragma unroll
    for(int m=0;m<7;m++){
      const unsigned ib=(m<3)?ie+m*16*A_IROW:(m==3?iz:io+(m-4)*16*A_IROW);
      #pragma unroll
      for(int c=0;c<3;c++) st64v(ib+c*32,(v2u){pk2(acc[c][m][0],acc[c][m][1]),pk2(acc[c][m][2],acc[c][m][3])});
    }
    SGB(0x8,3);
    #pragma unroll
    for(int i=0;i<21;i++){ SGB(0x8,1); SGB(0x2,2); SGB(0x200,1); }
    SB;
    STAMP(2);
  }
  ldsbar();
  STAMP(3);
  const unsigned tag=ld32(A_TAG);
  if(wave>=6){
    const v4u so=mksrd(p.wo,576*1152);
    #pragma unroll
    for(int i=0;i<5;i++) if(i<4 || wave==6) dma16o(so,vl[i],0,8*9216+(wave&1)*1024+i*2048);
    WAITV(0);
  } else {
    v4u qf[4],kp[4],kc[4],vf[5];
    #pragma unroll
    for(int h=0;h<4;h++){
      const int hb=36*h-4*(h&1);
      qf[h]=cat(ld64v(ka+16*A_IROW+hb),ld64v(qb+hb));
      kp[h]=cat(ld64v(ka+144+hb),ld64v(kb+144+hb));
      kc[h]=cat(ld64v(ka+16*A_IROW+144+hb),ld64v(qb+144+hb));
    }
    #pragma unroll
    for(int c=0;c<5;c++) vf[c]=cat(ldtr(va+32*c),ldtr(va+16*A_IROW+32*c));
    SB;
    f4 sp[4],sc[4];
    #pragma unroll
    for(int h=0;h<4;h++){
      v4u q=qf[h];
      if(h&1) q.x=(g==0)?0u:q.x; else q.w=0u;
      sp[h]=mfma(kp[h],q,(f4){0,0,0,0}); sc[h]=mfma(kc[h],q,(f4){0,0,0,0});
    }
    SB;
    STAMP(13);
    unsigned a0[4],a1[4];
    #pragma unroll
    for(int h=0;h<4;h++){
      const float c0=sc[h][0],c1=sc[h][1],c2=sc[h][2],c3=sc[h][3],p0=sp[h][0],p1=sp[h][1],p2=sp[h][2],p3=sp[h][3];
      const float ca=par?c1:c0, pa=par?p1:p0, cb=par?c3:c2, pb=par?p3:p2;
      a0[h]=__builtin_bit_cast(unsigned,cur0?ca:pa);
      a1[h]=__builtin_bit_cast(unsigned,cur1?cb:pb);
    }
    if(row0+16*wave==0){
      const unsigned mk=__builtin_bit_cast(unsigned,-1e9f/0.23570226039551584f);
      #pragma unroll
      for(int h=0;h<4;h++){ a0[h]=cur0?a0[h]:mk; a1[h]=cur1?a1[h]:mk; }
    }
    tr4(a0); tr4(a1);
    float l[8];
    #pragma unroll
    for(int i=0;i<4;i++){ l[2*i]=__builtin_bit_cast(float,a0[i]); l[2*i+1]=__builtin_bit_cast(float,a1[i]); }
    constexpr float SC=0.23570226039551584f*1.4426950408889634f;
    const float mx=-SC*max3(max3(l[0],l[1],l[2]),max3(l[3],l[4],l[5]),max2(l[6],l[7]));
    #pragma unroll
    for(int i=0;i<8;i++) l[i]=__builtin_amdgcn_exp2f(__builtin_fmaf(l[i],SC,mx));
    const float inv=__builtin_amdgcn_rcpf(((l[0]+l[1])+(l[2]+l[3]))+((l[4]+l[5])+(l[6]+l[7])));
    unsigned pp[4];
    #pragma unroll
    for(int i=0;i<4;i++) pp[i]=pk2(l[2*i]*inv,l[2*i+1]*inv);
    tr4(pp);
    v4u pf[4];
    #pragma unroll
    for(int h=0;h<4;h++) pf[h]=(v4u){__builtin_amdgcn_perm(0u,pp[h],sel0),__builtin_amdgcn_perm(0u,pp[h],sel1),__builtin_amdgcn_perm(0u,pp[h],sel2),__builtin_amdgcn_perm(0u,pp[h],sel3)};
    const f4 z={0,0,0,0};
    const f4 o0=mfma(vf[0],pf[0],z), a1_=mfma(vf[1],pf[0],z), b1=mfma(vf[1],pf[1],z), a2=mfma(vf[2],pf[1],z),
             b2_=mfma(vf[2],pf[2],z), a3=mfma(vf[3],pf[2],z), b3=mfma(vf[3],pf[3],z), o4=mfma(vf[4],pf[3],z);
    const v4u t01={pk2(o0[0],o0[1]),pk2(o0[2],o0[3]), (g==0)?pk2(a1_[0],a1_[1]):pk2(b1[0],b1[1]), pk2(b1[2],b1[3])};
    const v4u t23={(g==0)?pk2(a2[0],a2[1]):pk2(b2_[0],b2_[1]), (g==0)?pk2(a2[2],a2[3]):pk2(b2_[2],b2_[3]),
                   (g<2)?pk2(a3[0],a3[1]):pk2(b3[0],b3[1]), (g==0)?pk2(a3[2],a3[3]):pk2(b3[2],b3[3])};
    const unsigned u0=pk2(o4[0],o4[1]), u1=pk2(o4[2],o4[3]);
    auto r0=__builtin_amdgcn_permlane16_swap(u0,u0,false,false);
    auto r1=__builtin_amdgcn_permlane16_swap(u1,u1,false,false);
    STAMP(12);
    const unsigned ob=rt*WS_OT+wave*WS_OM;
    __builtin_amdgcn_raw_buffer_store_b128(t01,wsr,ob+2*grp*1024+lane*16,0,16);
    __builtin_amdgcn_raw_buffer_store_b128(t23,wsr,ob+2*grp*1024+1024+lane*16,0,16);
    const v4u t4={(unsigned)r0[0],(unsigned)r1[0],(unsigned)r0[1],(unsigned)r1[1]};
    unsigned hs=sum5(sum5(0,t01),t23);
    { auto x=__builtin_amdgcn_permlane16_swap(hs,hs,false,false); hs=x[0]+x[1];
      auto y=__builtin_amdgcn_permlane32_swap(hs,hs,false,false); hs=y[0]+y[1]; }
    if(g==0){
      __builtin_amdgcn_raw_buffer_store_b128(t4,wsr,ob+16*1024+grp*256+lane*16,0,16);
      __builtin_amdgcn_raw_buffer_store_b64((v2u){sum5(hs,t4)+tag,tag},wsr,ob+18*1024+grp*128+lane*8,0,16);
    }
    WAITV(4);
  }
  STAMP(4);
  BAR;
  STAMP(5);
  {
    const int m=(wave<4)?wave:4+((wave-4)>>1);
    const unsigned wa=(8*g+2*(i16>>2))*144+(lane&3)*8;
    const unsigned ob=rt*WS_OT+m*WS_OM+lane*16, rb=rt*WS_OT+m*WS_OM+18*1024+lane*8;
    const unsigned red=A_IMG+((wave>>1)&1)*5120+lane*16, rf=A_ZERO+272+((wave>>1)&1)*4;
    int l9=lane/9, c9=lane-l9*9;
    asm volatile("":"+v"(l9),"+v"(c9));
    f4 acc[5];
    #define OLD(ks) __builtin_amdgcn_raw_buffer_load_b128(wsr,ob,(ks)*1024,16)
    #define RLD(st) __builtin_amdgcn_raw_buffer_load_b64(wsr,rb,(st)*512,16)
    #define WFR(a,ks,c) cat(ldtr((a)+(ks)*4608+(c)*32),ldtr((a)+(ks)*4608+(c)*32+144))
    #define MEMF asm volatile("":::"memory")
    if(wave<4){
      constexpr int NPRE=PNPRE, NW=9, NISS=PNISS, NF=PNF;
      #pragma unroll 1
      for(int tries=0;tries<64;tries++){
        v4u wf[NPRE][5], of[18]; v2u r0,r1;
        #pragma unroll
        for(int k=0;k<NPRE;k++){
          #pragma unroll
          for(int c=0;c<5;c++) wf[k][c]=WFR(wa,k,c);
          if(k==NISS){ SB; MEMF; r0=RLD(0); r1=RLD(1); SB;
            #pragma unroll
            for(int j=0;j<NF;j++){ of[j]=OLD(j); SB; } }
        }
        SB;
        #pragma unroll 1
        for(int spin=0;spin<(1<<20);spin++){
          if(__builtin_amdgcn_ballot_w64(r0.y!=tag || r1.y!=tag)==0) break;
          MEMF; r0=RLD(0); r1=RLD(1); SB;
          #pragma unroll
          for(int j=0;j<NF;j++){ of[j]=OLD(j); SB; }
        }
        #pragma unroll
        for(int j=NF;j<NW;j++){ of[j]=OLD(j); SB; }
        STAMP(6);
        #pragma unroll
        for(int c=0;c<5;c++) acc[c]=(f4){0,0,0,0};
        unsigned hs[10];
        #pragma unroll
        for(int k=0;k<18;k++){
          #pragma unroll
          for(int c=0;c<5;c++){
            acc[c]=mfma(wf[k%NPRE][c],of[k],acc[c]);
            if(k+NPRE<18){ wf[k%NPRE][c]=WFR(wa,k+NPRE,c); SGB(0x8,1); SGB(0x100,2); }
          }
          if(k>=16) hs[k-8]=sum5(tag,of[k]); else hs[k>>1]=sum5((k&1)?hs[k>>1]:0u,of[k]);
          if(k==7) hs[0]=rs4(hs[0],hs[1],hs[2],hs[3]);
          if(k==15) hs[4]=rs4(hs[4],hs[5],hs[6],hs[7]);
          SB;
          if(k+NW<18){ of[k+NW]=OLD(k+NW); SB; }
        }
        if(__builtin_amdgcn_ballot_w64(hs[0]+hs[8]!=r0.x || hs[4]+hs[9]!=r1.x)==0) break;
      }
      STAMP(7);
    } else {
      const int hf=wave&1;
      const unsigned wb=wa+hf*8*4608, wc=wa+(16+hf)*4608;
      #pragma unroll 1
      for(int tries=0;tries<64;tries++){
        v4u wf[9][5], of[9]; v2u r0;
        #pragma unroll
        for(int k=0;k<8;k++){
          #pragma unroll
          for(int c=0;c<5;c++) wf[k][c]=WFR(wb,k,c);
          if(k==PNISS){ SB; MEMF; r0=RLD(hf); SB;
            #pragma unroll
            for(int j=0;j<PNF;j++){ of[j]=OLD(8*hf+j); SB; } }
        }
        #pragma unroll
        for(int c=0;c<5;c++) wf[8][c]=WFR(wc,0,c);
        SB;
        #pragma unroll 1
        for(int spin=0;spin<(1<<20);spin++){
          if(__builtin_amdgcn_ballot_w64(r0.y!=tag)==0) break;
          MEMF; r0=RLD(hf); SB;
          #pragma unroll
          for(int j=0;j<PNF;j++){ of[j]=OLD(8*hf+j); SB; }
        }
        #pragma unroll
        for(int j=PNF;j<9;j++){ of[j]=OLD(j<8?8*hf+j:16+hf); SB; }
        __builtin_amdgcn_s_setprio(1);
        STAMP(6);
        #pragma unroll
        for(int c=0;c<5;c++) acc[c]=(f4){0,0,0,0};
        unsigned hs[5];
        #pragma unroll
        for(int k=0;k<9;k++){
          #pragma unroll
          for(int c=0;c<5;c++) acc[c]=mfma(wf[k][c],of[k],acc[c]);
          hs[k>>1]=sum5((k==8)?tag:((k&1)?hs[k>>1]:0u),of[k]);
          SB;
        }
        if(__builtin_amdgcn_ballot_w64(rs4(hs[0],hs[1],hs[2],hs[3])+hs[4]!=r0.x)==0) break;
      }
      if(hf){
        #pragma unroll
        for(int c=0;c<5;c++) st128(red+c*1024,__builtin_bit_cast(v4u,acc[c]));
        WAITL;
        *(volatile LAS unsigned*)rf=1u;
      } else {
        for(int spin=0;spin<(1<<24);spin++) if(__builtin_amdgcn_readfirstlane(*(volatile LAS unsigned*)rf)) break;
        asm volatile("":::"memory");
        #pragma unroll
        for(int c=0;c<5;c++) acc[c]+=__builtin_bit_cast(f4,ld128(red+c*1024));
      }
      STAMP(7);
    }
    if(wave<4 || !(wave&1)){
      const unsigned tb=A_IMG+10240+wave*2560;
      #pragma unroll
      for(int c=0;c<5;c++) st64(tb+i16*160+g*8+c*32,(v2u){pk2(acc[c][0],acc[c][1]),pk2(acc[c][2],acc[c][3])});
      #pragma unroll
      for(int i=0;i<3;i++){
        const int r=7*i+l9;
        if(lane<63 && r<16 && m*16+r<nrow) *(GAS v4u*)(p.out+(size_t)(row0+m*16+r)*2304+grp*144+c9*16)=ld128(tb+r*160+c9*16);
      }
    }
    asm volatile(".Lcpf1:");
    if(tid==0 && grp==0) __hip_atomic_store(epoch,tag,__ATOMIC_RELAXED,__HIP_MEMORY_SCOPE_AGENT);
  }
  STAMP(8);
}

NOPK __global__ __launch_bounds__(512,2) void fused(Params p){
  STAMPDECL;
  STAMP(0);
  const int b=__builtin_amdgcn_workgroup_id_x(), xcd=b&7, j=b>>3, pr=xcd>>1, half=xcd&1;
  const int nA=(pr<3)?24:20;
  {
    u64 pc; unsigned len;
    asm volatile("s_getpc_b64 %0\n.Lcpf0:\n\ts_mov_b32 %1, .Lcpf1-.Lcpf0":"=s"(pc),"=s"(len));
    const unsigned t=__builtin_amdgcn_workitem_id_x();
    if(t<40){ unsigned o=((j&7)+t*8)*128; o=o<len?o:len;
      asm volatile("s_mov_b32 m0, 0x27f00\n\ts_nop 0\n\tglobal_load_lds_dword %0, %1"::"v"(o&~3u),"s"(pc):"memory"); }
  }
  if(j>=nA){
    const int jb=j-nA;
    role_b(p, jb<8? xcd*8+jb : 64+half*4+(jb-8) STAMPPASS);
  } else
    { const int grp=half*4+(j&3); int rot=(((j>>2)*3)>>1)+2*pr+((0x86317420u>>(4*grp))&15); rot-=(rot>=9)?9:0; rot-=(rot>=9)?9:0;
    role_a(p, 6*pr+(j>>2), grp, rot STAMPPASS); }
}

constexpr int LDS_BYTES=160*1024;
extern "C" void kernel_launch(void* const* d_in, const int* in_sizes, int n_in, void* d_out, int out_size, void* d_ws, size_t ws_size, hipStream_t stream){
  Params p;
  p.x=(const char*)d_in[0]; p.wqkv=(const char*)d_in[1]; p.wo=(const char*)d_in[2];
  p.q=(const char*)d_in[3]; p.k=(const char*)d_in[4]; p.v=(const char*)d_in[5];
  p.sk=(const char*)d_in[6]; p.hk=(const char*)d_in[7]; p.dv=(const char*)d_in[8];
  p.out=(char*)d_out; p.ws=(char*)d_ws;
  (void)hipFuncSetAttribute((const void*)fused,hipFuncAttributeMaxDynamicSharedMemorySize,LDS_BYTES);
  fused<<<256,512,LDS_BYTES,stream>>>(p);
}
```

```cpp
#include <hip/hip_runtime.h>
#include <hip/hip_bf16.h>

typedef unsigned short u16;
typedef unsigned long long u64;
typedef unsigned v4u __attribute__((ext_vector_type(4)));
typedef unsigned v2u __attribute__((ext_vector_type(2)));
typedef short s4 __attribute__((ext_vector_type(4)));
typedef __bf16 b2 __attribute__((ext_vector_type(2)));
typedef __bf16 b8 __attribute__((ext_vector_type(8)));
typedef float f4 __attribute__((ext_vector_type(4)));
typedef float f2 __attribute__((ext_vector_type(2)));
#define LAS __attribute__((address_space(3)))
#define GAS __attribute__((address_space(1)))
#if defined(__HIP_DEVICE_COMPILE__)
#define NOPK __attribute__((target("no-packed-fp32-ops")))
#else
#define NOPK
#endif
#define DEV __device__ __forceinline__ NOPK
#define SB __builtin_amdgcn_sched_barrier(0)
#define SGB(m,n) __builtin_amdgcn_sched_group_barrier(m,n,0)
#pragma clang diagnostic ignored "-Wint-to-pointer-cast"

#define STAMP(i)
#define STAMPDECL
#define STAMPARG
#define STAMPPASS
#define STAMPOUT

#define PNPRE 6
#define PNISS 3
#define PNF 4
#define ALOOK 2
#define NOAT
#define ROT 22
#define NOPR
#define BV 1
#define CPFN 32
#define NTB " nt"
#define NTQ ""
#define NTX ""
#define NTW ""
#define NTO ""
#define BSTAMP(i)
#define NSTAMP(i) STAMP(i)
#define BSLP 16
#define MTS 0
#define WOV 2
#define STV 1
struct Params {
  const char *x, *wqkv, *wo, *q, *k, *v, *sk, *hk, *dv;
  char *out, *ws;
};

DEV float lo2f(unsigned d){ return __builtin_bit_cast(float,d<<16); }
DEV float hi2f(unsigned d){ return __builtin_bit_cast(float,d&0xffff0000u); }
DEV unsigned pk2(float lo, float hi){ f2 p={lo,hi}; return __builtin_bit_cast(unsigned,__builtin_convertvector(p,b2)); }
DEV unsigned pk2a(float lo, float hi){ unsigned d; asm("v_cvt_pk_bf16_f32 %0, %1, %2":"=v"(d):"v"(lo),"v"(hi)); return d; }
DEV v4u ld128(unsigned a){ return *(LAS v4u*)a; }
DEV v2u ld64v(unsigned a){ return *(volatile LAS v2u*)a; }
DEV unsigned ld32(unsigned a){ return *(LAS unsigned*)a; }
DEV void st128(unsigned a, v4u v){ *(LAS v4u*)a=v; }
DEV void st64(unsigned a, v2u v){ *(LAS v2u*)a=v; }
DEV void st64v(unsigned a, v2u v){ *(volatile LAS v2u*)a=v; }
DEV void st32(unsigned a, unsigned v){ *(LAS unsigned*)a=v; }
DEV v2u ldtr(unsigned a){ return __builtin_bit_cast(v2u,__builtin_amdgcn_ds_read_tr16_b64_v4i16((LAS s4*)a)); }
DEV f4 mfma(v4u a, v4u b, f4 c){ return __builtin_amdgcn_mfma_f32_16x16x32_bf16(__builtin_bit_cast(b8,a),__builtin_bit_cast(b8,b),c,0,0,0); }
DEV v4u cat(v2u a, v2u b){ return (v4u){a.x,a.y,b.x,b.y}; }
DEV v4u gld128(const char* p){ return *(const GAS v4u*)p; }
DEV void gloadq(v4u& d, const char* sbase, unsigned voff){ asm volatile("global_load_dwordx4 %0, %1, %2" NTQ:"=v"(d):"v"(voff),"s"(sbase):"memory"); }
DEV void gload(v4u& d, const char* sbase, unsigned voff){ asm volatile("global_load_dwordx4 %0, %1, %2" NTB:"=v"(d):"v"(voff),"s"(sbase):"memory"); }
DEV void greload(v4u& d, const char* sbase, unsigned voff){
 asm volatile("global_load_dwordx4 %0, %1, %2":"+v"(d):"v"(voff),"s"(sbase):"memory"); }
#define WAIT1(n,a) asm volatile("s_waitcnt vmcnt(" #n ")":"+v"(a)::"memory")
#define WAIT2(n,a,b) asm volatile("s_waitcnt vmcnt(" #n ")":"+v"(a),"+v"(b)::"memory")
#define WAIT3(n,a,b,c) asm volatile("s_waitcnt vmcnt(" #n ")":"+v"(a),"+v"(b),"+v"(c)::"memory")
#define WAIT4(n,a,b,c,d) asm volatile("s_waitcnt vmcnt(" #n ")":"+v"(a),"+v"(b),"+v"(c),"+v"(d)::"memory")
#define WAITL asm volatile("":::"memory"); __builtin_amdgcn_s_waitcnt(0xC07F); asm volatile("":::"memory")
DEV void ldsbar(){ WAITL; __builtin_amdgcn_s_barrier(); asm volatile("":::"memory"); }
DEV int imin(int a,int b){ return a<b?a:b; }
DEV int imax(int a,int b){ return a>b?a:b; }
#define WAITV(n) asm volatile("s_waitcnt vmcnt(" #n ")":::"memory")
#define BAR do{ asm volatile("":::"memory"); __builtin_amdgcn_s_barrier(); asm volatile("":::"memory"); }while(0)
template<int NR,int NM> DEV void interleave(){
  constexpr int PER=(NR+NM-1)/NM, NG=(NR+PER-1)/PER;
  #pragma unroll
  for(int i=0;i<NG;i++){ SGB(0x8,1); SGB(0x100,PER); }
  if(NM>NG) SGB(0x8,NM-NG);
}

#define AMM(acc,a,b) asm volatile("v_mfma_f32_16x16x32_bf16 %0, %1, %2, %0":"+v"(acc):"v"(a),"v"(b))
#define ARD(d,a,off) asm volatile("ds_read_b128 %0, %1 offset:%2":"=v"(d):"v"(a),"i"(off):"memory")
#define ATR(d,a,off) asm volatile("ds_read_b64_tr_b16 %0, %1 offset:%2":"=v"(d):"v"(a),"i"(off):"memory")
#define AADD(v,s) asm volatile("v_add_u32 %0, %1, %0":"+v"(v):"s"(s))
#define AR64(d,a,off) asm volatile("ds_read_b64 %0, %1 offset:%2":"=v"(d):"v"(a),"i"(off):"memory")
constexpr int B_HA=448, B_HB=4576+448, B_X0=9120, B_X1=13344, B_ST=17520, B_PITCH=19696, B_SCR=8*B_PITCH;

template<int BLO,int NB> struct PF { v4u h[8-BLO]; v2u x[NB][2]; };
template<int BLO,int NB,int OFF> DEV void p_rd(int i, PF<BLO,NB>& f, unsigned hb, unsigned xb){
  #pragma unroll
  for(int a=0;a<8-BLO;a++) if(i==2+a) ARD(f.h[a],hb,512*a+OFF);
  #pragma unroll
  for(int b=0;b<NB;b++){ const int q=b?8-BLO+2*b:0; if(i==q) AR64(f.x[b][0],xb,512*(7-BLO-b)+OFF); if(i==q+1) AR64(f.x[b][1],xb,512*(7-BLO-b)+8+OFF); }
}
template<int BLO,int NB> DEV void p_mm(int i, f4 (&acc)[8], PF<BLO,NB>& f){
  int n=0;
  #pragma unroll
  for(int b=0;b<NB;b++){
    #pragma unroll
    for(int a=0;a<8-BLO-b;a++){ if(n==i){ v4u x=cat(f.x[b][0],f.x[b][1]); AMM(acc[a+b+BLO],f.h[a],x); } n++; }
  }
}
template<int N,int BLO,int NB> DEV void p_wait(PF<BLO,NB>& f){
  asm volatile("s_waitcnt lgkmcnt(%0)"::"i"(N):"memory");
  #pragma unroll
  for(int a=0;a<8-BLO;a++) asm volatile("":"+v"(f.h[a]));
  #pragma unroll
  for(int b=0;b<NB;b++) asm volatile("":"+v"(f.x[b][0]),"+v"(f.x[b][1]));
}
template<int BLO,int NB,int OFF> DEV void p_block(f4 (&acc)[8], PF<BLO,NB>& fc, PF<BLO,NB>& fn, unsigned hb, unsigned xb){
  constexpr int NR=8-BLO+2*NB, NM=NB*(8-BLO)-NB*(NB-1)/2, EX=(NR>NM)?NR-NM:0;
  constexpr int I2=(NB==2? 6-BLO : (NB-2)*(8-BLO)-(NB-2)*(NB-3)/2);
  p_wait<4>(fc);
  #pragma unroll
  for(int i=0;i<NM;i++){
    if(i==I2) asm volatile("s_waitcnt lgkmcnt(%0)"::"i"(I2+EX<NR?I2+EX:NR):"memory");
    p_mm<BLO,NB>(i,acc,fc);
    if(i<EX){ p_rd<BLO,NB,OFF>(2*i,fn,hb,xb); p_rd<BLO,NB,OFF>(2*i+1,fn,hb,xb); }
    else if(i+EX<NR) p_rd<BLO,NB,OFF>(i+EX,fn,hb,xb);
  }
}
template<int BLO,int NB> DEV void b_seg(f4 (&acc)[8], PF<BLO,NB>& f0, unsigned hb, unsigned xb, int next){
  PF<BLO,NB> f1;
  #pragma unroll 1
  for(int r=0;r<4;r++){
    p_block<BLO,NB,64>(acc,f0,f1,hb,xb);
    const int d=(r==3)? next-384 : 128;
    AADD(hb,d); AADD(xb,d);
    p_block<BLO,NB,0>(acc,f1,f0,hb,xb);
  }
}
template<int JLO,int NJ> DEV void b_zout(f4 (&acc)[8], unsigned za){
  asm volatile("s_nop 7\n\ts_nop 3":::"memory");
  #pragma unroll
  for(int J=JLO;J<JLO+NJ;J++){
    asm volatile("":"+v"(acc[J]));
    st32(za+512*(7-J)+64,pk2a(acc[J][1],acc[J][0])); st32(za+512*(7-J),pk2a(acc[J][3],acc[J][2]));
  }
}
template<int BLO,int NB> DEV void b_stage(f4 (&a0)[8], f4 (&a1)[8], unsigned hb, unsigned xb, unsigned za){
  PF<BLO,NB> f;
  #pragma unroll
  for(int i=2;i<10-BLO;i++) p_rd<BLO,NB,0>(i,f,hb,xb);
  ldsbar();
  p_rd<BLO,NB,0>(0,f,hb,xb); p_rd<BLO,NB,0>(1,f,hb,xb);
  #pragma unroll
  for(int i=10-BLO;i<8-BLO+2*NB;i++) p_rd<BLO,NB,0>(i,f,hb,xb);
  asm volatile("s_waitcnt lgkmcnt(%0)"::"i"(2*NB-2):"memory");
  b_seg<BLO,NB>(a0,f,hb,xb,B_PITCH);
  b_zout<BLO,NB>(a0,za);
  b_seg<BLO,NB>(a1,f,hb+B_PITCH,xb+B_PITCH,0);
  b_zout<BLO,NB>(a1,za+B_PITCH);
  p_wait<0>(f);
}

DEV unsigned sum5(unsigned m, v4u v){ return (m+v.x+v.y)+v.z+v.w; }
DEV void b_prep2(unsigned ta, unsigned tv, v4u sl0, v4u sh0, v4u sl1, v4u sh1, v4u i01, v4u i23, v4u& oa, v4u& ob){
  v4u a[2][4], vr[2];
  #pragma unroll
  for(int t=0;t<2;t++){ vr[t]=ld128(tv+t*B_PITCH); a[t][0]=ld128(ta+t*B_PITCH); a[t][2]=ld128(ta+t*B_PITCH+64); }
  #pragma unroll
  for(int t=0;t<2;t++){ a[t][1]=ld128(ta+t*B_PITCH+512); a[t][3]=ld128(ta+t*B_PITCH+576); }
  SB;
  const f4 z={0,0,0,0};
  f4 v1[2],v2[2],d1[2],d2[2];
  #pragma unroll
  for(int t=0;t<2;t++){ v1[t]=mfma(vr[t],i01,z); v2[t]=mfma(vr[t],i23,z); d1[t]=mfma(a[t][0],t?sl1:sl0,z); d2[t]=mfma(a[t][2],t?sl1:sl0,z); }
  #pragma unroll
  for(int t=0;t<2;t++){ d1[t]=mfma(a[t][1],t?sh1:sh0,d1[t]); d2[t]=mfma(a[t][3],t?sh1:sh0,d2[t]); }
  SB;
  #pragma unroll
  for(int t=0;t<2;t++){
    const v4u o={pk2(d2[t][3]*v2[t][3],d2[t][2]*v2[t][2]),pk2(d2[t][1]*v2[t][1],d2[t][0]*v2[t][0]),pk2(d1[t][3]*v1[t][3],d1[t][2]*v1[t][2]),pk2(d1[t][1]*v1[t][1],d1[t][0]*v1[t][0])};
    if(t) ob=o; else oa=o;
  }
}
DEV void b_put(unsigned a, v4u o){
  st128(a+B_X0,o);
  st32(a+B_X1+4,o.x); st64(a+B_X1+8,(v2u){o.y,o.z}); st32(a+B_X1+16,o.w);
}
DEV v4u b_diag(int di, unsigned e){ return (v4u){di==0?e:0u,di==1?e:0u,di==2?e:0u,di==3?e:0u}; }
constexpr int WS_B=4<<20, WS_BT=1280;
DEV void b_gate(unsigned ga, unsigned gb, v4u q, const float (&dd)[8], char* outp){
  const v2u y0=ldtr(ga), x0=ldtr(ga+B_X0-B_HA), y1=ldtr(gb), x1=ldtr(gb+B_X0-B_HA);
  #define G2(Q,Y,X,i) pk2(lo2f(Q)*__builtin_fmaf(lo2f(X),dd[i],lo2f(Y)), hi2f(Q)*__builtin_fmaf(hi2f(X),dd[i+1],hi2f(Y)))
  const v4u o={G2(q.x,y0.x,x0.x,0),G2(q.y,y0.y,x0.y,2),G2(q.z,y1.x,x1.x,4),G2(q.w,y1.y,x1.y,6)};
  *(GAS v4u*)outp=o;
}

DEV void role_b(const Params& p, int cg STAMPARG){
  const int tid=__builtin_amdgcn_workitem_id_x(), lane=tid&63, wave=__builtin_amdgcn_readfirstlane(tid>>6);
  const int i16=lane&15, g=lane>>4;
  constexpr unsigned BLK=512*1152;
  const int w=wave&3, ql=lane^3;
  const unsigned rq=(w*128+ql)*1152+cg*16;
  unsigned ga,gx; char* outp; float dd[8];
  #define GCONST { const v4u dw=*(const __attribute__((address_space(4))) v4u*)(p.dv+cg*16); \
    dd[0]=lo2f(dw.x); dd[1]=hi2f(dw.x); dd[2]=lo2f(dw.y); dd[3]=hi2f(dw.y); dd[4]=lo2f(dw.z); dd[5]=hi2f(dw.z); dd[6]=lo2f(dw.w); dd[7]=hi2f(dw.w); \
    ga=(i16>>2)*B_PITCH+B_HA+(2044-1536-64-128*w-16*g-4*(lane&3))*2; gx=ga+4*B_PITCH; outp=p.out+(size_t)(w*128+ql)*2304+1152+cg*16; \
    asm volatile("":"+v"(ga),"+v"(gx),"+v"(outp)); }
  #define GATE(b,Q0,Q1) { b_gate(ga+1024*(3-(b))+128,gx+1024*(3-(b))+128,Q0,dd,outp+(size_t)(b)*512*2304); b_gate(ga+1024*(3-(b)),gx+1024*(3-(b)),Q1,dd,outp+(size_t)((b)*512+64)*2304); }
  if(wave<4){
    v4u hr[8];
    BAR;
    #pragma unroll
    for(int i=0;i<8;i++) gload(hr[i],p.hk,(cg*8+2*wave)*4096+i*1024+lane*16);
    const unsigned cb=2*wave*B_PITCH;
    #pragma unroll
    for(int c=0;c<2;c++){
      if(lane<56) st128(cb+c*B_PITCH+(lane<28? lane*16 : 4576+(lane-28)*16),(v4u){0,0,0,0});
      if(lane<31) st32(cb+c*B_PITCH+(lane<16? B_X0+4096+lane*4 : B_X1+4100+(lane-16)*4),0u);
    }
    unsigned hb=cb+((i16&1)?B_HA:B_HB)+(32*(i16>>1)+8*g)*2-448;
    unsigned xb=cb+((i16&1)? B_X0+(2046-2*i16+8*g)*2 : B_X1+(2048-2*i16+8*g)*2)-512*7-448;
    unsigned za=cb+B_HA+(2046-64*g-2*i16)*2-64-512*7;
    asm volatile("":"+v"(hb),"+v"(xb),"+v"(za));
    GCONST
    f4 a0[8],a1[8];
    #pragma unroll
    for(int j=0;j<8;j++){ a0[j]=(f4){0,0,0,0}; a1[j]=(f4){0,0,0,0}; asm volatile("":"+v"(a0[j]),"+v"(a1[j])); }
    #pragma unroll
    for(int c=0;c<2;c++){
      if(c==0) WAIT4(4,hr[0],hr[1],hr[2],hr[3]); else WAIT4(0,hr[4],hr[5],hr[6],hr[7]);
      #pragma unroll
      for(int i=0;i<4;i++){
        const v4u h=hr[4*c+i];
        const unsigned first=i? __builtin_amdgcn_readlane(hr[4*c+i-1].w,63) : 0u;
        const unsigned pv=__builtin_amdgcn_update_dpp(first,h.w,0x138,0xf,0xf,false);
        const v4u s={__builtin_amdgcn_alignbit(h.x,pv,16),__builtin_amdgcn_alignbit(h.y,h.x,16),__builtin_amdgcn_alignbit(h.z,h.y,16),__builtin_amdgcn_alignbit(h.w,h.z,16)};
        st128(cb+c*B_PITCH+B_HA+i*1024+lane*16,h);
        st128(cb+c*B_PITCH+B_HB+i*1024+lane*16,s);
      }
    }
    STAMP(1);
    __builtin_amdgcn_s_setprio(1);
    b_stage<0,2>(a0,a1,hb,xb,za);
    STAMP(3);
    b_stage<2,2>(a0,a1,hb,xb,za);
    STAMP(4);
    v4u qa,qb;
    gloadq(qa,p.q+3*BLK,rq); gloadq(qb,p.q+3*BLK+64*1152,rq);
    b_stage<4,4>(a0,a1,hb,xb,za);
    STAMP(5);
    WAIT2(0,qa,qb);
    ldsbar();
    GATE(3,qa,qb)
    STAMP(7);
  } else {
    const int j=cg&7, cp=(cg&~7)+2*w;
    const unsigned rk=(j*64+w*8+(lane>>3))*1152+(cg>>3)*128+(lane&7)*16;
    v4u k0[4],k1[4],v0[4],v1[4],q0[3],q1[3],halo; unsigned sk0,sk1,ep;
    #define LOADKV(b) { gload(k0[b],p.k+(b)*BLK,rk); gload(k1[b],p.k+(b)*BLK+32*1152,rk); gload(v0[b],p.v+(b)*BLK,rk); gload(v1[b],p.v+(b)*BLK+32*1152,rk); }
    #define LOADQ(b) { gloadq(q0[b],p.q+(b)*BLK,rq); gloadq(q1[b],p.q+(b)*BLK+64*1152,rq); }
    unsigned junk=0;
    #define TOUCH(P,O) asm volatile("global_load_dword %0, %1, %2":"+v"(junk):"v"(O),"s"(P):"memory")
    asm volatile("global_load_ushort %0, %2, %3\n\tglobal_load_ushort %1, %2, %3 offset:64":"=&v"(sk0),"=&v"(sk1):"v"(cp*64+(lane&7)*8+g*2),"s"(p.sk):"memory");
    GAS unsigned* epoch=(GAS unsigned*)(p.ws+(32+(cg>>3))*256);
    asm volatile("global_load_dword %0, %1, %2 sc1":"=v"(ep):"v"(0),"s"(epoch):"memory");
    LOADKV(0)
    const int ht=512*((lane>>3)&3)+64*j-4+w;
    gload(halo,p.k,imax(ht,0)*1152+(cg>>3)*128+(lane&7)*16);
    const unsigned po=(cp*32+j)*WS_BT, co=(cg*32+2*w)*WS_BT;
    st32(B_SCR+4*w,0u);
    ldsbar();
    TOUCH(p.ws+WS_B,co+imin(lane,19)*128);
    LOADKV(1)
    NSTAMP(10);
    GCONST
    #define KO(sl) ((sl)?B_ST+64:B_X1)
    #define VO(sl) ((sl)?B_ST+1088:B_X0)
    unsigned sw=(lane&7)*B_PITCH+(w*8+(lane>>3))*16, sh=(lane&7)*B_PITCH+w*16;
    unsigned ta=2*w*B_PITCH+(4+8*(i16>>2)+(lane&3)-g)*16, tv=2*w*B_PITCH+(32*(g&1)+8*(i16>>2)+4*(g>>1)+(lane&3))*16;
    unsigned xw=(lane&7)*B_PITCH+(2040-1536-64-128*w-32*(i16>>3)-8*g)*2;
    const int di=(lane&7)>>1;
    const unsigned one=0x3f80u<<(16*(lane&1));
    v4u i01=b_diag(di,(g==(i16>>3))?one:0u), i23=b_diag(di,(g==(i16>>3)+2)?one:0u);
    asm volatile("":"+v"(sw),"+v"(sh),"+v"(ta),"+v"(tv),"+v"(xw),"+v"(i01),"+v"(i23));
    const __amdgpu_buffer_rsrc_t xr=__builtin_amdgcn_make_buffer_rsrc(p.ws+WS_B,0,72*32*WS_BT,0x00020000);
    WAIT3(10,sk0,sk1,ep);
    unsigned tag=ep*2891336453u+1013904223u;
    sk0<<=16*(lane&1); sk1<<=16*(lane&1);
    v4u sl0=b_diag(di,(i16<8)?sk0:0u), sh0=b_diag(di,(i16<8)?0u:sk0), sl1=b_diag(di,(i16<8)?sk1:0u), sh1=b_diag(di,(i16<8)?0u:sk1);
    asm volatile("":"+v"(tag),"+v"(sl0),"+v"(sh0),"+v"(sl1),"+v"(sh1));
    WAIT1(5,halo);
    NSTAMP(11);
    if(ht<0) halo=(v4u){0,0,0,0};
    #define STAGE(b,sl,n) { WAIT4(n,k0[b],k1[b],v0[b],v1[b]); \
        st128(sw+KO(sl),k0[b]); st128(sw+KO(sl)+512,k1[b]); st128(sw+VO(sl),v0[b]); st128(sw+VO(sl)+512,v1[b]); \
        if((lane>>3)==(b)) st128(sh+KO(sl)-64,halo); }
    #define SYNC(n) { WAITL; *(volatile LAS unsigned*)(B_SCR+4*w)=(n); \
        _Pragma("unroll 1") for(int spin=0;spin<(1<<24);spin++){ if(__builtin_amdgcn_ballot_w64(*(volatile LAS unsigned*)(B_SCR+4*(lane&3))<(n))==0) break; __builtin_amdgcn_s_sleep(1); } asm volatile("":::"memory"); }
    #define PROD(b,sl) { \
        v4u oa,ob; b_prep2(ta+KO(sl)-64,tv+VO(sl),sl0,sh0,sl1,sh1,i01,i23,oa,ob); \
        __builtin_amdgcn_raw_buffer_store_b128(oa,xr,lane*16+po+(b)*8*WS_BT,0,16); __builtin_amdgcn_raw_buffer_store_b32(sum5(tag,oa),xr,1024+lane*4,po+(b)*8*WS_BT,16); \
        __builtin_amdgcn_raw_buffer_store_b128(ob,xr,lane*16+po+(32+(b)*8)*WS_BT,0,16); __builtin_amdgcn_raw_buffer_store_b32(sum5(tag,ob),xr,1024+lane*4,po+(32+(b)*8)*WS_BT,16); }
    #define RECV(b) { v4u oa,ob; _Pragma("unroll 1") for(int spin=0;spin<(1<<20);spin++){ asm volatile("":::"memory"); \
          oa=__builtin_amdgcn_raw_buffer_load_b128(xr,lane*16,co+(b)*8*WS_BT,16); ob=__builtin_amdgcn_raw_buffer_load_b128(xr,lane*16+WS_BT,co+(b)*8*WS_BT,16); \
          const unsigned ca=__builtin_amdgcn_raw_buffer_load_b32(xr,1024+lane*4,co+(b)*8*WS_BT,16), cb=__builtin_amdgcn_raw_buffer_load_b32(xr,1024+lane*4+WS_BT,co+(b)*8*WS_BT,16); \
          if(__builtin_amdgcn_ballot_w64(sum5(tag,oa)!=ca || sum5(tag,ob)!=cb)==0) break; __builtin_amdgcn_s_sleep(4); } \
        b_put(xw+1024*(3-(b))+128,oa); b_put(xw+1024*(3-(b)),ob); }
    __builtin_amdgcn_s_setprio(2);
    STAGE(0,1,5)
    SYNC(1)
    PROD(0,1)
    NSTAMP(12);
    __builtin_amdgcn_s_sleep(BSLP);
    RECV(0)
    NSTAMP(1);
    ldsbar();
    STAMP(2);
    LOADKV(2) LOADKV(3)
    BSTAMP(10);
    STAGE(1,0,8)
    SYNC(2)
    BSTAMP(11);
    PROD(1,0)
    BSTAMP(12);
    SYNC(3)
    STAGE(2,1,8)
    STAGE(3,0,4)
    BSTAMP(1);
    RECV(1)
    STAMP(13);
    TOUCH(p.q+w*BLK,(lane*8+j)*1152+cg*16);
    SYNC(4)
    PROD(2,1)
    PROD(3,0)
    STAMP(8);
    ldsbar();
    STAMP(3);
    LOADQ(0)
    BSTAMP(6);
    RECV(2)
    BSTAMP(7);
    RECV(3)
    BSTAMP(5);
    asm volatile(""::"v"(junk));
    LOADQ(1) LOADQ(2)
    WAIT2(4,q0[0],q1[0]);
    GATE(0,q0[0],q1[0])
    STAMP(9);
    ldsbar();
    STAMP(4);
    if(tid==256 && j==0) __hip_atomic_store(epoch,tag,__ATOMIC_RELAXED,__HIP_MEMORY_SCOPE_AGENT);
    WAIT4(2,q0[1],q1[1],q0[2],q1[2]);
    GATE(1,q0[1],q1[1])
    NSTAMP(5);
    ldsbar();
    NSTAMP(6);
    GATE(2,q0[2],q1[2])
    NSTAMP(7);
  }
}

constexpr int A_TM=90, A_SLOT=41984, A_WOFF=14336, A_IMG=83968, A_IROW=464, A_ZERO=136192, A_TAG=A_ZERO+288;
constexpr int WS_O=65536, WS_OM=19*1024, WS_OT=6*WS_OM;
typedef __amdgpu_buffer_rsrc_t rsrc_t;
DEV void dma16o(const v4u& srd, unsigned voff, unsigned soff, unsigned lds){
  asm volatile("s_mov_b32 m0, %3\n\ts_nop 0\n\tbuffer_load_dwordx4 %0, %1, %2 offen" NTO " lds"::"v"(voff),"s"(srd),"s"(soff),"s"(lds):"memory");
}
DEV void dma16(const v4u& srd, unsigned voff, unsigned soff, unsigned lds){
  asm volatile("s_mov_b32 m0, %3\n\ts_nop 0\n\tbuffer_load_dwordx4 %0, %1, %2 offen" NTX " lds"::"v"(voff),"s"(srd),"s"(soff),"s"(lds):"memory");
}
#define DW1(T) "s_add_u32 m0, %4, " T "\n\ts_lshl3_add_u32 %0, " T ", %3\n\tbuffer_load_dwordx4 %1, %2, %0 offen lds\n\t"
struct T8 { unsigned t[8]; };
DEV void dma_w8(const v4u& srd, unsigned voff, unsigned soff, unsigned lds, const T8& o){
  unsigned tmp;
  asm volatile("s_lshr_b64 exec, exec, 10\n\t"
               DW1("%5") DW1("%6") DW1("%7") DW1("%8") DW1("%9") DW1("%10") DW1("%11") DW1("%12") "s_mov_b64 exec, -1"
               :"=&s"(tmp):"v"(voff),"s"(srd),"s"(soff),"s"(lds),"s"(o.t[0]),"s"(o.t[1]),"s"(o.t[2]),"s"(o.t[3]),"s"(o.t[4]),"s"(o.t[5]),"s"(o.t[6]),"s"(o.t[7]):"memory","scc");
}
DEV v4u mksrd(const char* p, unsigned bytes){
  const u64 a=(u64)p; return (v4u){(unsigned)a,(unsigned)(a>>32)&0xffffu,bytes,0x00020000u};
}
struct AFr { v4u x[7]; v2u w[4][2]; };
struct AOp { v4u x[7], w[4]; };
template<int WO> DEV void a_rd(int i, AFr& f, unsigned xe, unsigned x0, unsigned xo, unsigned wa, unsigned we){
  switch(i){
    case 0: ATR(f.w[3][0],we,WO); break;      case 1: ATR(f.w[3][1],we,WO+432); break;
    case 2: ARD(f.x[0],xe,0); break;          case 3: ARD(f.x[1],xe,2048); break;   case 4: ARD(f.x[2],xe,4096); break;
    case 5: ATR(f.w[0][0],wa,WO); break;      case 6: ATR(f.w[0][1],wa,WO+432); break;
    case 7: ATR(f.w[1][0],wa,WO+32); break;   case 8: ATR(f.w[1][1],wa,WO+464); break;
    case 9: ATR(f.w[2][0],wa,WO+64); break;   case 10: ATR(f.w[2][1],wa,WO+496); break;
    case 11: ARD(f.x[3],x0,0); break;
    case 12: ARD(f.x[4],xo,0); break;         case 13: ARD(f.x[5],xo,2048); break;  case 14: ARD(f.x[6],xo,4096); break;
  }
}
DEV void a_wait(AFr& f, AOp& o){
  asm volatile("s_waitcnt lgkmcnt(0)":"+v"(f.x[0]),"+v"(f.x[1]),"+v"(f.x[2]),"+v"(f.x[3]),"+v"(f.x[4]),"+v"(f.x[5]),"+v"(f.x[6]),
     "+v"(f.w[0][0]),"+v"(f.w[0][1]),"+v"(f.w[1][0]),"+v"(f.w[1][1]),"+v"(f.w[2][0]),"+v"(f.w[2][1]),"+v"(f.w[3][0]),"+v"(f.w[3][1])::"memory");
  #pragma unroll
  for(int m=0;m<7;m++) o.x[m]=f.x[m];
  #pragma unroll
  for(int c=0;c<4;c++) o.w[c]=cat(f.w[c][0],f.w[c][1]);
}
DEV void a_mm(int i, f4 (&acc)[3][7], f4 (&ace)[3], const AOp& f){
  if(i<3) AMM(ace[i],f.w[3],f.x[i]); else AMM(acc[(i-3)%3][(i-3)/3],f.w[(i-3)%3],f.x[(i-3)/3]);
}
DEV float max3(float a,float b,float c){ float d; asm("v_max3_f32 %0, %1, %2, %3":"=v"(d):"v"(a),"v"(b),"v"(c)); return d; }
DEV float max2(float a,float b){ float d; asm("v_max_f32 %0, %1, %2":"=v"(d):"v"(a),"v"(b)); return d; }
DEV void tr4(unsigned (&a)[4]){
  auto r0=__builtin_amdgcn_permlane16_swap(a[0],a[1],false,false);
  auto r1=__builtin_amdgcn_permlane16_swap(a[2],a[3],false,false);
  auto s0=__builtin_amdgcn_permlane32_swap(r0[0],r1[0],false,false);
  auto s1=__builtin_amdgcn_permlane32_swap(r0[1],r1[1],false,false);
  a[0]=s0[0]; a[1]=s1[0]; a[2]=s0[1]; a[3]=s1[1];
}

DEV unsigned rs4(unsigned a, unsigned b, unsigned c, unsigned d){
  auto x=__builtin_amdgcn_permlane16_swap(a,b,false,false);
  auto y=__builtin_amdgcn_permlane16_swap(c,d,false,false);
  auto z=__builtin_amdgcn_permlane32_swap(x[0]+x[1],y[0]+y[1],false,false);
  return z[0]+z[1];
}
DEV void role_a(const Params& p, int rt, int grp, int rot, int pro STAMPARG){
  const int tid=__builtin_amdgcn_workitem_id_x(), lane=tid&63, wave=__builtin_amdgcn_readfirstlane(tid>>6);
  const int row0=rt*A_TM, nrow=imin(A_TM,2048-row0);
  const int i16=lane&15, g=lane>>4;
  GAS unsigned* epoch=(GAS unsigned*)(p.ws+rt*256);
  unsigned vl[5]={0,0,0,0,0};
  unsigned ka,kb,qb,va,sel0,sel1,sel2,sel3;
  const int par=i16&1; const bool cur0=(4*g+par<=i16), cur1=(4*g+par+2<=i16);
  #define A_CONSTS { ka=A_IMG+(16*wave+i16)*A_IROW+8*g; kb=(g==0)?ka+32:A_ZERO-144; qb=(g==0)?ka+32+16*A_IROW:A_ZERO; \
    va=A_IMG+(16*wave+4*g+(i16>>2))*A_IROW+288+(lane&3)*8; \
    const unsigned plo=par?0x01000c0cu:0x0c0c0100u, phi=par?0x03020c0cu:0x0c0c0302u, pzz=0x0c0c0c0cu; \
    sel0=cur0?pzz:plo; sel1=cur1?pzz:phi; sel2=cur0?plo:pzz; sel3=cur1?phi:pzz; \
    asm volatile("":"+v"(ka),"+v"(kb),"+v"(qb),"+v"(va),"+v"(sel0),"+v"(sel1),"+v"(sel2),"+v"(sel3)); }
  const rsrc_t wsr=__builtin_amdgcn_make_buffer_rsrc(p.ws+WS_O,0,23*WS_OT,0x00020000);
  if(wave>=4){
    const int lw=wave-4;
    const v4u sx=mksrd(p.x,2048*1152), sw=mksrd(p.wqkv,576*3456), so=mksrd(p.wo,576*1152);
    int st=rot;
    T8 ord;
    #pragma unroll
    for(int i=0;i<8;i++) ord.t[i]=((i+pro)&7)*3456;
    unsigned sl=lw*1024;
    const int l27=lane>=27? lane-27:lane, seg=(l27>=18)?2:(l27>=9?1:0);
    const unsigned vw=(lane>=27?4*3456:0)+seg*(1152-144)+l27*16+grp*144+lw*3456;
    dma_w8(sw,vw,st*64*3456,sl+A_WOFF-lw*160,ord);
    #define XP(i) (i)
    int cnt=0;
    #define NEXTST { cnt++; st=(cnt==8)?8:(cnt^rot); }
    unsigned vx[4];
    #pragma unroll
    for(int i=0;i<4;i++){
      const int ir=8*(lw+4*XP(i))+(lane>>3); int tok=row0-16+ir; tok=imax(tok,imax(row0-14,0)); tok=imin(tok,row0+nrow-1);
      vx[i]=tok*1152+(((lane&7)^((ir>>1)&7))<<4);
    }
    #define A_X1(i) if(lw<2 || XP(i)!=3) dma16(sx,vx[i],st*128,sl+XP(i)*4096);
    #define A_ISSUEX() do{ A_X1(0) A_X1(1) A_X1(2) A_X1(3) \
      NEXTST; sl=(sl>=2*A_SLOT)?sl-2*A_SLOT:sl+A_SLOT; }while(0)
    #define A_ISSUE() do{ dma_w8(sw,vw,st*64*3456,sl+A_WOFF-lw*160,ord); A_ISSUEX(); }while(0)
    A_ISSUEX();
    STAMP(10);
    {
      const int pr=rt/6, id=(rt-6*pr)*4+(grp&3), u=tid&127; const char* a;
      if(lw<2){
        const int per=(pr==3)?108:90, pw=(pr==3)?36:30;
        const int l=id*per+u, r=l/15, c=l-r*15, m=imin(id*pw+imin(u-per,pw-1),719), ro=m/5;
        a=(u<per)? p.wqkv+((pr*144+r)*3456+(c/5)*1152+(c%5)*128) : p.wo+((pr*144+ro)*1152+(m-ro*5)*128);
        a+=(grp>>2)*512;
      } else a=p.x+imin((row0+grp*12)*1152+imin(u,107)*128,2048*1152-128);
      asm volatile("s_mov_b32 m0, 0x27e00\n\ts_nop 0\n\tglobal_load_lds_dword %0, off"::"v"(a):"memory");
    }
    STAMP(11);
    dma_w8(sw,vw,st*64*3456,sl+A_WOFF-lw*160,ord);
    WAITV(9);
    BAR;
    A_ISSUEX();
    A_ISSUE();
    A_CONSTS
    unsigned vo[3];
    #pragma unroll
    for(int i=0;i<3;i++){
      const int ci=(i<2? lw+4*i : 8)*64+lane, rho=ci/9, cc=ci-rho*9;
      vo[i]=(32*(rho>>5)+16*(rho&1)+4*((rho>>3)&3)+((rho>>1)&3))*1152+grp*144+cc*16;
    }
    #pragma unroll
    for(int i=0;i<5;i++){
      const int ci=imin((lw&1)+2*i,8)*64+lane, rho=ci/9, cc=ci-rho*9;
      vl[i]=(72*(rho>>3)+64+4*(rho&1)+((rho>>1)&3))*1152+grp*144+cc*16;
    }
    asm volatile("":"+v"(vo[0]),"+v"(vo[1]),"+v"(vo[2]),"+v"(vl[0]),"+v"(vl[1]),"+v"(vl[2]),"+v"(vl[3]),"+v"(vl[4]));
    #define A_ISSUE_WO(P0,P1) do{ _Pragma("unroll 1") for(int P=(P0);P<(P1);P++){ \
        dma16o(so,vo[0],P*72*1152,P*9216+lw*1024); dma16o(so,vo[1],P*72*1152,P*9216+lw*1024+4096); \
        if((P&3)==lw) dma16o(so,vo[2],P*72*1152,P*9216+8192); } }while(0)
    #pragma unroll 1
    for(int n=1;n<8;n++){
      if(lw<2) WAITV(12); else WAITV(11);
      BAR;
      if(n<7) A_ISSUE();
    }
    A_ISSUE_WO(0,2);
    if(lw<2) WAITV(5); else WAITV(4);
    BAR;
    A_ISSUE_WO(2,5);
    BAR;
    A_ISSUE_WO(5,8);
    STAMP(2);
  } else {
    unsigned ep;
    asm volatile("global_load_dword %0, %1, %2 sc1":"=v"(ep):"v"(0),"s"(epoch):"memory");
    A_CONSTS
    f4 acc[3][7], ace[3];
    #pragma unroll
    for(int c=0;c<3;c++){ ace[c]=(f4){0,0,0,0};
      #pragma unroll
      for(int m=0;m<7;m++) acc[c][m]=(f4){0,0,0,0}; }
    const int e0=(wave&1)?4:1, o0=(wave&1)?1:4;
    const unsigned xl=i16*128+((g^(i16>>1))<<4);
    const unsigned wl=A_WOFF+(8*g+2*(i16>>2))*432+(lane&3)*8;
    unsigned ae0=xl+e0*2048, az0=xl, ao0=xl+o0*2048, ae1=ae0^64, az1=az0^64, ao1=ao0^64, wa=wl+(2+3*wave)*32, we=wl+(wave>>1)*32;
    const unsigned ia=A_IMG+i16*A_IROW+g*8;
    unsigned ie=ia+e0*16*A_IROW+(2+3*wave)*32, iz=ia+(2+3*wave)*32, io=ia+o0*16*A_IROW+(2+3*wave)*32, ix=ia+e0*16*A_IROW+(wave>>1)*32;
    asm volatile("":"+v"(ae0),"+v"(az0),"+v"(ao0),"+v"(ae1),"+v"(az1),"+v"(ao1),"+v"(wa),"+v"(we),"+v"(ie),"+v"(iz),"+v"(io),"+v"(ix));
    #pragma unroll
    for(int c=0;c<3;c++){ asm volatile("":"+v"(ace[c]));
      #pragma unroll
      for(int m=0;m<7;m++) asm volatile("":"+v"(acc[c][m])); }
    if(tid<72) st32(A_ZERO+tid*4,0u);
    WAIT1(0,ep);
    if(tid==0) st32(A_TAG,ep*2891336453u+1013904223u);
    int slot=0;
    AFr f0,f1; AOp g0,g1;
    ldsbar();
    STAMP(1);
    #pragma unroll
    for(int i=0;i<15;i++) a_rd<0>(i,f0,ae0,az0,ao0,wa,we);
    a_wait(f0,g0);
    #pragma unroll 1
    for(int s=0;s<8;s++){
      const int dl=(slot==2)?-2*A_SLOT:A_SLOT; slot=(slot==2)?0:slot+1;
      #pragma unroll
      for(int i=0;i<24;i++){
        a_mm(i,acc,ace,g0);
        if(i<15) a_rd<13824>(i,f1,ae1,az1,ao1,wa,we);
        if(i==15) AADD(ae0,dl); if(i==16) AADD(az0,dl); if(i==17) AADD(ao0,dl); if(i==18) AADD(wa,dl); if(i==19) AADD(we,dl);
      }
      a_wait(f1,g1);
      BAR;
      #pragma unroll
      for(int i=0;i<24;i++){
        a_mm(i,acc,ace,g1);
        if(i<15) a_rd<0>(i,f0,ae0,az0,ao0,wa,we);
        if(i==15) AADD(ae1,dl); if(i==16) AADD(az1,dl); if(i==17) AADD(ao1,dl);
      }
      a_wait(f0,g0);
    }
    #pragma unroll
    for(int i=0;i<24;i++){
      a_mm(i,acc,ace,g0);
      if(i<15) a_rd<13824>(i,f1,ae1,az1,ao1,wa,we);
    }
    a_wait(f1,g1);
    BAR;
    SB;
    #pragma unroll
    for(int m=0;m<3;m++) ace[m]=mfma(g1.w[3],g1.x[m],ace[m]);
    #pragma unroll
    for(int m=0;m<7;m++){
      #pragma unroll
      for(int c=0;c<3;c++) acc[c][m]=mfma(g1.w[c],g1.x[m],acc[c][m]);
    }
    #pragma unroll
    for(int m=0;m<3;m++) st64(ix+m*16*A_IROW,(v2u){pk2(ace[m][0],ace[m][1]),pk2(ace[m][2],ace[m][3])});
    #pragma unroll
    for(int m=0;m<7;m++){
      const unsigned ib=(m<3)?ie+m*16*A_IROW:(m==3?iz:io+(m-4)*16*A_IROW);
      #pragma unroll
      for(int c=0;c<3;c++) st64v(ib+c*32,(v2u){pk2(acc[c][m][0],acc[c][m][1]),pk2(acc[c][m][2],acc[c][m][3])});
    }
    SGB(0x8,3);
    #pragma unroll
    for(int i=0;i<21;i++){ SGB(0x8,1); SGB(0x2,2); SGB(0x200,1); }
    SB;
    STAMP(2);
  }
  ldsbar();
  STAMP(3);
  const unsigned tag=ld32(A_TAG);
  if(wave>=6){
    const v4u so=mksrd(p.wo,576*1152);
    #pragma unroll
    for(int i=0;i<5;i++) if(i<4 || wave==6) dma16o(so,vl[i],0,8*9216+(wave&1)*1024+i*2048);
    WAITV(0);
  } else {
    v4u qf[4],kp[4],kc[4],vf[5];
    #pragma unroll
    for(int h=0;h<4;h++){
      const int hb=36*h-4*(h&1);
      qf[h]=cat(ld64v(ka+16*A_IROW+hb),ld64v(qb+hb));
      kp[h]=cat(ld64v(ka+144+hb),ld64v(kb+144+hb));
      kc[h]=cat(ld64v(ka+16*A_IROW+144+hb),ld64v(qb+144+hb));
    }
    #pragma unroll
    for(int c=0;c<5;c++) vf[c]=cat(ldtr(va+32*c),ldtr(va+16*A_IROW+32*c));
    SB;
    f4 sp[4],sc[4];
    #pragma unroll
    for(int h=0;h<4;h++){
      v4u q=qf[h];
      if(h&1) q.x=(g==0)?0u:q.x; else q.w=0u;
      sp[h]=mfma(kp[h],q,(f4){0,0,0,0}); sc[h]=mfma(kc[h],q,(f4){0,0,0,0});
    }
    SB;
    STAMP(13);
    unsigned a0[4],a1[4];
    #pragma unroll
    for(int h=0;h<4;h++){
      const float c0=sc[h][0],c1=sc[h][1],c2=sc[h][2],c3=sc[h][3],p0=sp[h][0],p1=sp[h][1],p2=sp[h][2],p3=sp[h][3];
      const float ca=par?c1:c0, pa=par?p1:p0, cb=par?c3:c2, pb=par?p3:p2;
      a0[h]=__builtin_bit_cast(unsigned,cur0?ca:pa);
      a1[h]=__builtin_bit_cast(unsigned,cur1?cb:pb);
    }
    if(row0+16*wave==0){
      const unsigned mk=__builtin_bit_cast(unsigned,-1e9f/0.23570226039551584f);
      #pragma unroll
      for(int h=0;h<4;h++){ a0[h]=cur0?a0[h]:mk; a1[h]=cur1?a1[h]:mk; }
    }
    tr4(a0); tr4(a1);
    float l[8];
    #pragma unroll
    for(int i=0;i<4;i++){ l[2*i]=__builtin_bit_cast(float,a0[i]); l[2*i+1]=__builtin_bit_cast(float,a1[i]); }
    constexpr float SC=0.23570226039551584f*1.4426950408889634f;
    const float mx=-SC*max3(max3(l[0],l[1],l[2]),max3(l[3],l[4],l[5]),max2(l[6],l[7]));
    #pragma unroll
    for(int i=0;i<8;i++) l[i]=__builtin_amdgcn_exp2f(__builtin_fmaf(l[i],SC,mx));
    const float inv=__builtin_amdgcn_rcpf(((l[0]+l[1])+(l[2]+l[3]))+((l[4]+l[5])+(l[6]+l[7])));
    unsigned pp[4];
    #pragma unroll
    for(int i=0;i<4;i++) pp[i]=pk2(l[2*i]*inv,l[2*i+1]*inv);
    tr4(pp);
    v4u pf[4];
    #pragma unroll
    for(int h=0;h<4;h++) pf[h]=(v4u){__builtin_amdgcn_perm(0u,pp[h],sel0),__builtin_amdgcn_perm(0u,pp[h],sel1),__builtin_amdgcn_perm(0u,pp[h],sel2),__builtin_amdgcn_perm(0u,pp[h],sel3)};
    const f4 z={0,0,0,0};
    const f4 o0=mfma(vf[0],pf[0],z), a1_=mfma(vf[1],pf[0],z), b1=mfma(vf[1],pf[1],z), a2=mfma(vf[2],pf[1],z),
             b2_=mfma(vf[2],pf[2],z), a3=mfma(vf[3],pf[2],z), b3=mfma(vf[3],pf[3],z), o4=mfma(vf[4],pf[3],z);
    const v4u t01={pk2(o0[0],o0[1]),pk2(o0[2],o0[3]), (g==0)?pk2(a1_[0],a1_[1]):pk2(b1[0],b1[1]), pk2(b1[2],b1[3])};
    const v4u t23={(g==0)?pk2(a2[0],a2[1]):pk2(b2_[0],b2_[1]), (g==0)?pk2(a2[2],a2[3]):pk2(b2_[2],b2_[3]),
                   (g<2)?pk2(a3[0],a3[1]):pk2(b3[0],b3[1]), (g==0)?pk2(a3[2],a3[3]):pk2(b3[2],b3[3])};
    const unsigned u0=pk2(o4[0],o4[1]), u1=pk2(o4[2],o4[3]);
    auto r0=__builtin_amdgcn_permlane16_swap(u0,u0,false,false);
    auto r1=__builtin_amdgcn_permlane16_swap(u1,u1,false,false);
    STAMP(12);
    const unsigned ob=rt*WS_OT+wave*WS_OM;
    __builtin_amdgcn_raw_buffer_store_b128(t01,wsr,ob+2*grp*1024+lane*16,0,16);
    __builtin_amdgcn_raw_buffer_store_b128(t23,wsr,ob+2*grp*1024+1024+lane*16,0,16);
    const v4u t4={(unsigned)r0[0],(unsigned)r1[0],(unsigned)r0[1],(unsigned)r1[1]};
    unsigned hs=sum5(sum5(0,t01),t23);
    { auto x=__builtin_amdgcn_permlane16_swap(hs,hs,false,false); hs=x[0]+x[1];
      auto y=__builtin_amdgcn_permlane32_swap(hs,hs,false,false); hs=y[0]+y[1]; }
    if(g==0){
      __builtin_amdgcn_raw_buffer_store_b128(t4,wsr,ob+16*1024+grp*256+lane*16,0,16);
      __builtin_amdgcn_raw_buffer_store_b64((v2u){sum5(hs,t4)+tag,tag},wsr,ob+18*1024+grp*128+lane*8,0,16);
    }
    WAITV(4);
  }
  STAMP(4);
  BAR;
  STAMP(5);
  {
    const int m=(wave<4)?wave:4+((wave-4)>>1);
    const unsigned wa=(8*g+2*(i16>>2))*144+(lane&3)*8;
    const unsigned ob=rt*WS_OT+m*WS_OM+lane*16, rb=rt*WS_OT+m*WS_OM+18*1024+lane*8;
    const unsigned red=A_IMG+((wave>>1)&1)*5120+lane*16, rf=A_ZERO+272+((wave>>1)&1)*4;
    int l9=lane/9, c9=lane-l9*9;
    asm volatile("":"+v"(l9),"+v"(c9));
    f4 acc[5];
    #define OLD(ks) __builtin_amdgcn_raw_buffer_load_b128(wsr,ob,(ks)*1024,16)
    #define RLD(st) __builtin_amdgcn_raw_buffer_load_b64(wsr,rb,(st)*512,16)
    #define WFR(a,ks,c) cat(ldtr((a)+(ks)*4608+(c)*32),ldtr((a)+(ks)*4608+(c)*32+144))
    #define MEMF asm volatile("":::"memory")
    if(wave<4){
      constexpr int NPRE=PNPRE, NW=9, NISS=PNISS, NF=PNF;
      #pragma unroll 1
      for(int tries=0;tries<64;tries++){
        v4u wf[NPRE][5], of[18]; v2u r0,r1;
        #pragma unroll
        for(int k=0;k<NPRE;k++){
          #pragma unroll
          for(int c=0;c<5;c++) wf[k][c]=WFR(wa,k,c);
          if(k==NISS){ SB; MEMF; r0=RLD(0); r1=RLD(1); SB;
            #pragma unroll
            for(int j=0;j<NF;j++){ of[j]=OLD(j); SB; } }
        }
        SB;
        #pragma unroll 1
        for(int spin=0;spin<(1<<20);spin++){
          if(__builtin_amdgcn_ballot_w64(r0.y!=tag || r1.y!=tag)==0) break;
          MEMF; r0=RLD(0); r1=RLD(1); SB;
          #pragma unroll
          for(int j=0;j<NF;j++){ of[j]=OLD(j); SB; }
        }
        #pragma unroll
        for(int j=NF;j<NW;j++){ of[j]=OLD(j); SB; }
        STAMP(6);
        #pragma unroll
        for(int c=0;c<5;c++) acc[c]=(f4){0,0,0,0};
        unsigned hs[10];
        #pragma unroll
        for(int k=0;k<18;k++){
          #pragma unroll
          for(int c=0;c<5;c++){
            acc[c]=mfma(wf[k%NPRE][c],of[k],acc[c]);
            if(k+NPRE<18){ wf[k%NPRE][c]=WFR(wa,k+NPRE,c); SGB(0x8,1); SGB(0x100,2); }
          }
          if(k>=16) hs[k-8]=sum5(tag,of[k]); else hs[k>>1]=sum5((k&1)?hs[k>>1]:0u,of[k]);
          if(k==7) hs[0]=rs4(hs[0],hs[1],hs[2],hs[3]);
          if(k==15) hs[4]=rs4(hs[4],hs[5],hs[6],hs[7]);
          SB;
          if(k+NW<18){ of[k+NW]=OLD(k+NW); SB; }
        }
        if(__builtin_amdgcn_ballot_w64(hs[0]+hs[8]!=r0.x || hs[4]+hs[9]!=r1.x)==0) break;
      }
      STAMP(7);
    } else {
      const int hf=wave&1;
      const unsigned wb=wa+hf*8*4608, wc=wa+(16+hf)*4608;
      #pragma unroll 1
      for(int tries=0;tries<64;tries++){
        v4u wf[9][5], of[9]; v2u r0;
        #pragma unroll
        for(int k=0;k<8;k++){
          #pragma unroll
          for(int c=0;c<5;c++) wf[k][c]=WFR(wb,k,c);
          if(k==PNISS){ SB; MEMF; r0=RLD(hf); SB;
            #pragma unroll
            for(int j=0;j<PNF;j++){ of[j]=OLD(8*hf+j); SB; } }
        }
        #pragma unroll
        for(int c=0;c<5;c++) wf[8][c]=WFR(wc,0,c);
        SB;
        #pragma unroll 1
        for(int spin=0;spin<(1<<20);spin++){
          if(__builtin_amdgcn_ballot_w64(r0.y!=tag)==0) break;
          MEMF; r0=RLD(hf); SB;
          #pragma unroll
          for(int j=0;j<PNF;j++){ of[j]=OLD(8*hf+j); SB; }
        }
        #pragma unroll
        for(int j=PNF;j<9;j++){ of[j]=OLD(j<8?8*hf+j:16+hf); SB; }
        __builtin_amdgcn_s_setprio(1);
        STAMP(6);
        #pragma unroll
        for(int c=0;c<5;c++) acc[c]=(f4){0,0,0,0};
        unsigned hs[5];
        #pragma unroll
        for(int k=0;k<9;k++){
          #pragma unroll
          for(int c=0;c<5;c++) acc[c]=mfma(wf[k][c],of[k],acc[c]);
          hs[k>>1]=sum5((k==8)?tag:((k&1)?hs[k>>1]:0u),of[k]);
          SB;
        }
        if(__builtin_amdgcn_ballot_w64(rs4(hs[0],hs[1],hs[2],hs[3])+hs[4]!=r0.x)==0) break;
      }
      if(hf){
        #pragma unroll
        for(int c=0;c<5;c++) st128(red+c*1024,__builtin_bit_cast(v4u,acc[c]));
        WAITL;
        *(volatile LAS unsigned*)rf=1u;
      } else {
        for(int spin=0;spin<(1<<24);spin++) if(__builtin_amdgcn_readfirstlane(*(volatile LAS unsigned*)rf)) break;
        asm volatile("":::"memory");
        #pragma unroll
        for(int c=0;c<5;c++) acc[c]+=__builtin_bit_cast(f4,ld128(red+c*1024));
      }
      STAMP(7);
    }
    if(wave<4 || !(wave&1)){
      const unsigned tb=A_IMG+10240+wave*2560;
      #pragma unroll
      for(int c=0;c<5;c++) st64(tb+i16*160+g*8+c*32,(v2u){pk2(acc[c][0],acc[c][1]),pk2(acc[c][2],acc[c][3])});
      #pragma unroll
      for(int i=0;i<3;i++){
        const int r=7*i+l9;
        if(lane<63 && r<16 && m*16+r<nrow) *(GAS v4u*)(p.out+(size_t)(row0+m*16+r)*2304+grp*144+c9*16)=ld128(tb+r*160+c9*16);
      }
    }
    asm volatile(".Lcpf1:");
    if(tid==0 && grp==0) __hip_atomic_store(epoch,tag,__ATOMIC_RELAXED,__HIP_MEMORY_SCOPE_AGENT);
  }
  STAMP(8);
}

NOPK __global__ __launch_bounds__(512,2) void fused(Params p){
  STAMPDECL;
  STAMP(0);
  const int b=__builtin_amdgcn_workgroup_id_x(), xcd=b&7, j=b>>3, pr=xcd>>1, half=xcd&1;
  const int nA=(pr<3)?24:20;
  {
    u64 pc; unsigned len;
    asm volatile("s_getpc_b64 %0\n.Lcpf0:\n\ts_mov_b32 %1, .Lcpf1-.Lcpf0":"=s"(pc),"=s"(len));
    const unsigned t=__builtin_amdgcn_workitem_id_x();
    if(t<40){ unsigned o=((j&7)+t*8)*128; o=o<len?o:len;
      asm volatile("s_mov_b32 m0, 0x27f00\n\ts_nop 0\n\tglobal_load_lds_dword %0, %1"::"v"(o&~3u),"s"(pc):"memory"); }
  }
  if(j>=nA){
    const int jb=j-nA;
    role_b(p, jb<8? xcd*8+jb : 64+half*4+(jb-8) STAMPPASS);
  } else
    { const int grp=half*4+(j&3);
    int rot=((j>>2)+grp)&1;
    const int pro=0;
    role_a(p, 6*pr+(j>>2), grp, rot, pro STAMPPASS); }
}

constexpr int LDS_BYTES=160*1024;
extern "C" void kernel_launch(void* const* d_in, const int* in_sizes, int n_in, void* d_out, int out_size, void* d_ws, size_t ws_size, hipStream_t stream){
  Params p;
  p.x=(const char*)d_in[0]; p.wqkv=(const char*)d_in[1]; p.wo=(const char*)d_in[2];
  p.q=(const char*)d_in[3]; p.k=(const char*)d_in[4]; p.v=(const char*)d_in[5];
  p.sk=(const char*)d_in[6]; p.hk=(const char*)d_in[7]; p.dv=(const char*)d_in[8];
  p.out=(char*)d_out; p.ws=(char*)d_ws;
  (void)hipFuncSetAttribute((const void*)fused,hipFuncAttributeMaxDynamicSharedMemorySize,LDS_BYTES);
  fused<<<256,512,LDS_BYTES,stream>>>(p);
}
```

```cpp
#include <hip/hip_runtime.h>
#include <hip/hip_bf16.h>

typedef unsigned short u16;
typedef unsigned long long u64;
typedef unsigned v4u __attribute__((ext_vector_type(4)));
typedef unsigned v2u __attribute__((ext_vector_type(2)));
typedef short s4 __attribute__((ext_vector_type(4)));
typedef __bf16 b2 __attribute__((ext_vector_type(2)));
typedef __bf16 b8 __attribute__((ext_vector_type(8)));
typedef float f4 __attribute__((ext_vector_type(4)));
typedef float f2 __attribute__((ext_vector_type(2)));
#define LAS __attribute__((address_space(3)))
#define GAS __attribute__((address_space(1)))
#if defined(__HIP_DEVICE_COMPILE__)
#define NOPK __attribute__((target("no-packed-fp32-ops")))
#else
#define NOPK
#endif
#define DEV __device__ __forceinline__ NOPK
#define SB __builtin_amdgcn_sched_barrier(0)
#define SGB(m,n) __builtin_amdgcn_sched_group_barrier(m,n,0)
#pragma clang diagnostic ignored "-Wint-to-pointer-cast"

#define STAMP(i)
#define STAMPDECL
#define STAMPARG
#define STAMPPASS
#define STAMPOUT

#define PNPRE 6
#define PNISS 3
#define PNF 4
#define ALOOK 2
#define NOAT
#define ROT 22
#define NOPR
#define BV 1
#define CPFN 32
#define NTB " nt"
#define NTQ ""
#define NTX ""
#define NTW ""
#define NTO ""
#define BSTAMP(i)
#define NSTAMP(i) STAMP(i)
#define BSLP 16
#define MTS 0
#define WOV 2
#define STV 1
#define NH 4
#define STA ""
#define STB ""
struct Params {
  const char *x, *wqkv, *wo, *q, *k, *v, *sk, *hk, *dv;
  char *out, *ws;
};

DEV float lo2f(unsigned d){ return __builtin_bit_cast(float,d<<16); }
DEV float hi2f(unsigned d){ return __builtin_bit_cast(float,d&0xffff0000u); }
DEV unsigned pk2(float lo, float hi){ f2 p={lo,hi}; return __builtin_bit_cast(unsigned,__builtin_convertvector(p,b2)); }
DEV unsigned pk2a(float lo, float hi){ unsigned d; asm("v_cvt_pk_bf16_f32 %0, %1, %2":"=v"(d):"v"(lo),"v"(hi)); return d; }
DEV v4u ld128(unsigned a){ return *(LAS v4u*)a; }
DEV v2u ld64v(unsigned a){ return *(volatile LAS v2u*)a; }
DEV unsigned ld32(unsigned a){ return *(LAS unsigned*)a; }
DEV void st128(unsigned a, v4u v){ *(LAS v4u*)a=v; }
DEV void st64(unsigned a, v2u v){ *(LAS v2u*)a=v; }
DEV void st64v(unsigned a, v2u v){ *(volatile LAS v2u*)a=v; }
DEV void st32(unsigned a, unsigned v){ *(LAS unsigned*)a=v; }
DEV v2u ldtr(unsigned a){ return __builtin_bit_cast(v2u,__builtin_amdgcn_ds_read_tr16_b64_v4i16((LAS s4*)a)); }
DEV f4 mfma(v4u a, v4u b, f4 c){ return __builtin_amdgcn_mfma_f32_16x16x32_bf16(__builtin_bit_cast(b8,a),__builtin_bit_cast(b8,b),c,0,0,0); }
DEV v4u cat(v2u a, v2u b){ return (v4u){a.x,a.y,b.x,b.y}; }
DEV v4u gld128(const char* p){ return *(const GAS v4u*)p; }
DEV void gloadq(v4u& d, const char* sbase, unsigned voff){ asm volatile("global_load_dwordx4 %0, %1, %2" NTQ:"=v"(d):"v"(voff),"s"(sbase):"memory"); }
DEV void gload(v4u& d, const char* sbase, unsigned voff){ asm volatile("global_load_dwordx4 %0, %1, %2" NTB:"=v"(d):"v"(voff),"s"(sbase):"memory"); }
DEV void greload(v4u& d, const char* sbase, unsigned voff){
 asm volatile("global_load_dwordx4 %0, %1, %2":"+v"(d):"v"(voff),"s"(sbase):"memory"); }
#define WAIT1(n,a) asm volatile("s_waitcnt vmcnt(" #n ")":"+v"(a)::"memory")
#define WAIT2(n,a,b) asm volatile("s_waitcnt vmcnt(" #n ")":"+v"(a),"+v"(b)::"memory")
#define WAIT3(n,a,b,c) asm volatile("s_waitcnt vmcnt(" #n ")":"+v"(a),"+v"(b),"+v"(c)::"memory")
#define WAIT4(n,a,b,c,d) asm volatile("s_waitcnt vmcnt(" #n ")":"+v"(a),"+v"(b),"+v"(c),"+v"(d)::"memory")
#define WAITL asm volatile("":::"memory"); __builtin_amdgcn_s_waitcnt(0xC07F); asm volatile("":::"memory")
DEV void ldsbar(){ WAITL; __builtin_amdgcn_s_barrier(); asm volatile("":::"memory"); }
DEV int imin(int a,int b){ return a<b?a:b; }
DEV int imax(int a,int b){ return a>b?a:b; }
#define WAITV(n) asm volatile("s_waitcnt vmcnt(" #n ")":::"memory")
#define BAR do{ asm volatile("":::"memory"); __builtin_amdgcn_s_barrier(); asm volatile("":::"memory"); }while(0)
template<int NR,int NM> DEV void interleave(){
  constexpr int PER=(NR+NM-1)/NM, NG=(NR+PER-1)/PER;
  #pragma unroll
  for(int i=0;i<NG;i++){ SGB(0x8,1); SGB(0x100,PER); }
  if(NM>NG) SGB(0x8,NM-NG);
}

#define AMM(acc,a,b) asm volatile("v_mfma_f32_16x16x32_bf16 %0, %1, %2, %0":"+v"(acc):"v"(a),"v"(b))
#define ARD(d,a,off) asm volatile("ds_read_b128 %0, %1 offset:%2":"=v"(d):"v"(a),"i"(off):"memory")
#define ATR(d,a,off) asm volatile("ds_read_b64_tr_b16 %0, %1 offset:%2":"=v"(d):"v"(a),"i"(off):"memory")
#define AADD(v,s) asm volatile("v_add_u32 %0, %1, %0":"+v"(v):"s"(s))
#define AR64(d,a,off) asm volatile("ds_read_b64 %0, %1 offset:%2":"=v"(d):"v"(a),"i"(off):"memory")
constexpr int B_HA=448, B_HB=4576+448, B_X0=9120, B_X1=13344, B_ST=17520, B_PITCH=19696, B_SCR=8*B_PITCH;

template<int BLO,int NB> struct PF { v4u h[8-BLO]; v2u x[NB][2]; };
template<int BLO,int NB,int OFF> DEV void p_rd(int i, PF<BLO,NB>& f, unsigned hb, unsigned xb){
  #pragma unroll
  for(int a=0;a<8-BLO;a++) if(i==2+a) ARD(f.h[a],hb,512*a+OFF);
  #pragma unroll
  for(int b=0;b<NB;b++){ const int q=b?8-BLO+2*b:0; if(i==q) AR64(f.x[b][0],xb,512*(7-BLO-b)+OFF); if(i==q+1) AR64(f.x[b][1],xb,512*(7-BLO-b)+8+OFF); }
}
template<int BLO,int NB> DEV void p_mm(int i, f4 (&acc)[8], PF<BLO,NB>& f){
  int n=0;
  #pragma unroll
  for(int b=0;b<NB;b++){
    #pragma unroll
    for(int a=0;a<8-BLO-b;a++){ if(n==i){ v4u x=cat(f.x[b][0],f.x[b][1]); AMM(acc[a+b+BLO],f.h[a],x); } n++; }
  }
}
template<int N,int BLO,int NB> DEV void p_wait(PF<BLO,NB>& f){
  asm volatile("s_waitcnt lgkmcnt(%0)"::"i"(N):"memory");
  #pragma unroll
  for(int a=0;a<8-BLO;a++) asm volatile("":"+v"(f.h[a]));
  #pragma unroll
  for(int b=0;b<NB;b++) asm volatile("":"+v"(f.x[b][0]),"+v"(f.x[b][1]));
}
template<int BLO,int NB,int OFF> DEV void p_block(f4 (&acc)[8], PF<BLO,NB>& fc, PF<BLO,NB>& fn, unsigned hb, unsigned xb){
  constexpr int NR=8-BLO+2*NB, NM=NB*(8-BLO)-NB*(NB-1)/2, EX=(NR>NM)?NR-NM:0;
  constexpr int I2=(NB==2? 6-BLO : (NB-2)*(8-BLO)-(NB-2)*(NB-3)/2);
  p_wait<4>(fc);
  #pragma unroll
  for(int i=0;i<NM;i++){
    if(i==I2) asm volatile("s_waitcnt lgkmcnt(%0)"::"i"(I2+EX<NR?I2+EX:NR):"memory");
    p_mm<BLO,NB>(i,acc,fc);
    if(i<EX){ p_rd<BLO,NB,OFF>(2*i,fn,hb,xb); p_rd<BLO,NB,OFF>(2*i+1,fn,hb,xb); }
    else if(i+EX<NR) p_rd<BLO,NB,OFF>(i+EX,fn,hb,xb);
  }
}
template<int BLO,int NB> DEV void b_seg(f4 (&acc)[8], PF<BLO,NB>& f0, unsigned hb, unsigned xb, int next){
  PF<BLO,NB> f1;
  #pragma unroll 1
  for(int r=0;r<4;r++){
    p_block<BLO,NB,64>(acc,f0,f1,hb,xb);
    const int d=(r==3)? next-384 : 128;
    AADD(hb,d); AADD(xb,d);
    p_block<BLO,NB,0>(acc,f1,f0,hb,xb);
  }
}
template<int JLO,int NJ> DEV void b_zout(f4 (&acc)[8], unsigned za){
  asm volatile("s_nop 7\n\ts_nop 3":::"memory");
  #pragma unroll
  for(int J=JLO;J<JLO+NJ;J++){
    asm volatile("":"+v"(acc[J]));
    st32(za+512*(7-J)+64,pk2a(acc[J][1],acc[J][0])); st32(za+512*(7-J),pk2a(acc[J][3],acc[J][2]));
  }
}
template<int BLO,int NB> DEV void b_stage(f4 (&a0)[8], f4 (&a1)[8], unsigned hb, unsigned xb, unsigned za){
  PF<BLO,NB> f;
  #pragma unroll
  for(int i=2;i<10-BLO;i++) p_rd<BLO,NB,0>(i,f,hb,xb);
  ldsbar();
  p_rd<BLO,NB,0>(0,f,hb,xb); p_rd<BLO,NB,0>(1,f,hb,xb);
  #pragma unroll
  for(int i=10-BLO;i<8-BLO+2*NB;i++) p_rd<BLO,NB,0>(i,f,hb,xb);
  asm volatile("s_waitcnt lgkmcnt(%0)"::"i"(2*NB-2):"memory");
  b_seg<BLO,NB>(a0,f,hb,xb,B_PITCH);
  b_zout<BLO,NB>(a0,za);
  b_seg<BLO,NB>(a1,f,hb+B_PITCH,xb+B_PITCH,0);
  b_zout<BLO,NB>(a1,za+B_PITCH);
  p_wait<0>(f);
}

DEV unsigned sum5(unsigned m, v4u v){ return (m+v.x+v.y)+v.z+v.w; }
DEV void b_prep2(unsigned ta, unsigned tv, v4u sl0, v4u sh0, v4u sl1, v4u sh1, v4u i01, v4u i23, v4u& oa, v4u& ob){
  v4u a[2][4], vr[2];
  #pragma unroll
  for(int t=0;t<2;t++){ vr[t]=ld128(tv+t*B_PITCH); a[t][0]=ld128(ta+t*B_PITCH); a[t][2]=ld128(ta+t*B_PITCH+64); }
  #pragma unroll
  for(int t=0;t<2;t++){ a[t][1]=ld128(ta+t*B_PITCH+512); a[t][3]=ld128(ta+t*B_PITCH+576); }
  SB;
  const f4 z={0,0,0,0};
  f4 v1[2],v2[2],d1[2],d2[2];
  #pragma unroll
  for(int t=0;t<2;t++){ v1[t]=mfma(vr[t],i01,z); v2[t]=mfma(vr[t],i23,z); d1[t]=mfma(a[t][0],t?sl1:sl0,z); d2[t]=mfma(a[t][2],t?sl1:sl0,z); }
  #pragma unroll
  for(int t=0;t<2;t++){ d1[t]=mfma(a[t][1],t?sh1:sh0,d1[t]); d2[t]=mfma(a[t][3],t?sh1:sh0,d2[t]); }
  SB;
  #pragma unroll
  for(int t=0;t<2;t++){
    const v4u o={pk2(d2[t][3]*v2[t][3],d2[t][2]*v2[t][2]),pk2(d2[t][1]*v2[t][1],d2[t][0]*v2[t][0]),pk2(d1[t][3]*v1[t][3],d1[t][2]*v1[t][2]),pk2(d1[t][1]*v1[t][1],d1[t][0]*v1[t][0])};
    if(t) ob=o; else oa=o;
  }
}
DEV void b_put(unsigned a, v4u o){
  st128(a+B_X0,o);
  st32(a+B_X1+4,o.x); st64(a+B_X1+8,(v2u){o.y,o.z}); st32(a+B_X1+16,o.w);
}
DEV v4u b_diag(int di, unsigned e){ return (v4u){di==0?e:0u,di==1?e:0u,di==2?e:0u,di==3?e:0u}; }
constexpr int WS_B=4<<20, WS_BT=1280;
DEV void b_gate(unsigned ga, unsigned gb, v4u q, const float (&dd)[8], char* outp){
  const v2u y0=ldtr(ga), x0=ldtr(ga+B_X0-B_HA), y1=ldtr(gb), x1=ldtr(gb+B_X0-B_HA);
  #define G2(Q,Y,X,i) pk2(lo2f(Q)*__builtin_fmaf(lo2f(X),dd[i],lo2f(Y)), hi2f(Q)*__builtin_fmaf(hi2f(X),dd[i+1],hi2f(Y)))
  const v4u o={G2(q.x,y0.x,x0.x,0),G2(q.y,y0.y,x0.y,2),G2(q.z,y1.x,x1.x,4),G2(q.w,y1.y,x1.y,6)};
  asm volatile("global_store_dwordx4 %0, %1, off" STB "\n\ts_nop 1"::"v"(outp),"v"(o):"memory");
}

DEV void role_b(const Params& p, int cg STAMPARG){
  const int tid=__builtin_amdgcn_workitem_id_x(), lane=tid&63, wave=__builtin_amdgcn_readfirstlane(tid>>6);
  const int i16=lane&15, g=lane>>4;
  constexpr unsigned BLK=512*1152;
  const int w=wave&3, ql=lane^3;
  const unsigned rq=(w*128+ql)*1152+cg*16;
  unsigned ga,gx; char* outp; float dd[8];
  #define GCONST { const v4u dw=*(const __attribute__((address_space(4))) v4u*)(p.dv+cg*16); \
    dd[0]=lo2f(dw.x); dd[1]=hi2f(dw.x); dd[2]=lo2f(dw.y); dd[3]=hi2f(dw.y); dd[4]=lo2f(dw.z); dd[5]=hi2f(dw.z); dd[6]=lo2f(dw.w); dd[7]=hi2f(dw.w); \
    ga=(i16>>2)*B_PITCH+B_HA+(2044-1536-64-128*w-16*g-4*(lane&3))*2; gx=ga+4*B_PITCH; outp=p.out+(size_t)(w*128+ql)*2304+1152+cg*16; \
    asm volatile("":"+v"(ga),"+v"(gx),"+v"(outp)); }
  #define GATE(b,Q0,Q1) { b_gate(ga+1024*(3-(b))+128,gx+1024*(3-(b))+128,Q0,dd,outp+(size_t)(b)*512*2304); b_gate(ga+1024*(3-(b)),gx+1024*(3-(b)),Q1,dd,outp+(size_t)((b)*512+64)*2304); }
  if(wave<4){
    v4u hr[8];
    BAR;
    #pragma unroll
    for(int i=0;i<8;i++) gload(hr[i],p.hk,(cg*8+2*wave)*4096+i*1024+lane*16);
    const unsigned cb=2*wave*B_PITCH;
    #pragma unroll
    for(int c=0;c<2;c++){
      if(lane<56) st128(cb+c*B_PITCH+(lane<28? lane*16 : 4576+(lane-28)*16),(v4u){0,0,0,0});
      if(lane<31) st32(cb+c*B_PITCH+(lane<16? B_X0+4096+lane*4 : B_X1+4100+(lane-16)*4),0u);
    }
    unsigned hb=cb+((i16&1)?B_HA:B_HB)+(32*(i16>>1)+8*g)*2-448;
    unsigned xb=cb+((i16&1)? B_X0+(2046-2*i16+8*g)*2 : B_X1+(2048-2*i16+8*g)*2)-512*7-448;
    unsigned za=cb+B_HA+(2046-64*g-2*i16)*2-64-512*7;
    asm volatile("":"+v"(hb),"+v"(xb),"+v"(za));
    GCONST
    f4 a0[8],a1[8];
    #pragma unroll
    for(int j=0;j<8;j++){ a0[j]=(f4){0,0,0,0}; a1[j]=(f4){0,0,0,0}; asm volatile("":"+v"(a0[j]),"+v"(a1[j])); }
    #pragma unroll
    for(int c=0;c<2;c++){
      if(c==0) WAIT4(4,hr[0],hr[1],hr[2],hr[3]); else WAIT4(0,hr[4],hr[5],hr[6],hr[7]);
      #pragma unroll
      for(int i=0;i<4;i++){
        const v4u h=hr[4*c+i];
        const unsigned first=i? __builtin_amdgcn_readlane(hr[4*c+i-1].w,63) : 0u;
        const unsigned pv=__builtin_amdgcn_update_dpp(first,h.w,0x138,0xf,0xf,false);
        const v4u s={__builtin_amdgcn_alignbit(h.x,pv,16),__builtin_amdgcn_alignbit(h.y,h.x,16),__builtin_amdgcn_alignbit(h.z,h.y,16),__builtin_amdgcn_alignbit(h.w,h.z,16)};
        st128(cb+c*B_PITCH+B_HA+i*1024+lane*16,h);
        st128(cb+c*B_PITCH+B_HB+i*1024+lane*16,s);
      }
    }
    STAMP(1);
    __builtin_amdgcn_s_setprio(1);
    b_stage<0,2>(a0,a1,hb,xb,za);
    STAMP(3);
    b_stage<2,2>(a0,a1,hb,xb,za);
    STAMP(4);
    v4u qa,qb;
    gloadq(qa,p.q+3*BLK,rq); gloadq(qb,p.q+3*BLK+64*1152,rq);
    b_stage<4,4>(a0,a1,hb,xb,za);
    STAMP(5);
    WAIT2(0,qa,qb);
    ldsbar();
    GATE(3,qa,qb)
    STAMP(7);
  } else {
    const int j=cg&7, cp=(cg&~7)+2*w;
    const unsigned rk=(j*64+w*8+(lane>>3))*1152+(cg>>3)*128+(lane&7)*16;
    v4u k0[4],k1[4],v0[4],v1[4],q0[3],q1[3],halo; unsigned sk0,sk1,ep;
    #define LOADKV(b) { gload(k0[b],p.k+(b)*BLK,rk); gload(k1[b],p.k+(b)*BLK+32*1152,rk); gload(v0[b],p.v+(b)*BLK,rk); gload(v1[b],p.v+(b)*BLK+32*1152,rk); }
    #define LOADQ(b) { gloadq(q0[b],p.q+(b)*BLK,rq); gloadq(q1[b],p.q+(b)*BLK+64*1152,rq); }
    unsigned junk=0;
    #define TOUCH(P,O) asm volatile("global_load_dword %0, %1, %2":"+v"(junk):"v"(O),"s"(P):"memory")
    asm volatile("global_load_ushort %0, %2, %3\n\tglobal_load_ushort %1, %2, %3 offset:64":"=&v"(sk0),"=&v"(sk1):"v"(cp*64+(lane&7)*8+g*2),"s"(p.sk):"memory");
    GAS unsigned* epoch=(GAS unsigned*)(p.ws+(32+(cg>>3))*256);
    asm volatile("global_load_dword %0, %1, %2 sc1":"=v"(ep):"v"(0),"s"(epoch):"memory");
    LOADKV(0)
    const int ht=512*((lane>>3)&3)+64*j-4+w;
    gload(halo,p.k,imax(ht,0)*1152+(cg>>3)*128+(lane&7)*16);
    const unsigned po=(cp*32+j)*WS_BT, co=(cg*32+2*w)*WS_BT;
    st32(B_SCR+4*w,0u);
    ldsbar();
    TOUCH(p.ws+WS_B,co+imin(lane,19)*128);
    LOADKV(1)
    NSTAMP(10);
    GCONST
    #define KO(sl) ((sl)?B_ST+64:B_X1)
    #define VO(sl) ((sl)?B_ST+1088:B_X0)
    unsigned sw=(lane&7)*B_PITCH+(w*8+(lane>>3))*16, sh=(lane&7)*B_PITCH+w*16;
    unsigned ta=2*w*B_PITCH+(4+8*(i16>>2)+(lane&3)-g)*16, tv=2*w*B_PITCH+(32*(g&1)+8*(i16>>2)+4*(g>>1)+(lane&3))*16;
    unsigned xw=(lane&7)*B_PITCH+(2040-1536-64-128*w-32*(i16>>3)-8*g)*2;
    const int di=(lane&7)>>1;
    const unsigned one=0x3f80u<<(16*(lane&1));
    v4u i01=b_diag(di,(g==(i16>>3))?one:0u), i23=b_diag(di,(g==(i16>>3)+2)?one:0u);
    asm volatile("":"+v"(sw),"+v"(sh),"+v"(ta),"+v"(tv),"+v"(xw),"+v"(i01),"+v"(i23));
    const __amdgpu_buffer_rsrc_t xr=__builtin_amdgcn_make_buffer_rsrc(p.ws+WS_B,0,72*32*WS_BT,0x00020000);
    WAIT3(10,sk0,sk1,ep);
    unsigned tag=ep*2891336453u+1013904223u;
    sk0<<=16*(lane&1); sk1<<=16*(lane&1);
    v4u sl0=b_diag(di,(i16<8)?sk0:0u), sh0=b_diag(di,(i16<8)?0u:sk0), sl1=b_diag(di,(i16<8)?sk1:0u), sh1=b_diag(di,(i16<8)?0u:sk1);
    asm volatile("":"+v"(tag),"+v"(sl0),"+v"(sh0),"+v"(sl1),"+v"(sh1));
    WAIT1(5,halo);
    NSTAMP(11);
    if(ht<0) halo=(v4u){0,0,0,0};
    #define STAGE(b,sl,n) { WAIT4(n,k0[b],k1[b],v0[b],v1[b]); \
        st128(sw+KO(sl),k0[b]); st128(sw+KO(sl)+512,k1[b]); st128(sw+VO(sl),v0[b]); st128(sw+VO(sl)+512,v1[b]); \
        if((lane>>3)==(b)) st128(sh+KO(sl)-64,halo); }
    #define SYNC(n) { WAITL; *(volatile LAS unsigned*)(B_SCR+4*w)=(n); \
        _Pragma("unroll 1") for(int spin=0;spin<(1<<24);spin++){ if(__builtin_amdgcn_ballot_w64(*(volatile LAS unsigned*)(B_SCR+4*(lane&3))<(n))==0) break; __builtin_amdgcn_s_sleep(1); } asm volatile("":::"memory"); }
    #define PROD(b,sl) { \
        v4u oa,ob; b_prep2(ta+KO(sl)-64,tv+VO(sl),sl0,sh0,sl1,sh1,i01,i23,oa,ob); \
        __builtin_amdgcn_raw_buffer_store_b128(oa,xr,lane*16+po+(b)*8*WS_BT,0,16); __builtin_amdgcn_raw_buffer_store_b32(sum5(tag,oa),xr,1024+lane*4,po+(b)*8*WS_BT,16); \
        __builtin_amdgcn_raw_buffer_store_b128(ob,xr,lane*16+po+(32+(b)*8)*WS_BT,0,16); __builtin_amdgcn_raw_buffer_store_b32(sum5(tag,ob),xr,1024+lane*4,po+(32+(b)*8)*WS_BT,16); }
    #define RECV(b) { v4u oa,ob; _Pragma("unroll 1") for(int spin=0;spin<(1<<20);spin++){ asm volatile("":::"memory"); \
          oa=__builtin_amdgcn_raw_buffer_load_b128(xr,lane*16,co+(b)*8*WS_BT,16); ob=__builtin_amdgcn_raw_buffer_load_b128(xr,lane*16+WS_BT,co+(b)*8*WS_BT,16); \
          const unsigned ca=__builtin_amdgcn_raw_buffer_load_b32(xr,1024+lane*4,co+(b)*8*WS_BT,16), cb=__builtin_amdgcn_raw_buffer_load_b32(xr,1024+lane*4+WS_BT,co+(b)*8*WS_BT,16); \
          if(__builtin_amdgcn_ballot_w64(sum5(tag,oa)!=ca || sum5(tag,ob)!=cb)==0) break; __builtin_amdgcn_s_sleep(4); } \
        b_put(xw+1024*(3-(b))+128,oa); b_put(xw+1024*(3-(b)),ob); }
    __builtin_amdgcn_s_setprio(2);
    STAGE(0,1,5)
    SYNC(1)
    PROD(0,1)
    NSTAMP(12);
    __builtin_amdgcn_s_sleep(BSLP);
    RECV(0)
    NSTAMP(1);
    ldsbar();
    STAMP(2);
    LOADKV(2) LOADKV(3)
    BSTAMP(10);
    STAGE(1,0,8)
    SYNC(2)
    BSTAMP(11);
    PROD(1,0)
    BSTAMP(12);
    SYNC(3)
    STAGE(2,1,8)
    STAGE(3,0,4)
    BSTAMP(1);
    RECV(1)
    STAMP(13);
    TOUCH(p.q+w*BLK,(lane*8+j)*1152+cg*16);
    SYNC(4)
    PROD(2,1)
    PROD(3,0)
    STAMP(8);
    ldsbar();
    STAMP(3);
    LOADQ(0)
    BSTAMP(6);
    RECV(2)
    BSTAMP(7);
    RECV(3)
    BSTAMP(5);
    asm volatile(""::"v"(junk));
    LOADQ(1) LOADQ(2)
    WAIT2(4,q0[0],q1[0]);
    GATE(0,q0[0],q1[0])
    STAMP(9);
    ldsbar();
    STAMP(4);
    if(tid==256 && j==0) __hip_atomic_store(epoch,tag,__ATOMIC_RELAXED,__HIP_MEMORY_SCOPE_AGENT);
    WAIT4(2,q0[1],q1[1],q0[2],q1[2]);
    GATE(1,q0[1],q1[1])
    NSTAMP(5);
    ldsbar();
    NSTAMP(6);
    GATE(2,q0[2],q1[2])
    NSTAMP(7);
  }
}

constexpr int A_TM=90, A_SLOT=41984, A_WOFF=14336, A_IMG=83968, A_IROW=464, A_ZERO=136192, A_TAG=A_ZERO+288, A_CST=136704;
constexpr int WS_O=65536, WS_OM=19*1024, WS_OT=6*WS_OM;
typedef __amdgpu_buffer_rsrc_t rsrc_t;
DEV void dma16o(const v4u& srd, unsigned voff, unsigned soff, unsigned lds){
  asm volatile("s_mov_b32 m0, %3\n\ts_nop 0\n\tbuffer_load_dwordx4 %0, %1, %2 offen" NTO " lds"::"v"(voff),"s"(srd),"s"(soff),"s"(lds):"memory");
}
DEV void dma16(const v4u& srd, unsigned voff, unsigned soff, unsigned lds){
  asm volatile("s_mov_b32 m0, %3\n\ts_nop 0\n\tbuffer_load_dwordx4 %0, %1, %2 offen" NTX " lds"::"v"(voff),"s"(srd),"s"(soff),"s"(lds):"memory");
}
#define DW1 "s_add_u32 m0, m0, 3456\n\ts_add_u32 %0, %0, 27648\n\tbuffer_load_dwordx4 %1, %2, %0 offen lds\n\t"
DEV void dma_w4(const v4u& srd, unsigned voff, unsigned soff, unsigned lds){
  asm volatile("s_lshr_b64 exec, exec, 10\n\ts_mov_b32 m0, %3\n\ts_nop 0\n\tbuffer_load_dwordx4 %1, %2, %0 offen lds\n\t"
               DW1 DW1 DW1 "s_mov_b64 exec, -1"
               :"+s"(soff):"v"(voff),"s"(srd),"s"(lds):"memory","scc");
}
DEV v4u mksrd(const char* p, unsigned bytes){
  const u64 a=(u64)p; return (v4u){(unsigned)a,(unsigned)(a>>32)&0xffffu,bytes,0x00020000u};
}
struct AFr { v4u x[7]; v2u w[4][2]; };
struct AOp { v4u x[7], w[4]; };
template<int WO> DEV void a_rd(int i, AFr& f, unsigned xe, unsigned x0, unsigned xo, unsigned wa, unsigned we){
  switch(i){
    case 0: ATR(f.w[3][0],we,WO); break;      case 1: ATR(f.w[3][1],we,WO+432); break;
    case 2: ARD(f.x[0],xe,0); break;          case 3: ARD(f.x[1],xe,2048); break;   case 4: ARD(f.x[2],xe,4096); break;
    case 5: ATR(f.w[0][0],wa,WO); break;      case 6: ATR(f.w[0][1],wa,WO+432); break;
    case 7: ATR(f.w[1][0],wa,WO+32); break;   case 8: ATR(f.w[1][1],wa,WO+464); break;
    case 9: ATR(f.w[2][0],wa,WO+64); break;   case 10: ATR(f.w[2][1],wa,WO+496); break;
    case 11: ARD(f.x[3],x0,0); break;
    case 12: ARD(f.x[4],xo,0); break;         case 13: ARD(f.x[5],xo,2048); break;  case 14: ARD(f.x[6],xo,4096); break;
  }
}
DEV void a_wait(AFr& f, AOp& o){
  asm volatile("s_waitcnt lgkmcnt(0)":"+v"(f.x[0]),"+v"(f.x[1]),"+v"(f.x[2]),"+v"(f.x[3]),"+v"(f.x[4]),"+v"(f.x[5]),"+v"(f.x[6]),
     "+v"(f.w[0][0]),"+v"(f.w[0][1]),"+v"(f.w[1][0]),"+v"(f.w[1][1]),"+v"(f.w[2][0]),"+v"(f.w[2][1]),"+v"(f.w[3][0]),"+v"(f.w[3][1])::"memory");
  #pragma unroll
  for(int m=0;m<7;m++) o.x[m]=f.x[m];
  #pragma unroll
  for(int c=0;c<4;c++) o.w[c]=cat(f.w[c][0],f.w[c][1]);
}
DEV void a_mm(int i, f4 (&acc)[3][7], f4 (&ace)[3], const AOp& f){
  if(i<3) AMM(ace[i],f.w[3],f.x[i]); else AMM(acc[(i-3)%3][(i-3)/3],f.w[(i-3)%3],f.x[(i-3)/3]);
}
DEV float max3(float a,float b,float c){ float d; asm("v_max3_f32 %0, %1, %2, %3":"=v"(d):"v"(a),"v"(b),"v"(c)); return d; }
DEV float max2(float a,float b){ float d; asm("v_max_f32 %0, %1, %2":"=v"(d):"v"(a),"v"(b)); return d; }
DEV void tr4(unsigned (&a)[4]){
  auto r0=__builtin_amdgcn_permlane16_swap(a[0],a[1],false,false);
  auto r1=__builtin_amdgcn_permlane16_swap(a[2],a[3],false,false);
  auto s0=__builtin_amdgcn_permlane32_swap(r0[0],r1[0],false,false);
  auto s1=__builtin_amdgcn_permlane32_swap(r0[1],r1[1],false,false);
  a[0]=s0[0]; a[1]=s1[0]; a[2]=s0[1]; a[3]=s1[1];
}

DEV unsigned rs4(unsigned a, unsigned b, unsigned c, unsigned d){
  auto x=__builtin_amdgcn_permlane16_swap(a,b,false,false);
  auto y=__builtin_amdgcn_permlane16_swap(c,d,false,false);
  auto z=__builtin_amdgcn_permlane32_swap(x[0]+x[1],y[0]+y[1],false,false);
  return z[0]+z[1];
}
DEV void role_a(const Params& p, int rt, int grp, int rot STAMPARG){
  const int tid=__builtin_amdgcn_workitem_id_x(), lane=tid&63, wave=__builtin_amdgcn_readfirstlane(tid>>6);
  const int row0=rt*A_TM, nrow=imin(A_TM,2048-row0);
  const int i16=lane&15, g=lane>>4;
  GAS unsigned* epoch=(GAS unsigned*)(p.ws+rt*256);
  unsigned ka,kb,qb,va,sel0,sel1,sel2,sel3; v4u vl=(v4u){0,0,0,0}; unsigned vl4=0;
  const int par=i16&1; const bool cur0=(4*g+par<=i16), cur1=(4*g+par+2<=i16);
  const rsrc_t wsr=__builtin_amdgcn_make_buffer_rsrc(p.ws+WS_O,0,23*WS_OT,0x00020000);
  if(wave>=4){
    const int lw=wave-4;
    const v4u sx=mksrd(p.x,2048*1152), sw=mksrd(p.wqkv,576*3456), so=mksrd(p.wo,576*1152);
    int st=rot, cnt=0;
    unsigned sl=lw*1024;
    const int l27=lane>=27? lane-27:lane, seg=(l27>=18)?2:(l27>=9?1:0);
    const unsigned vw=(lane>=27?4*3456:0)+seg*(1152-144)+l27*16+grp*144+lw*3456;
    dma_w4(sw,vw,st*64*3456,sl+A_WOFF-lw*160);
    __builtin_amdgcn_s_setprio(3);
    unsigned vx[4];
    #pragma unroll
    for(int i=0;i<4;i++){
      const int ir=8*(lw+4*i)+(lane>>3); int tok=row0-16+ir; tok=imax(tok,imax(row0-14,0)); tok=imin(tok,row0+nrow-1);
      vx[i]=tok*1152+(((lane&7)^((ir>>1)&7))<<4);
    }
    #define A_XA() do{ dma16(sx,vx[0],st*128,sl); dma16(sx,vx[1],st*128,sl+4096); }while(0)
    #define A_HA() do{ dma_w4(sw,vw,st*64*3456,sl+A_WOFF-lw*160); A_XA(); }while(0)
    #define A_HB() do{ dma16(sx,vx[2],st*128,sl+8192); if(lw<2) dma16(sx,vx[3],st*128,sl+12288); dma_w4(sw,vw,(st*64+32)*3456,sl+A_WOFF-lw*160+13824); \
      cnt++; st=(cnt==8)?8:(cnt^rot); sl=(sl>=2*A_SLOT)?sl-2*A_SLOT:sl+A_SLOT; }while(0)
    A_XA(); A_HB();
    {
      const int pr=rt/6, id=(rt-6*pr)*4+(grp&3), u=tid&127; const char* a;
      if(lw<2){
        const int per=(pr==3)?108:90, pw=(pr==3)?36:30;
        const int l=id*per+u, r=l/15, c=l-r*15, m=imin(id*pw+imin(u-per,pw-1),719), ro=m/5;
        a=(u<per)? p.wqkv+((pr*144+r)*3456+(c/5)*1152+(c%5)*128) : p.wo+((pr*144+ro)*1152+(m-ro*5)*128);
        a+=(grp>>2)*512;
      } else a=p.x+imin((row0+grp*12)*1152+imin(u,107)*128,2048*1152-128);
      asm volatile("s_mov_b32 m0, 0x27e00\n\ts_nop 0\n\tglobal_load_lds_dword %0, off"::"v"(a):"memory");
    }
    A_HA();
    WAITV(7);
    BAR;
    #define LSTAMP(n)
    #define LUN _Pragma("unroll 1")
    LUN
    for(int n=1;n<=NH;n++){ A_HB(); A_HA(); WAITV(6); BAR; LSTAMP(n) }
    A_HB();
    LUN
    for(int n=NH+1;n<8;n++){ A_HA(); A_HB(); if(lw<2) WAITV(12); else WAITV(11); BAR; LSTAMP(n) }
    const v4u c0=ld128(A_CST+tid*16-4096), c1=ld128(A_CST+tid*16), c2=ld128(A_CST+tid*16+4096);
    vl=ld128(A_CST+tid*16+8192);
    ka=c0.x; kb=c0.y; qb=c0.z; va=c0.w; sel0=c1.x; sel1=c1.y; sel2=c1.z; sel3=c1.w; vl4=c2.w;
    #define A_ISSUE_WO(P0,P1) do{ _Pragma("unroll 1") for(int P=(P0);P<(P1);P++){ \
        dma16o(so,c2.x,P*72*1152,P*9216+lw*1024); dma16o(so,c2.y,P*72*1152,P*9216+lw*1024+4096); \
        if((P&3)==lw) dma16o(so,c2.z,P*72*1152,P*9216+8192); } }while(0)
    A_ISSUE_WO(0,2);
    if(lw<2) WAITV(5); else WAITV(4);
    BAR;
    STAMP(13);
    A_ISSUE_WO(2,5);
    BAR;
    A_ISSUE_WO(5,8);
    __builtin_amdgcn_s_setprio(0);
    STAMP(2);
  } else {
    unsigned ep;
    asm volatile("global_load_dword %0, %1, %2 sc1":"=v"(ep):"v"(0),"s"(epoch):"memory");
    { const unsigned plo=par?0x01000c0cu:0x0c0c0100u, phi=par?0x03020c0cu:0x0c0c0302u, pzz=0x0c0c0c0cu;
      sel0=cur0?pzz:plo; sel1=cur1?pzz:phi; sel2=cur0?plo:pzz; sel3=cur1?phi:pzz;
      ka=A_IMG+(16*wave+i16)*A_IROW+8*g; kb=(g==0)?ka+32:A_ZERO-144; qb=(g==0)?ka+32+16*A_IROW:A_ZERO;
      va=A_IMG+(16*wave+4*g+(i16>>2))*A_IROW+288+(lane&3)*8;
      const unsigned d=64*A_IROW, dz=(g==0)?d:0u;
      st128(A_CST+tid*16,(v4u){ka+d,kb+dz,qb+dz,va+d}); st128(A_CST+tid*16+4096,(v4u){sel0,sel1,sel2,sel3});
      unsigned vo[3], vv[5];
      #pragma unroll
      for(int i=0;i<3;i++){
        const int ci=(i<2? wave+4*i : 8)*64+lane, rho=ci/9, cc=ci-rho*9;
        vo[i]=(32*(rho>>5)+16*(rho&1)+4*((rho>>3)&3)+((rho>>1)&3))*1152+grp*144+cc*16;
      }
      #pragma unroll
      for(int i=0;i<5;i++){
        const int ci=imin((wave&1)+2*i,8)*64+lane, rho=ci/9, cc=ci-rho*9;
        vv[i]=(72*(rho>>3)+64+4*(rho&1)+((rho>>1)&3))*1152+grp*144+cc*16;
      }
      st128(A_CST+tid*16+8192,(v4u){vo[0],vo[1],vo[2],vv[4]}); st128(A_CST+tid*16+12288,(v4u){vv[0],vv[1],vv[2],vv[3]});
      asm volatile("":"+v"(ka),"+v"(kb),"+v"(qb),"+v"(va),"+v"(sel0),"+v"(sel1),"+v"(sel2),"+v"(sel3)); }
    f4 acc[3][7], ace[3];
    #pragma unroll
    for(int c=0;c<3;c++){ ace[c]=(f4){0,0,0,0};
      #pragma unroll
      for(int m=0;m<7;m++) acc[c][m]=(f4){0,0,0,0}; }
    const int e0=(wave&1)?4:1, o0=(wave&1)?1:4;
    const unsigned xl=i16*128+((g^(i16>>1))<<4);
    const unsigned wl=A_WOFF+(8*g+2*(i16>>2))*432+(lane&3)*8;
    unsigned ae0=xl+e0*2048, az0=xl, ao0=xl+o0*2048, ae1=ae0^64, az1=az0^64, ao1=ao0^64, wa=wl+(2+3*wave)*32, we=wl+(wave>>1)*32;
    const unsigned ia=A_IMG+i16*A_IROW+g*8;
    unsigned ie=ia+e0*16*A_IROW+(2+3*wave)*32, iz=ia+(2+3*wave)*32, io=ia+o0*16*A_IROW+(2+3*wave)*32, ix=ia+e0*16*A_IROW+(wave>>1)*32;
    asm volatile("":"+v"(ae0),"+v"(az0),"+v"(ao0),"+v"(ae1),"+v"(az1),"+v"(ao1),"+v"(wa),"+v"(we),"+v"(ie),"+v"(iz),"+v"(io),"+v"(ix));
    #pragma unroll
    for(int c=0;c<3;c++){ asm volatile("":"+v"(ace[c]));
      #pragma unroll
      for(int m=0;m<7;m++) asm volatile("":"+v"(acc[c][m])); }
    if(tid<72) st32(A_ZERO+tid*4,0u);
    WAIT1(0,ep);
    if(tid==0) st32(A_TAG,ep*2891336453u+1013904223u);
    int slot=0;
    AFr f0,f1; AOp g0,g1;
    ldsbar();
    STAMP(1);
    #pragma unroll
    for(int i=0;i<15;i++) a_rd<0>(i,f0,ae0,az0,ao0,wa,we);
    a_wait(f0,g0);
    #pragma unroll 1
    for(int s=0;s<8;s++){
      const int dl=(slot==2)?-2*A_SLOT:A_SLOT; slot=(slot==2)?0:slot+1;
      #pragma unroll
      for(int i=0;i<24;i++){
        a_mm(i,acc,ace,g0);
        if(i<15) a_rd<13824>(i,f1,ae1,az1,ao1,wa,we);
        if(i==15) AADD(ae0,dl); if(i==16) AADD(az0,dl); if(i==17) AADD(ao0,dl); if(i==18) AADD(wa,dl); if(i==19) AADD(we,dl);
      }
      a_wait(f1,g1);
      BAR;

      #pragma unroll
      for(int i=0;i<24;i++){
        a_mm(i,acc,ace,g1);
        if(i<15) a_rd<0>(i,f0,ae0,az0,ao0,wa,we);
        if(i==15) AADD(ae1,dl); if(i==16) AADD(az1,dl); if(i==17) AADD(ao1,dl);
      }
      a_wait(f0,g0);
    }
    #pragma unroll
    for(int i=0;i<24;i++){
      a_mm(i,acc,ace,g0);
      if(i<15) a_rd<13824>(i,f1,ae1,az1,ao1,wa,we);
    }
    a_wait(f1,g1);
    BAR;
    SB;
    #pragma unroll
    for(int m=0;m<3;m++) ace[m]=mfma(g1.w[3],g1.x[m],ace[m]);
    #pragma unroll
    for(int m=0;m<7;m++){
      #pragma unroll
      for(int c=0;c<3;c++) acc[c][m]=mfma(g1.w[c],g1.x[m],acc[c][m]);
    }
    #pragma unroll
    for(int m=0;m<3;m++) st64(ix+m*16*A_IROW,(v2u){pk2(ace[m][0],ace[m][1]),pk2(ace[m][2],ace[m][3])});
    #pragma unroll
    for(int m=0;m<7;m++){
      const unsigned ib=(m<3)?ie+m*16*A_IROW:(m==3?iz:io+(m-4)*16*A_IROW);
      #pragma unroll
      for(int c=0;c<3;c++) st64v(ib+c*32,(v2u){pk2(acc[c][m][0],acc[c][m][1]),pk2(acc[c][m][2],acc[c][m][3])});
    }
    SGB(0x8,3);
    #pragma unroll
    for(int i=0;i<21;i++){ SGB(0x8,1); SGB(0x2,2); SGB(0x200,1); }
    SB;
    STAMP(2);
  }
  ldsbar();
  STAMP(3);
  const unsigned tag=ld32(A_TAG);
  if(wave>=6){
    const v4u so=mksrd(p.wo,576*1152);
    #pragma unroll
    for(int i=0;i<5;i++) if(i<4 || wave==6) dma16o(so,i<4?vl[i]:vl4,0,8*9216+(wave&1)*1024+i*2048);
    WAITV(0);
  } else {
    v4u qf[4],kp[4],kc[4],vf[5];
    #pragma unroll
    for(int h=0;h<4;h++){
      const int hb=36*h-4*(h&1);
      qf[h]=cat(ld64v(ka+16*A_IROW+hb),ld64v(qb+hb));
      kp[h]=cat(ld64v(ka+144+hb),ld64v(kb+144+hb));
      kc[h]=cat(ld64v(ka+16*A_IROW+144+hb),ld64v(qb+144+hb));
    }
    #pragma unroll
    for(int c=0;c<5;c++) vf[c]=cat(ldtr(va+32*c),ldtr(va+16*A_IROW+32*c));
    SB;
    f4 sp[4],sc[4];
    #pragma unroll
    for(int h=0;h<4;h++){
      v4u q=qf[h];
      if(h&1) q.x=(g==0)?0u:q.x; else q.w=0u;
      sp[h]=mfma(kp[h],q,(f4){0,0,0,0}); sc[h]=mfma(kc[h],q,(f4){0,0,0,0});
    }
    SB;
    unsigned a0[4],a1[4];
    #pragma unroll
    for(int h=0;h<4;h++){
      const float c0=sc[h][0],c1=sc[h][1],c2=sc[h][2],c3=sc[h][3],p0=sp[h][0],p1=sp[h][1],p2=sp[h][2],p3=sp[h][3];
      const float ca=par?c1:c0, pa=par?p1:p0, cb=par?c3:c2, pb=par?p3:p2;
      a0[h]=__builtin_bit_cast(unsigned,cur0?ca:pa);
      a1[h]=__builtin_bit_cast(unsigned,cur1?cb:pb);
    }
    if(row0+16*wave==0){
      const unsigned mk=__builtin_bit_cast(unsigned,-1e9f/0.23570226039551584f);
      #pragma unroll
      for(int h=0;h<4;h++){ a0[h]=cur0?a0[h]:mk; a1[h]=cur1?a1[h]:mk; }
    }
    tr4(a0); tr4(a1);
    float l[8];
    #pragma unroll
    for(int i=0;i<4;i++){ l[2*i]=__builtin_bit_cast(float,a0[i]); l[2*i+1]=__builtin_bit_cast(float,a1[i]); }
    constexpr float SC=0.23570226039551584f*1.4426950408889634f;
    const float mx=-SC*max3(max3(l[0],l[1],l[2]),max3(l[3],l[4],l[5]),max2(l[6],l[7]));
    #pragma unroll
    for(int i=0;i<8;i++) l[i]=__builtin_amdgcn_exp2f(__builtin_fmaf(l[i],SC,mx));
    const float inv=__builtin_amdgcn_rcpf(((l[0]+l[1])+(l[2]+l[3]))+((l[4]+l[5])+(l[6]+l[7])));
    unsigned pp[4];
    #pragma unroll
    for(int i=0;i<4;i++) pp[i]=pk2(l[2*i]*inv,l[2*i+1]*inv);
    tr4(pp);
    v4u pf[4];
    #pragma unroll
    for(int h=0;h<4;h++) pf[h]=(v4u){__builtin_amdgcn_perm(0u,pp[h],sel0),__builtin_amdgcn_perm(0u,pp[h],sel1),__builtin_amdgcn_perm(0u,pp[h],sel2),__builtin_amdgcn_perm(0u,pp[h],sel3)};
    const f4 z={0,0,0,0};
    const f4 o0=mfma(vf[0],pf[0],z), a1_=mfma(vf[1],pf[0],z), b1=mfma(vf[1],pf[1],z), a2=mfma(vf[2],pf[1],z),
             b2_=mfma(vf[2],pf[2],z), a3=mfma(vf[3],pf[2],z), b3=mfma(vf[3],pf[3],z), o4=mfma(vf[4],pf[3],z);
    const v4u t01={pk2(o0[0],o0[1]),pk2(o0[2],o0[3]), (g==0)?pk2(a1_[0],a1_[1]):pk2(b1[0],b1[1]), pk2(b1[2],b1[3])};
    const v4u t23={(g==0)?pk2(a2[0],a2[1]):pk2(b2_[0],b2_[1]), (g==0)?pk2(a2[2],a2[3]):pk2(b2_[2],b2_[3]),
                   (g<2)?pk2(a3[0],a3[1]):pk2(b3[0],b3[1]), (g==0)?pk2(a3[2],a3[3]):pk2(b3[2],b3[3])};
    const unsigned u0=pk2(o4[0],o4[1]), u1=pk2(o4[2],o4[3]);
    auto r0=__builtin_amdgcn_permlane16_swap(u0,u0,false,false);
    auto r1=__builtin_amdgcn_permlane16_swap(u1,u1,false,false);
    const unsigned ob=rt*WS_OT+wave*WS_OM;
    __builtin_amdgcn_raw_buffer_store_b128(t01,wsr,ob+2*grp*1024+lane*16,0,16);
    __builtin_amdgcn_raw_buffer_store_b128(t23,wsr,ob+2*grp*1024+1024+lane*16,0,16);
    const v4u t4={(unsigned)r0[0],(unsigned)r1[0],(unsigned)r0[1],(unsigned)r1[1]};
    unsigned hs=sum5(sum5(0,t01),t23);
    { auto x=__builtin_amdgcn_permlane16_swap(hs,hs,false,false); hs=x[0]+x[1];
      auto y=__builtin_amdgcn_permlane32_swap(hs,hs,false,false); hs=y[0]+y[1]; }
    if(g==0){
      __builtin_amdgcn_raw_buffer_store_b128(t4,wsr,ob+16*1024+grp*256+lane*16,0,16);
      __builtin_amdgcn_raw_buffer_store_b64((v2u){sum5(hs,t4)+tag,tag},wsr,ob+18*1024+grp*128+lane*8,0,16);
    }
    WAITV(4);
  }
  STAMP(4);
  BAR;
  STAMP(5);
  {
    const int m=(wave<4)?wave:4+((wave-4)>>1);
    const unsigned wa=(8*g+2*(i16>>2))*144+(lane&3)*8;
    const unsigned ob=rt*WS_OT+m*WS_OM+lane*16, rb=rt*WS_OT+m*WS_OM+18*1024+lane*8;
    const unsigned red=A_IMG+((wave>>1)&1)*5120+lane*16, rf=A_ZERO+272+((wave>>1)&1)*4;
    int l9=lane/9, c9=lane-l9*9;
    asm volatile("":"+v"(l9),"+v"(c9));
    f4 acc[5];
    #define OLD(ks) __builtin_amdgcn_raw_buffer_load_b128(wsr,ob,(ks)*1024,16)
    #define RLD(st) __builtin_amdgcn_raw_buffer_load_b64(wsr,rb,(st)*512,16)
    #define WFR(a,ks,c) cat(ldtr((a)+(ks)*4608+(c)*32),ldtr((a)+(ks)*4608+(c)*32+144))
    #define MEMF asm volatile("":::"memory")
    if(wave<4){
      constexpr int NPRE=PNPRE, NW=9, NISS=PNISS, NF=PNF;
      #pragma unroll 1
      for(int tries=0;tries<64;tries++){
        v4u wf[NPRE][5], of[18]; v2u r0,r1;
        #pragma unroll
        for(int k=0;k<NPRE;k++){
          #pragma unroll
          for(int c=0;c<5;c++) wf[k][c]=WFR(wa,k,c);
          if(k==NISS){ SB; MEMF; r0=RLD(0); r1=RLD(1); SB;
            #pragma unroll
            for(int j=0;j<NF;j++){ of[j]=OLD(j); SB; } }
        }
        SB;
        #pragma unroll 1
        for(int spin=0;spin<(1<<20);spin++){
          if(__builtin_amdgcn_ballot_w64(r0.y!=tag || r1.y!=tag)==0) break;
          MEMF; r0=RLD(0); r1=RLD(1); SB;
          #pragma unroll
          for(int j=0;j<NF;j++){ of[j]=OLD(j); SB; }
        }
        #pragma unroll
        for(int j=NF;j<NW;j++){ of[j]=OLD(j); SB; }
        STAMP(6);
        #pragma unroll
        for(int c=0;c<5;c++) acc[c]=(f4){0,0,0,0};
        unsigned hs[10];
        #pragma unroll
        for(int k=0;k<18;k++){
          #pragma unroll
          for(int c=0;c<5;c++){
            acc[c]=mfma(wf[k%NPRE][c],of[k],acc[c]);
            if(k+NPRE<18){ wf[k%NPRE][c]=WFR(wa,k+NPRE,c); SGB(0x8,1); SGB(0x100,2); }
          }
          if(k>=16) hs[k-8]=sum5(tag,of[k]); else hs[k>>1]=sum5((k&1)?hs[k>>1]:0u,of[k]);
          if(k==7) hs[0]=rs4(hs[0],hs[1],hs[2],hs[3]);
          if(k==15) hs[4]=rs4(hs[4],hs[5],hs[6],hs[7]);
          SB;
          if(k+NW<18){ of[k+NW]=OLD(k+NW); SB; }
        }
        if(__builtin_amdgcn_ballot_w64(hs[0]+hs[8]!=r0.x || hs[4]+hs[9]!=r1.x)==0) break;
      }
      STAMP(7);
    } else {
      const int hf=wave&1;
      const unsigned wb=wa+hf*8*4608, wc=wa+(16+hf)*4608;
      #pragma unroll 1
      for(int tries=0;tries<64;tries++){
        v4u wf[9][5], of[9]; v2u r0;
        #pragma unroll
        for(int k=0;k<8;k++){
          #pragma unroll
          for(int c=0;c<5;c++) wf[k][c]=WFR(wb,k,c);
          if(k==PNISS){ SB; MEMF; r0=RLD(hf); SB;
            #pragma unroll
            for(int j=0;j<PNF;j++){ of[j]=OLD(8*hf+j); SB; } }
        }
        #pragma unroll
        for(int c=0;c<5;c++) wf[8][c]=WFR(wc,0,c);
        SB;
        #pragma unroll 1
        for(int spin=0;spin<(1<<20);spin++){
          if(__builtin_amdgcn_ballot_w64(r0.y!=tag)==0) break;
          MEMF; r0=RLD(hf); SB;
          #pragma unroll
          for(int j=0;j<PNF;j++){ of[j]=OLD(8*hf+j); SB; }
        }
        #pragma unroll
        for(int j=PNF;j<9;j++){ of[j]=OLD(j<8?8*hf+j:16+hf); SB; }
        __builtin_amdgcn_s_setprio(1);
        STAMP(6);
        #pragma unroll
        for(int c=0;c<5;c++) acc[c]=(f4){0,0,0,0};
        unsigned hs[5];
        #pragma unroll
        for(int k=0;k<9;k++){
          #pragma unroll
          for(int c=0;c<5;c++) acc[c]=mfma(wf[k][c],of[k],acc[c]);
          hs[k>>1]=sum5((k==8)?tag:((k&1)?hs[k>>1]:0u),of[k]);
          SB;
        }
        if(__builtin_amdgcn_ballot_w64(rs4(hs[0],hs[1],hs[2],hs[3])+hs[4]!=r0.x)==0) break;
      }
      if(hf){
        #pragma unroll
        for(int c=0;c<5;c++) st128(red+c*1024,__builtin_bit_cast(v4u,acc[c]));
        WAITL;
        *(volatile LAS unsigned*)rf=1u;
      } else {
        for(int spin=0;spin<(1<<24);spin++) if(__builtin_amdgcn_readfirstlane(*(volatile LAS unsigned*)rf)) break;
        asm volatile("":::"memory");
        #pragma unroll
        for(int c=0;c<5;c++) acc[c]+=__builtin_bit_cast(f4,ld128(red+c*1024));
      }
      STAMP(7);
    }
    if(wave<4 || !(wave&1)){
      const unsigned tb=A_IMG+10240+wave*2560;
      #pragma unroll
      for(int c=0;c<5;c++) st64(tb+i16*160+g*8+c*32,(v2u){pk2(acc[c][0],acc[c][1]),pk2(acc[c][2],acc[c][3])});
      #pragma unroll
      for(int i=0;i<3;i++){
        const int r=7*i+l9;
        if(lane<63 && r<16 && m*16+r<nrow) { const v4u ov=ld128(tb+r*160+c9*16); asm volatile("global_store_dwordx4 %0, %1, off" STA "\n\ts_nop 1"::"v"(p.out+(size_t)(row0+m*16+r)*2304+grp*144+c9*16),"v"(ov):"memory"); }
      }
    }
    asm volatile(".Lcpf1:");
    if(tid==0 && grp==0) __hip_atomic_store(epoch,tag,__ATOMIC_RELAXED,__HIP_MEMORY_SCOPE_AGENT);
  }
  STAMP(8);
}

NOPK __global__ __launch_bounds__(512,2) void fused(Params p){
  STAMPDECL;
  STAMP(0);
  const int b=__builtin_amdgcn_workgroup_id_x(), xcd=b&7, j=b>>3, pr=xcd>>1, half=xcd&1;
  const int nA=(pr<3)?24:20;
  {
    u64 pc; unsigned len;
    asm volatile("s_getpc_b64 %0\n.Lcpf0:\n\ts_mov_b32 %1, .Lcpf1-.Lcpf0":"=s"(pc),"=s"(len));
    const unsigned t=__builtin_amdgcn_workitem_id_x();
    if(t<40){ unsigned o=((j&7)+t*8)*128; o=o<len?o:len;
      asm volatile("s_mov_b32 m0, 0x27f00\n\ts_nop 0\n\tglobal_load_lds_dword %0, %1"::"v"(o&~3u),"s"(pc):"memory"); }
  }
  if(j>=nA){
    const int jb=j-nA;
    role_b(p, jb<8? xcd*8+jb : 64+half*4+(jb-8) STAMPPASS);
  } else
    role_a(p, 6*pr+(j>>2), half*4+(j&3), ((j>>2)+j)&1 STAMPPASS);
}

constexpr int LDS_BYTES=160*1024;
extern "C" void kernel_launch(void* const* d_in, const int* in_sizes, int n_in, void* d_out, int out_size, void* d_ws, size_t ws_size, hipStream_t stream){
  Params p;
  p.x=(const char*)d_in[0]; p.wqkv=(const char*)d_in[1]; p.wo=(const char*)d_in[2];
  p.q=(const char*)d_in[3]; p.k=(const char*)d_in[4]; p.v=(const char*)d_in[5];
  p.sk=(const char*)d_in[6]; p.hk=(const char*)d_in[7]; p.dv=(const char*)d_in[8];
  p.out=(char*)d_out; p.ws=(char*)d_ws;
  (void)hipFuncSetAttribute((const void*)fused,hipFuncAttributeMaxDynamicSharedMemorySize,LDS_BYTES);
  fused<<<256,512,LDS_BYTES,stream>>>(p);
}
```

```cpp
#define PROBE 0
#define PROBEBLK 230
#define PROBEWAVE 0
#include <hip/hip_runtime.h>
#include <hip/hip_bf16.h>

typedef unsigned short u16;
typedef unsigned long long u64;
typedef unsigned v4u __attribute__((ext_vector_type(4)));
typedef unsigned v2u __attribute__((ext_vector_type(2)));
typedef short s4 __attribute__((ext_vector_type(4)));
typedef __bf16 b2 __attribute__((ext_vector_type(2)));
typedef __bf16 b8 __attribute__((ext_vector_type(8)));
typedef float f4 __attribute__((ext_vector_type(4)));
typedef float f2 __attribute__((ext_vector_type(2)));
#define LAS __attribute__((address_space(3)))
#define GAS __attribute__((address_space(1)))
#if defined(__HIP_DEVICE_COMPILE__)
#define NOPK __attribute__((target("no-packed-fp32-ops")))
#else
#define NOPK
#endif
#define DEV __device__ __forceinline__ NOPK
#define SB __builtin_amdgcn_sched_barrier(0)
#define SGB(m,n) __builtin_amdgcn_sched_group_barrier(m,n,0)
#pragma clang diagnostic ignored "-Wint-to-pointer-cast"

#define STAMP(i) do{ if((i)==PROBE || (i)==0) { SB; asm volatile("s_memrealtime %0\n\ts_waitcnt lgkmcnt(0)":"=s"(g_stamp[(i)?1:0])::"memory"); SB; } }while(0)
#define STAMPDECL u64 g_stamp[2]={0,0}
#define STAMPARG , u64 (&g_stamp)[2]
#define STAMPPASS , g_stamp
#define STAMPOUT

#define PNPRE 6
#define PNISS 3
#define PNF 4
#define ALOOK 2
#define NOAT
#define ROT 22
#define NOPR
#define BV 1
#define CPFN 32
#define NTB " nt"
#define NTQ ""
#define NTX ""
#define NTW ""
#define NTO ""
#define BSTAMP(i)
#define NSTAMP(i) STAMP(i)
#define BSLP 16
#define MTS 0
#define WOV 2
#define STV 1
#define NH 4
#define STA ""
#define STB ""
struct Params {
  const char *x, *wqkv, *wo, *q, *k, *v, *sk, *hk, *dv;
  char *out, *ws;
};

DEV float lo2f(unsigned d){ return __builtin_bit_cast(float,d<<16); }
DEV float hi2f(unsigned d){ return __builtin_bit_cast(float,d&0xffff0000u); }
DEV unsigned pk2(float lo, float hi){ f2 p={lo,hi}; return __builtin_bit_cast(unsigned,__builtin_convertvector(p,b2)); }
DEV unsigned pk2a(float lo, float hi){ unsigned d; asm("v_cvt_pk_bf16_f32 %0, %1, %2":"=v"(d):"v"(lo),"v"(hi)); return d; }
DEV v4u ld128(unsigned a){ return *(LAS v4u*)a; }
DEV v2u ld64v(unsigned a){ return *(volatile LAS v2u*)a; }
DEV unsigned ld32(unsigned a){ return *(LAS unsigned*)a; }
DEV void st128(unsigned a, v4u v){ *(LAS v4u*)a=v; }
DEV void st64(unsigned a, v2u v){ *(LAS v2u*)a=v; }
DEV void st64v(unsigned a, v2u v){ *(volatile LAS v2u*)a=v; }
DEV void st32(unsigned a, unsigned v){ *(LAS unsigned*)a=v; }
DEV v2u ldtr(unsigned a){ return __builtin_bit_cast(v2u,__builtin_amdgcn_ds_read_tr16_b64_v4i16((LAS s4*)a)); }
DEV f4 mfma(v4u a, v4u b, f4 c){ return __builtin_amdgcn_mfma_f32_16x16x32_bf16(__builtin_bit_cast(b8,a),__builtin_bit_cast(b8,b),c,0,0,0); }
DEV v4u cat(v2u a, v2u b){ return (v4u){a.x,a.y,b.x,b.y}; }
DEV v4u gld128(const char* p){ return *(const GAS v4u*)p; }
DEV void gloadq(v4u& d, const char* sbase, unsigned voff){ asm volatile("global_load_dwordx4 %0, %1, %2" NTQ:"=v"(d):"v"(voff),"s"(sbase):"memory"); }
DEV void gload(v4u& d, const char* sbase, unsigned voff){ asm volatile("global_load_dwordx4 %0, %1, %2" NTB:"=v"(d):"v"(voff),"s"(sbase):"memory"); }
DEV void greload(v4u& d, const char* sbase, unsigned voff){
 asm volatile("global_load_dwordx4 %0, %1, %2":"+v"(d):"v"(voff),"s"(sbase):"memory"); }
#define WAIT1(n,a) asm volatile("s_waitcnt vmcnt(" #n ")":"+v"(a)::"memory")
#define WAIT2(n,a,b) asm volatile("s_waitcnt vmcnt(" #n ")":"+v"(a),"+v"(b)::"memory")
#define WAIT3(n,a,b,c) asm volatile("s_waitcnt vmcnt(" #n ")":"+v"(a),"+v"(b),"+v"(c)::"memory")
#define WAIT4(n,a,b,c,d) asm volatile("s_waitcnt vmcnt(" #n ")":"+v"(a),"+v"(b),"+v"(c),"+v"(d)::"memory")
#define WAITL asm volatile("":::"memory"); __builtin_amdgcn_s_waitcnt(0xC07F); asm volatile("":::"memory")
DEV void ldsbar(){ WAITL; __builtin_amdgcn_s_barrier(); asm volatile("":::"memory"); }
DEV int imin(int a,int b){ return a<b?a:b; }
DEV int imax(int a,int b){ return a>b?a:b; }
#define WAITV(n) asm volatile("s_waitcnt vmcnt(" #n ")":::"memory")
#define BAR do{ asm volatile("":::"memory"); __builtin_amdgcn_s_barrier(); asm volatile("":::"memory"); }while(0)
template<int NR,int NM> DEV void interleave(){
  constexpr int PER=(NR+NM-1)/NM, NG=(NR+PER-1)/PER;
  #pragma unroll
  for(int i=0;i<NG;i++){ SGB(0x8,1); SGB(0x100,PER); }
  if(NM>NG) SGB(0x8,NM-NG);
}

#define AMM(acc,a,b) asm volatile("v_mfma_f32_16x16x32_bf16 %0, %1, %2, %0":"+v"(acc):"v"(a),"v"(b))
#define ARD(d,a,off) asm volatile("ds_read_b128 %0, %1 offset:%2":"=v"(d):"v"(a),"i"(off):"memory")
#define ATR(d,a,off) asm volatile("ds_read_b64_tr_b16 %0, %1 offset:%2":"=v"(d):"v"(a),"i"(off):"memory")
#define AADD(v,s) asm volatile("v_add_u32 %0, %1, %0":"+v"(v):"s"(s))
#define AR64(d,a,off) asm volatile("ds_read_b64 %0, %1 offset:%2":"=v"(d):"v"(a),"i"(off):"memory")
constexpr int B_HA=448, B_HB=4576+448, B_X0=9120, B_X1=13344, B_ST=17520, B_PITCH=19696, B_SCR=8*B_PITCH;

template<int BLO,int NB> struct PF { v4u h[8-BLO]; v2u x[NB][2]; };
template<int BLO,int NB,int OFF> DEV void p_rd(int i, PF<BLO,NB>& f, unsigned hb, unsigned xb){
  #pragma unroll
  for(int a=0;a<8-BLO;a++) if(i==2+a) ARD(f.h[a],hb,512*a+OFF);
  #pragma unroll
  for(int b=0;b<NB;b++){ const int q=b?8-BLO+2*b:0; if(i==q) AR64(f.x[b][0],xb,512*(7-BLO-b)+OFF); if(i==q+1) AR64(f.x[b][1],xb,512*(7-BLO-b)+8+OFF); }
}
template<int BLO,int NB> DEV void p_mm(int i, f4 (&acc)[8], PF<BLO,NB>& f){
  int n=0;
  #pragma unroll
  for(int b=0;b<NB;b++){
    #pragma unroll
    for(int a=0;a<8-BLO-b;a++){ if(n==i){ v4u x=cat(f.x[b][0],f.x[b][1]); AMM(acc[a+b+BLO],f.h[a],x); } n++; }
  }
}
template<int N,int BLO,int NB> DEV void p_wait(PF<BLO,NB>& f){
  asm volatile("s_waitcnt lgkmcnt(%0)"::"i"(N):"memory");
  #pragma unroll
  for(int a=0;a<8-BLO;a++) asm volatile("":"+v"(f.h[a]));
  #pragma unroll
  for(int b=0;b<NB;b++) asm volatile("":"+v"(f.x[b][0]),"+v"(f.x[b][1]));
}
template<int BLO,int NB,int OFF> DEV void p_block(f4 (&acc)[8], PF<BLO,NB>& fc, PF<BLO,NB>& fn, unsigned hb, unsigned xb){
  constexpr int NR=8-BLO+2*NB, NM=NB*(8-BLO)-NB*(NB-1)/2, EX=(NR>NM)?NR-NM:0;
  constexpr int I2=(NB==2? 6-BLO : (NB-2)*(8-BLO)-(NB-2)*(NB-3)/2);
  p_wait<4>(fc);
  #pragma unroll
  for(int i=0;i<NM;i++){
    if(i==I2) asm volatile("s_waitcnt lgkmcnt(%0)"::"i"(I2+EX<NR?I2+EX:NR):"memory");
    p_mm<BLO,NB>(i,acc,fc);
    if(i<EX){ p_rd<BLO,NB,OFF>(2*i,fn,hb,xb); p_rd<BLO,NB,OFF>(2*i+1,fn,hb,xb); }
    else if(i+EX<NR) p_rd<BLO,NB,OFF>(i+EX,fn,hb,xb);
  }
}
template<int BLO,int NB> DEV void b_seg(f4 (&acc)[8], PF<BLO,NB>& f0, unsigned hb, unsigned xb, int next){
  PF<BLO,NB> f1;
  #pragma unroll 1
  for(int r=0;r<4;r++){
    p_block<BLO,NB,64>(acc,f0,f1,hb,xb);
    const int d=(r==3)? next-384 : 128;
    AADD(hb,d); AADD(xb,d);
    p_block<BLO,NB,0>(acc,f1,f0,hb,xb);
  }
}
template<int JLO,int NJ> DEV void b_zout(f4 (&acc)[8], unsigned za){
  asm volatile("s_nop 7\n\ts_nop 3":::"memory");
  #pragma unroll
  for(int J=JLO;J<JLO+NJ;J++){
    asm volatile("":"+v"(acc[J]));
    st32(za+512*(7-J)+64,pk2a(acc[J][1],acc[J][0])); st32(za+512*(7-J),pk2a(acc[J][3],acc[J][2]));
  }
}
template<int BLO,int NB> DEV void b_stage(f4 (&a0)[8], f4 (&a1)[8], unsigned hb, unsigned xb, unsigned za){
  PF<BLO,NB> f;
  #pragma unroll
  for(int i=2;i<10-BLO;i++) p_rd<BLO,NB,0>(i,f,hb,xb);
  ldsbar();
  p_rd<BLO,NB,0>(0,f,hb,xb); p_rd<BLO,NB,0>(1,f,hb,xb);
  #pragma unroll
  for(int i=10-BLO;i<8-BLO+2*NB;i++) p_rd<BLO,NB,0>(i,f,hb,xb);
  asm volatile("s_waitcnt lgkmcnt(%0)"::"i"(2*NB-2):"memory");
  b_seg<BLO,NB>(a0,f,hb,xb,B_PITCH);
  b_zout<BLO,NB>(a0,za);
  b_seg<BLO,NB>(a1,f,hb+B_PITCH,xb+B_PITCH,0);
  b_zout<BLO,NB>(a1,za+B_PITCH);
  p_wait<0>(f);
}

DEV unsigned sum5(unsigned m, v4u v){ return (m+v.x+v.y)+v.z+v.w; }
DEV void b_prep2(unsigned ta, unsigned tv, v4u sl0, v4u sh0, v4u sl1, v4u sh1, v4u i01, v4u i23, v4u& oa, v4u& ob){
  v4u a[2][4], vr[2];
  #pragma unroll
  for(int t=0;t<2;t++){ vr[t]=ld128(tv+t*B_PITCH); a[t][0]=ld128(ta+t*B_PITCH); a[t][2]=ld128(ta+t*B_PITCH+64); }
  #pragma unroll
  for(int t=0;t<2;t++){ a[t][1]=ld128(ta+t*B_PITCH+512); a[t][3]=ld128(ta+t*B_PITCH+576); }
  SB;
  const f4 z={0,0,0,0};
  f4 v1[2],v2[2],d1[2],d2[2];
  #pragma unroll
  for(int t=0;t<2;t++){ v1[t]=mfma(vr[t],i01,z); v2[t]=mfma(vr[t],i23,z); d1[t]=mfma(a[t][0],t?sl1:sl0,z); d2[t]=mfma(a[t][2],t?sl1:sl0,z); }
  #pragma unroll
  for(int t=0;t<2;t++){ d1[t]=mfma(a[t][1],t?sh1:sh0,d1[t]); d2[t]=mfma(a[t][3],t?sh1:sh0,d2[t]); }
  SB;
  #pragma unroll
  for(int t=0;t<2;t++){
    const v4u o={pk2(d2[t][3]*v2[t][3],d2[t][2]*v2[t][2]),pk2(d2[t][1]*v2[t][1],d2[t][0]*v2[t][0]),pk2(d1[t][3]*v1[t][3],d1[t][2]*v1[t][2]),pk2(d1[t][1]*v1[t][1],d1[t][0]*v1[t][0])};
    if(t) ob=o; else oa=o;
  }
}
DEV void b_put(unsigned a, v4u o){
  st128(a+B_X0,o);
  st32(a+B_X1+4,o.x); st64(a+B_X1+8,(v2u){o.y,o.z}); st32(a+B_X1+16,o.w);
}
DEV v4u b_diag(int di, unsigned e){ return (v4u){di==0?e:0u,di==1?e:0u,di==2?e:0u,di==3?e:0u}; }
constexpr int WS_B=4<<20, WS_BT=1280;
DEV void b_gate(unsigned ga, unsigned gb, v4u q, const float (&dd)[8], char* outp){
  const v2u y0=ldtr(ga), x0=ldtr(ga+B_X0-B_HA), y1=ldtr(gb), x1=ldtr(gb+B_X0-B_HA);
  #define G2(Q,Y,X,i) pk2(lo2f(Q)*__builtin_fmaf(lo2f(X),dd[i],lo2f(Y)), hi2f(Q)*__builtin_fmaf(hi2f(X),dd[i+1],hi2f(Y)))
  const v4u o={G2(q.x,y0.x,x0.x,0),G2(q.y,y0.y,x0.y,2),G2(q.z,y1.x,x1.x,4),G2(q.w,y1.y,x1.y,6)};
  asm volatile("global_store_dwordx4 %0, %1, off" STB "\n\ts_nop 1"::"v"(outp),"v"(o):"memory");
}

DEV void role_b(const Params& p, int cg STAMPARG){
  const int tid=__builtin_amdgcn_workitem_id_x(), lane=tid&63, wave=__builtin_amdgcn_readfirstlane(tid>>6);
  const int i16=lane&15, g=lane>>4;
  constexpr unsigned BLK=512*1152;
  const int w=wave&3, ql=lane^3;
  const unsigned rq=(w*128+ql)*1152+cg*16;
  unsigned ga,gx; char* outp; float dd[8];
  #define GCONST { const v4u dw=*(const __attribute__((address_space(4))) v4u*)(p.dv+cg*16); \
    dd[0]=lo2f(dw.x); dd[1]=hi2f(dw.x); dd[2]=lo2f(dw.y); dd[3]=hi2f(dw.y); dd[4]=lo2f(dw.z); dd[5]=hi2f(dw.z); dd[6]=lo2f(dw.w); dd[7]=hi2f(dw.w); \
    ga=(i16>>2)*B_PITCH+B_HA+(2044-1536-64-128*w-16*g-4*(lane&3))*2; gx=ga+4*B_PITCH; outp=p.out+(size_t)(w*128+ql)*2304+1152+cg*16; \
    asm volatile("":"+v"(ga),"+v"(gx),"+v"(outp)); }
  #define GATE(b,Q0,Q1) { b_gate(ga+1024*(3-(b))+128,gx+1024*(3-(b))+128,Q0,dd,outp+(size_t)(b)*512*2304); b_gate(ga+1024*(3-(b)),gx+1024*(3-(b)),Q1,dd,outp+(size_t)((b)*512+64)*2304); }
  if(wave<4){
    v4u hr[8];
    BAR;
    #pragma unroll
    for(int i=0;i<8;i++) gload(hr[i],p.hk,(cg*8+2*wave)*4096+i*1024+lane*16);
    const unsigned cb=2*wave*B_PITCH;
    #pragma unroll
    for(int c=0;c<2;c++){
      if(lane<56) st128(cb+c*B_PITCH+(lane<28? lane*16 : 4576+(lane-28)*16),(v4u){0,0,0,0});
      if(lane<31) st32(cb+c*B_PITCH+(lane<16? B_X0+4096+lane*4 : B_X1+4100+(lane-16)*4),0u);
    }
    unsigned hb=cb+((i16&1)?B_HA:B_HB)+(32*(i16>>1)+8*g)*2-448;
    unsigned xb=cb+((i16&1)? B_X0+(2046-2*i16+8*g)*2 : B_X1+(2048-2*i16+8*g)*2)-512*7-448;
    unsigned za=cb+B_HA+(2046-64*g-2*i16)*2-64-512*7;
    asm volatile("":"+v"(hb),"+v"(xb),"+v"(za));
    GCONST
    f4 a0[8],a1[8];
    #pragma unroll
    for(int j=0;j<8;j++){ a0[j]=(f4){0,0,0,0}; a1[j]=(f4){0,0,0,0}; asm volatile("":"+v"(a0[j]),"+v"(a1[j])); }
    #pragma unroll
    for(int c=0;c<2;c++){
      if(c==0) WAIT4(4,hr[0],hr[1],hr[2],hr[3]); else WAIT4(0,hr[4],hr[5],hr[6],hr[7]);
      #pragma unroll
      for(int i=0;i<4;i++){
        const v4u h=hr[4*c+i];
        const unsigned first=i? __builtin_amdgcn_readlane(hr[4*c+i-1].w,63) : 0u;
        const unsigned pv=__builtin_amdgcn_update_dpp(first,h.w,0x138,0xf,0xf,false);
        const v4u s={__builtin_amdgcn_alignbit(h.x,pv,16),__builtin_amdgcn_alignbit(h.y,h.x,16),__builtin_amdgcn_alignbit(h.z,h.y,16),__builtin_amdgcn_alignbit(h.w,h.z,16)};
        st128(cb+c*B_PITCH+B_HA+i*1024+lane*16,h);
        st128(cb+c*B_PITCH+B_HB+i*1024+lane*16,s);
      }
    }
    STAMP(1);
    __builtin_amdgcn_s_setprio(1);
    b_stage<0,2>(a0,a1,hb,xb,za);
    STAMP(3);
    b_stage<2,2>(a0,a1,hb,xb,za);
    STAMP(4);
    v4u qa,qb;
    gloadq(qa,p.q+3*BLK,rq); gloadq(qb,p.q+3*BLK+64*1152,rq);
    b_stage<4,4>(a0,a1,hb,xb,za);
    STAMP(5);
    WAIT2(0,qa,qb);
    ldsbar();
    GATE(3,qa,qb)
    STAMP(7);
  } else {
    const int j=cg&7, cp=(cg&~7)+2*w;
    const unsigned rk=(j*64+w*8+(lane>>3))*1152+(cg>>3)*128+(lane&7)*16;
    v4u k0[4],k1[4],v0[4],v1[4],q0[3],q1[3],halo; unsigned sk0,sk1,ep;
    #define LOADKV(b) { gload(k0[b],p.k+(b)*BLK,rk); gload(k1[b],p.k+(b)*BLK+32*1152,rk); gload(v0[b],p.v+(b)*BLK,rk); gload(v1[b],p.v+(b)*BLK+32*1152,rk); }
    #define LOADQ(b) { gloadq(q0[b],p.q+(b)*BLK,rq); gloadq(q1[b],p.q+(b)*BLK+64*1152,rq); }
    unsigned junk=0;
    #define TOUCH(P,O) asm volatile("global_load_dword %0, %1, %2":"+v"(junk):"v"(O),"s"(P):"memory")
    asm volatile("global_load_ushort %0, %2, %3\n\tglobal_load_ushort %1, %2, %3 offset:64":"=&v"(sk0),"=&v"(sk1):"v"(cp*64+(lane&7)*8+g*2),"s"(p.sk):"memory");
    GAS unsigned* epoch=(GAS unsigned*)(p.ws+(32+(cg>>3))*256);
    asm volatile("global_load_dword %0, %1, %2 sc1":"=v"(ep):"v"(0),"s"(epoch):"memory");
    LOADKV(0)
    const int ht=512*((lane>>3)&3)+64*j-4+w;
    gload(halo,p.k,imax(ht,0)*1152+(cg>>3)*128+(lane&7)*16);
    const unsigned po=(cp*32+j)*WS_BT, co=(cg*32+2*w)*WS_BT;
    st32(B_SCR+4*w,0u);
    ldsbar();
    TOUCH(p.ws+WS_B,co+imin(lane,19)*128);
    LOADKV(1)
    NSTAMP(10);
    GCONST
    #define KO(sl) ((sl)?B_ST+64:B_X1)
    #define VO(sl) ((sl)?B_ST+1088:B_X0)
    unsigned sw=(lane&7)*B_PITCH+(w*8+(lane>>3))*16, sh=(lane&7)*B_PITCH+w*16;
    unsigned ta=2*w*B_PITCH+(4+8*(i16>>2)+(lane&3)-g)*16, tv=2*w*B_PITCH+(32*(g&1)+8*(i16>>2)+4*(g>>1)+(lane&3))*16;
    unsigned xw=(lane&7)*B_PITCH+(2040-1536-64-128*w-32*(i16>>3)-8*g)*2;
    const int di=(lane&7)>>1;
    const unsigned one=0x3f80u<<(16*(lane&1));
    v4u i01=b_diag(di,(g==(i16>>3))?one:0u), i23=b_diag(di,(g==(i16>>3)+2)?one:0u);
    asm volatile("":"+v"(sw),"+v"(sh),"+v"(ta),"+v"(tv),"+v"(xw),"+v"(i01),"+v"(i23));
    const __amdgpu_buffer_rsrc_t xr=__builtin_amdgcn_make_buffer_rsrc(p.ws+WS_B,0,72*32*WS_BT,0x00020000);
    WAIT3(10,sk0,sk1,ep);
    unsigned tag=ep*2891336453u+1013904223u;
    sk0<<=16*(lane&1); sk1<<=16*(lane&1);
    v4u sl0=b_diag(di,(i16<8)?sk0:0u), sh0=b_diag(di,(i16<8)?0u:sk0), sl1=b_diag(di,(i16<8)?sk1:0u), sh1=b_diag(di,(i16<8)?0u:sk1);
    asm volatile("":"+v"(tag),"+v"(sl0),"+v"(sh0),"+v"(sl1),"+v"(sh1));
    WAIT1(5,halo);
    NSTAMP(11);
    if(ht<0) halo=(v4u){0,0,0,0};
    #define STAGE(b,sl,n) { WAIT4(n,k0[b],k1[b],v0[b],v1[b]); \
        st128(sw+KO(sl),k0[b]); st128(sw+KO(sl)+512,k1[b]); st128(sw+VO(sl),v0[b]); st128(sw+VO(sl)+512,v1[b]); \
        if((lane>>3)==(b)) st128(sh+KO(sl)-64,halo); }
    #define SYNC(n) { WAITL; *(volatile LAS unsigned*)(B_SCR+4*w)=(n); \
        _Pragma("unroll 1") for(int spin=0;spin<(1<<24);spin++){ if(__builtin_amdgcn_ballot_w64(*(volatile LAS unsigned*)(B_SCR+4*(lane&3))<(n))==0) break; __builtin_amdgcn_s_sleep(1); } asm volatile("":::"memory"); }
    #define PROD(b,sl) { \
        v4u oa,ob; b_prep2(ta+KO(sl)-64,tv+VO(sl),sl0,sh0,sl1,sh1,i01,i23,oa,ob); \
        __builtin_amdgcn_raw_buffer_store_b128(oa,xr,lane*16+po+(b)*8*WS_BT,0,16); __builtin_amdgcn_raw_buffer_store_b32(sum5(tag,oa),xr,1024+lane*4,po+(b)*8*WS_BT,16); \
        __builtin_amdgcn_raw_buffer_store_b128(ob,xr,lane*16+po+(32+(b)*8)*WS_BT,0,16); __builtin_amdgcn_raw_buffer_store_b32(sum5(tag,ob),xr,1024+lane*4,po+(32+(b)*8)*WS_BT,16); }
    #define RECV(b) { v4u oa,ob; _Pragma("unroll 1") for(int spin=0;spin<(1<<20);spin++){ asm volatile("":::"memory"); \
          oa=__builtin_amdgcn_raw_buffer_load_b128(xr,lane*16,co+(b)*8*WS_BT,16); ob=__builtin_amdgcn_raw_buffer_load_b128(xr,lane*16+WS_BT,co+(b)*8*WS_BT,16); \
          const unsigned ca=__builtin_amdgcn_raw_buffer_load_b32(xr,1024+lane*4,co+(b)*8*WS_BT,16), cb=__builtin_amdgcn_raw_buffer_load_b32(xr,1024+lane*4+WS_BT,co+(b)*8*WS_BT,16); \
          if(__builtin_amdgcn_ballot_w64(sum5(tag,oa)!=ca || sum5(tag,ob)!=cb)==0) break; __builtin_amdgcn_s_sleep(4); } \
        b_put(xw+1024*(3-(b))+128,oa); b_put(xw+1024*(3-(b)),ob); }
    __builtin_amdgcn_s_setprio(2);
    STAGE(0,1,5)
    SYNC(1)
    PROD(0,1)
    NSTAMP(12);
    __builtin_amdgcn_s_sleep(BSLP);
    RECV(0)
    NSTAMP(1);
    ldsbar();
    STAMP(2);
    LOADKV(2) LOADKV(3)
    BSTAMP(10);
    STAGE(1,0,8)
    SYNC(2)
    BSTAMP(11);
    PROD(1,0)
    BSTAMP(12);
    SYNC(3)
    STAGE(2,1,8)
    STAGE(3,0,4)
    BSTAMP(1);
    RECV(1)
    STAMP(13);
    TOUCH(p.q+w*BLK,(lane*8+j)*1152+cg*16);
    SYNC(4)
    PROD(2,1)
    PROD(3,0)
    STAMP(8);
    ldsbar();
    STAMP(3);
    LOADQ(0)
    BSTAMP(6);
    RECV(2)
    BSTAMP(7);
    RECV(3)
    BSTAMP(5);
    asm volatile(""::"v"(junk));
    LOADQ(1) LOADQ(2)
    WAIT2(4,q0[0],q1[0]);
    GATE(0,q0[0],q1[0])
    STAMP(9);
    ldsbar();
    STAMP(4);
    if(tid==256 && j==0) __hip_atomic_store(epoch,tag,__ATOMIC_RELAXED,__HIP_MEMORY_SCOPE_AGENT);
    WAIT4(2,q0[1],q1[1],q0[2],q1[2]);
    GATE(1,q0[1],q1[1])
    NSTAMP(5);
    ldsbar();
    NSTAMP(6);
    GATE(2,q0[2],q1[2])
    NSTAMP(7);
  }
}

constexpr int A_TM=90, A_SLOT=41984, A_WOFF=14336, A_IMG=83968, A_IROW=464, A_ZERO=136192, A_TAG=A_ZERO+288, A_CST=136704;
constexpr int WS_O=65536, WS_OM=19*1024, WS_OT=6*WS_OM;
typedef __amdgpu_buffer_rsrc_t rsrc_t;
DEV void dma16o(const v4u& srd, unsigned voff, unsigned soff, unsigned lds){
  asm volatile("s_mov_b32 m0, %3\n\ts_nop 0\n\tbuffer_load_dwordx4 %0, %1, %2 offen" NTO " lds"::"v"(voff),"s"(srd),"s"(soff),"s"(lds):"memory");
}
DEV void dma16(const v4u& srd, unsigned voff, unsigned soff, unsigned lds){
  asm volatile("s_mov_b32 m0, %3\n\ts_nop 0\n\tbuffer_load_dwordx4 %0, %1, %2 offen" NTX " lds"::"v"(voff),"s"(srd),"s"(soff),"s"(lds):"memory");
}
#define DW1 "s_add_u32 m0, m0, 3456\n\ts_add_u32 %0, %0, 27648\n\tbuffer_load_dwordx4 %1, %2, %0 offen lds\n\t"
DEV void dma_w4(const v4u& srd, unsigned voff, unsigned soff, unsigned lds){
  asm volatile("s_lshr_b64 exec, exec, 10\n\ts_mov_b32 m0, %3\n\ts_nop 0\n\tbuffer_load_dwordx4 %1, %2, %0 offen lds\n\t"
               DW1 DW1 DW1 "s_mov_b64 exec, -1"
               :"+s"(soff):"v"(voff),"s"(srd),"s"(lds):"memory","scc");
}
DEV v4u mksrd(const char* p, unsigned bytes){
  const u64 a=(u64)p; return (v4u){(unsigned)a,(unsigned)(a>>32)&0xffffu,bytes,0x00020000u};
}
struct AFr { v4u x[7]; v2u w[4][2]; };
struct AOp { v4u x[7], w[4]; };
template<int WO> DEV void a_rd(int i, AFr& f, unsigned xe, unsigned x0, unsigned xo, unsigned wa, unsigned we){
  switch(i){
    case 0: ATR(f.w[3][0],we,WO); break;      case 1: ATR(f.w[3][1],we,WO+432); break;
    case 2: ARD(f.x[0],xe,0); break;          case 3: ARD(f.x[1],xe,2048); break;   case 4: ARD(f.x[2],xe,4096); break;
    case 5: ATR(f.w[0][0],wa,WO); break;      case 6: ATR(f.w[0][1],wa,WO+432); break;
    case 7: ATR(f.w[1][0],wa,WO+32); break;   case 8: ATR(f.w[1][1],wa,WO+464); break;
    case 9: ATR(f.w[2][0],wa,WO+64); break;   case 10: ATR(f.w[2][1],wa,WO+496); break;
    case 11: ARD(f.x[3],x0,0); break;
    case 12: ARD(f.x[4],xo,0); break;         case 13: ARD(f.x[5],xo,2048); break;  case 14: ARD(f.x[6],xo,4096); break;
  }
}
DEV void a_wait(AFr& f, AOp& o){
  asm volatile("s_waitcnt lgkmcnt(0)":"+v"(f.x[0]),"+v"(f.x[1]),"+v"(f.x[2]),"+v"(f.x[3]),"+v"(f.x[4]),"+v"(f.x[5]),"+v"(f.x[6]),
     "+v"(f.w[0][0]),"+v"(f.w[0][1]),"+v"(f.w[1][0]),"+v"(f.w[1][1]),"+v"(f.w[2][0]),"+v"(f.w[2][1]),"+v"(f.w[3][0]),"+v"(f.w[3][1])::"memory");
  #pragma unroll
  for(int m=0;m<7;m++) o.x[m]=f.x[m];
  #pragma unroll
  for(int c=0;c<4;c++) o.w[c]=cat(f.w[c][0],f.w[c][1]);
}
DEV void a_mm(int i, f4 (&acc)[3][7], f4 (&ace)[3], const AOp& f){
  if(i<3) AMM(ace[i],f.w[3],f.x[i]); else AMM(acc[(i-3)%3][(i-3)/3],f.w[(i-3)%3],f.x[(i-3)/3]);
}
DEV float max3(float a,float b,float c){ float d; asm("v_max3_f32 %0, %1, %2, %3":"=v"(d):"v"(a),"v"(b),"v"(c)); return d; }
DEV float max2(float a,float b){ float d; asm("v_max_f32 %0, %1, %2":"=v"(d):"v"(a),"v"(b)); return d; }
DEV void tr4(unsigned (&a)[4]){
  auto r0=__builtin_amdgcn_permlane16_swap(a[0],a[1],false,false);
  auto r1=__builtin_amdgcn_permlane16_swap(a[2],a[3],false,false);
  auto s0=__builtin_amdgcn_permlane32_swap(r0[0],r1[0],false,false);
  auto s1=__builtin_amdgcn_permlane32_swap(r0[1],r1[1],false,false);
  a[0]=s0[0]; a[1]=s1[0]; a[2]=s0[1]; a[3]=s1[1];
}

DEV unsigned rs4(unsigned a, unsigned b, unsigned c, unsigned d){
  auto x=__builtin_amdgcn_permlane16_swap(a,b,false,false);
  auto y=__builtin_amdgcn_permlane16_swap(c,d,false,false);
  auto z=__builtin_amdgcn_permlane32_swap(x[0]+x[1],y[0]+y[1],false,false);
  return z[0]+z[1];
}
DEV void role_a(const Params& p, int rt, int grp, int rot STAMPARG){
  const int tid=__builtin_amdgcn_workitem_id_x(), lane=tid&63, wave=__builtin_amdgcn_readfirstlane(tid>>6);
  const int row0=rt*A_TM, nrow=imin(A_TM,2048-row0);
  const int i16=lane&15, g=lane>>4;
  GAS unsigned* epoch=(GAS unsigned*)(p.ws+rt*256);
  unsigned ka,kb,qb,va,sel0,sel1,sel2,sel3; v4u vl=(v4u){0,0,0,0}; unsigned vl4=0;
  const int par=i16&1; const bool cur0=(4*g+par<=i16), cur1=(4*g+par+2<=i16);
  const rsrc_t wsr=__builtin_amdgcn_make_buffer_rsrc(p.ws+WS_O,0,23*WS_OT,0x00020000);
  if(wave>=4){
    const int lw=wave-4;
    const v4u sx=mksrd(p.x,2048*1152), sw=mksrd(p.wqkv,576*3456), so=mksrd(p.wo,576*1152);
    int st=rot, cnt=0;
    unsigned sl=lw*1024;
    const int l27=lane>=27? lane-27:lane, seg=(l27>=18)?2:(l27>=9?1:0);
    const unsigned vw=(lane>=27?4*3456:0)+seg*(1152-144)+l27*16+grp*144+lw*3456;
    dma_w4(sw,vw,st*64*3456,sl+A_WOFF-lw*160);
    __builtin_amdgcn_s_setprio(3);
    unsigned vx[4];
    #pragma unroll
    for(int i=0;i<4;i++){
      const int ir=8*(lw+4*i)+(lane>>3); int tok=row0-16+ir; tok=imax(tok,imax(row0-14,0)); tok=imin(tok,row0+nrow-1);
      vx[i]=tok*1152+(((lane&7)^((ir>>1)&7))<<4);
    }
    #define A_XA() do{ dma16(sx,vx[0],st*128,sl); dma16(sx,vx[1],st*128,sl+4096); }while(0)
    #define A_HA() do{ dma_w4(sw,vw,st*64*3456,sl+A_WOFF-lw*160); A_XA(); }while(0)
    #define A_HB() do{ dma16(sx,vx[2],st*128,sl+8192); if(lw<2) dma16(sx,vx[3],st*128,sl+12288); dma_w4(sw,vw,(st*64+32)*3456,sl+A_WOFF-lw*160+13824); \
      cnt++; st=(cnt==8)?8:(cnt^rot); sl=(sl>=2*A_SLOT)?sl-2*A_SLOT:sl+A_SLOT; }while(0)
    A_XA(); A_HB();
    {
      const int pr=rt/6, id=(rt-6*pr)*4+(grp&3), u=tid&127; const char* a;
      if(lw<2){
        const int per=(pr==3)?108:90, pw=(pr==3)?36:30;
        const int l=id*per+u, r=l/15, c=l-r*15, m=imin(id*pw+imin(u-per,pw-1),719), ro=m/5;
        a=(u<per)? p.wqkv+((pr*144+r)*3456+(c/5)*1152+(c%5)*128) : p.wo+((pr*144+ro)*1152+(m-ro*5)*128);
        a+=(grp>>2)*512;
      } else a=p.x+imin((row0+grp*12)*1152+imin(u,107)*128,2048*1152-128);
      asm volatile("s_mov_b32 m0, 0x27e00\n\ts_nop 0\n\tglobal_load_lds_dword %0, off"::"v"(a):"memory");
    }
    A_HA();
    WAITV(7);
    BAR;
    #define LSTAMP(n)
    #define LUN _Pragma("unroll 1")
    LUN
    for(int n=1;n<=NH;n++){ A_HB(); A_HA(); WAITV(6); BAR; LSTAMP(n) }
    A_HB();
    LUN
    for(int n=NH+1;n<8;n++){ A_HA(); A_HB(); if(lw<2) WAITV(12); else WAITV(11); BAR; LSTAMP(n) }
    const v4u c0=ld128(A_CST+tid*16-4096), c1=ld128(A_CST+tid*16), c2=ld128(A_CST+tid*16+4096);
    vl=ld128(A_CST+tid*16+8192);
    ka=c0.x; kb=c0.y; qb=c0.z; va=c0.w; sel0=c1.x; sel1=c1.y; sel2=c1.z; sel3=c1.w; vl4=c2.w;
    #define A_ISSUE_WO(P0,P1) do{ _Pragma("unroll 1") for(int P=(P0);P<(P1);P++){ \
        dma16o(so,c2.x,P*72*1152,P*9216+lw*1024); dma16o(so,c2.y,P*72*1152,P*9216+lw*1024+4096); \
        if((P&3)==lw) dma16o(so,c2.z,P*72*1152,P*9216+8192); } }while(0)
    A_ISSUE_WO(0,2);
    if(lw<2) WAITV(5); else WAITV(4);
    BAR;
    STAMP(13);
    A_ISSUE_WO(2,5);
    BAR;
    A_ISSUE_WO(5,8);
    __builtin_amdgcn_s_setprio(0);
    STAMP(2);
  } else {
    unsigned ep;
    asm volatile("global_load_dword %0, %1, %2 sc1":"=v"(ep):"v"(0),"s"(epoch):"memory");
    { const unsigned plo=par?0x01000c0cu:0x0c0c0100u, phi=par?0x03020c0cu:0x0c0c0302u, pzz=0x0c0c0c0cu;
      sel0=cur0?pzz:plo; sel1=cur1?pzz:phi; sel2=cur0?plo:pzz; sel3=cur1?phi:pzz;
      ka=A_IMG+(16*wave+i16)*A_IROW+8*g; kb=(g==0)?ka+32:A_ZERO-144; qb=(g==0)?ka+32+16*A_IROW:A_ZERO;
      va=A_IMG+(16*wave+4*g+(i16>>2))*A_IROW+288+(lane&3)*8;
      const unsigned d=64*A_IROW, dz=(g==0)?d:0u;
      st128(A_CST+tid*16,(v4u){ka+d,kb+dz,qb+dz,va+d}); st128(A_CST+tid*16+4096,(v4u){sel0,sel1,sel2,sel3});
      unsigned vo[3], vv[5];
      #pragma unroll
      for(int i=0;i<3;i++){
        const int ci=(i<2? wave+4*i : 8)*64+lane, rho=ci/9, cc=ci-rho*9;
        vo[i]=(32*(rho>>5)+16*(rho&1)+4*((rho>>3)&3)+((rho>>1)&3))*1152+grp*144+cc*16;
      }
      #pragma unroll
      for(int i=0;i<5;i++){
        const int ci=imin((wave&1)+2*i,8)*64+lane, rho=ci/9, cc=ci-rho*9;
        vv[i]=(72*(rho>>3)+64+4*(rho&1)+((rho>>1)&3))*1152+grp*144+cc*16;
      }
      st128(A_CST+tid*16+8192,(v4u){vo[0],vo[1],vo[2],vv[4]}); st128(A_CST+tid*16+12288,(v4u){vv[0],vv[1],vv[2],vv[3]});
      asm volatile("":"+v"(ka),"+v"(kb),"+v"(qb),"+v"(va),"+v"(sel0),"+v"(sel1),"+v"(sel2),"+v"(sel3)); }
    f4 acc[3][7], ace[3];
    #pragma unroll
    for(int c=0;c<3;c++){ ace[c]=(f4){0,0,0,0};
      #pragma unroll
      for(int m=0;m<7;m++) acc[c][m]=(f4){0,0,0,0}; }
    const int e0=(wave&1)?4:1, o0=(wave&1)?1:4;
    const unsigned xl=i16*128+((g^(i16>>1))<<4);
    const unsigned wl=A_WOFF+(8*g+2*(i16>>2))*432+(lane&3)*8;
    unsigned ae0=xl+e0*2048, az0=xl, ao0=xl+o0*2048, ae1=ae0^64, az1=az0^64, ao1=ao0^64, wa=wl+(2+3*wave)*32, we=wl+(wave>>1)*32;
    const unsigned ia=A_IMG+i16*A_IROW+g*8;
    unsigned ie=ia+e0*16*A_IROW+(2+3*wave)*32, iz=ia+(2+3*wave)*32, io=ia+o0*16*A_IROW+(2+3*wave)*32, ix=ia+e0*16*A_IROW+(wave>>1)*32;
    asm volatile("":"+v"(ae0),"+v"(az0),"+v"(ao0),"+v"(ae1),"+v"(az1),"+v"(ao1),"+v"(wa),"+v"(we),"+v"(ie),"+v"(iz),"+v"(io),"+v"(ix));
    #pragma unroll
    for(int c=0;c<3;c++){ asm volatile("":"+v"(ace[c]));
      #pragma unroll
      for(int m=0;m<7;m++) asm volatile("":"+v"(acc[c][m])); }
    if(tid<72) st32(A_ZERO+tid*4,0u);
    WAIT1(0,ep);
    if(tid==0) st32(A_TAG,ep*2891336453u+1013904223u);
    int slot=0;
    AFr f0,f1; AOp g0,g1;
    ldsbar();
    STAMP(1);
    #pragma unroll
    for(int i=0;i<15;i++) a_rd<0>(i,f0,ae0,az0,ao0,wa,we);
    a_wait(f0,g0);
    #pragma unroll 1
    for(int s=0;s<8;s++){
      const int dl=(slot==2)?-2*A_SLOT:A_SLOT; slot=(slot==2)?0:slot+1;
      #pragma unroll
      for(int i=0;i<24;i++){
        a_mm(i,acc,ace,g0);
        if(i<15) a_rd<13824>(i,f1,ae1,az1,ao1,wa,we);
        if(i==15) AADD(ae0,dl); if(i==16) AADD(az0,dl); if(i==17) AADD(ao0,dl); if(i==18) AADD(wa,dl); if(i==19) AADD(we,dl);
      }
      a_wait(f1,g1);
      BAR;

      #pragma unroll
      for(int i=0;i<24;i++){
        a_mm(i,acc,ace,g1);
        if(i<15) a_rd<0>(i,f0,ae0,az0,ao0,wa,we);
        if(i==15) AADD(ae1,dl); if(i==16) AADD(az1,dl); if(i==17) AADD(ao1,dl);
      }
      a_wait(f0,g0);
    }
    #pragma unroll
    for(int i=0;i<24;i++){
      a_mm(i,acc,ace,g0);
      if(i<15) a_rd<13824>(i,f1,ae1,az1,ao1,wa,we);
    }
    a_wait(f1,g1);
    BAR;
    SB;
    #pragma unroll
    for(int m=0;m<3;m++) ace[m]=mfma(g1.w[3],g1.x[m],ace[m]);
    #pragma unroll
    for(int m=0;m<7;m++){
      #pragma unroll
      for(int c=0;c<3;c++) acc[c][m]=mfma(g1.w[c],g1.x[m],acc[c][m]);
    }
    #pragma unroll
    for(int m=0;m<3;m++) st64(ix+m*16*A_IROW,(v2u){pk2(ace[m][0],ace[m][1]),pk2(ace[m][2],ace[m][3])});
    #pragma unroll
    for(int m=0;m<7;m++){
      const unsigned ib=(m<3)?ie+m*16*A_IROW:(m==3?iz:io+(m-4)*16*A_IROW);
      #pragma unroll
      for(int c=0;c<3;c++) st64v(ib+c*32,(v2u){pk2(acc[c][m][0],acc[c][m][1]),pk2(acc[c][m][2],acc[c][m][3])});
    }
    SGB(0x8,3);
    #pragma unroll
    for(int i=0;i<21;i++){ SGB(0x8,1); SGB(0x2,2); SGB(0x200,1); }
    SB;
    STAMP(2);
  }
  ldsbar();
  STAMP(3);
  const unsigned tag=ld32(A_TAG);
  if(wave>=6){
    const v4u so=mksrd(p.wo,576*1152);
    #pragma unroll
    for(int i=0;i<5;i++) if(i<4 || wave==6) dma16o(so,i<4?vl[i]:vl4,0,8*9216+(wave&1)*1024+i*2048);
    WAITV(0);
  } else {
    v4u qf[4],kp[4],kc[4],vf[5];
    #pragma unroll
    for(int h=0;h<4;h++){
      const int hb=36*h-4*(h&1);
      qf[h]=cat(ld64v(ka+16*A_IROW+hb),ld64v(qb+hb));
      kp[h]=cat(ld64v(ka+144+hb),ld64v(kb+144+hb));
      kc[h]=cat(ld64v(ka+16*A_IROW+144+hb),ld64v(qb+144+hb));
    }
    #pragma unroll
    for(int c=0;c<5;c++) vf[c]=cat(ldtr(va+32*c),ldtr(va+16*A_IROW+32*c));
    SB;
    f4 sp[4],sc[4];
    #pragma unroll
    for(int h=0;h<4;h++){
      v4u q=qf[h];
      if(h&1) q.x=(g==0)?0u:q.x; else q.w=0u;
      sp[h]=mfma(kp[h],q,(f4){0,0,0,0}); sc[h]=mfma(kc[h],q,(f4){0,0,0,0});
    }
    SB;
    unsigned a0[4],a1[4];
    #pragma unroll
    for(int h=0;h<4;h++){
      const float c0=sc[h][0],c1=sc[h][1],c2=sc[h][2],c3=sc[h][3],p0=sp[h][0],p1=sp[h][1],p2=sp[h][2],p3=sp[h][3];
      const float ca=par?c1:c0, pa=par?p1:p0, cb=par?c3:c2, pb=par?p3:p2;
      a0[h]=__builtin_bit_cast(unsigned,cur0?ca:pa);
      a1[h]=__builtin_bit_cast(unsigned,cur1?cb:pb);
    }
    if(row0+16*wave==0){
      const unsigned mk=__builtin_bit_cast(unsigned,-1e9f/0.23570226039551584f);
      #pragma unroll
      for(int h=0;h<4;h++){ a0[h]=cur0?a0[h]:mk; a1[h]=cur1?a1[h]:mk; }
    }
    tr4(a0); tr4(a1);
    float l[8];
    #pragma unroll
    for(int i=0;i<4;i++){ l[2*i]=__builtin_bit_cast(float,a0[i]); l[2*i+1]=__builtin_bit_cast(float,a1[i]); }
    constexpr float SC=0.23570226039551584f*1.4426950408889634f;
    const float mx=-SC*max3(max3(l[0],l[1],l[2]),max3(l[3],l[4],l[5]),max2(l[6],l[7]));
    #pragma unroll
    for(int i=0;i<8;i++) l[i]=__builtin_amdgcn_exp2f(__builtin_fmaf(l[i],SC,mx));
    const float inv=__builtin_amdgcn_rcpf(((l[0]+l[1])+(l[2]+l[3]))+((l[4]+l[5])+(l[6]+l[7])));
    unsigned pp[4];
    #pragma unroll
    for(int i=0;i<4;i++) pp[i]=pk2(l[2*i]*inv,l[2*i+1]*inv);
    tr4(pp);
    v4u pf[4];
    #pragma unroll
    for(int h=0;h<4;h++) pf[h]=(v4u){__builtin_amdgcn_perm(0u,pp[h],sel0),__builtin_amdgcn_perm(0u,pp[h],sel1),__builtin_amdgcn_perm(0u,pp[h],sel2),__builtin_amdgcn_perm(0u,pp[h],sel3)};
    const f4 z={0,0,0,0};
    const f4 o0=mfma(vf[0],pf[0],z), a1_=mfma(vf[1],pf[0],z), b1=mfma(vf[1],pf[1],z), a2=mfma(vf[2],pf[1],z),
             b2_=mfma(vf[2],pf[2],z), a3=mfma(vf[3],pf[2],z), b3=mfma(vf[3],pf[3],z), o4=mfma(vf[4],pf[3],z);
    const v4u t01={pk2(o0[0],o0[1]),pk2(o0[2],o0[3]), (g==0)?pk2(a1_[0],a1_[1]):pk2(b1[0],b1[1]), pk2(b1[2],b1[3])};
    const v4u t23={(g==0)?pk2(a2[0],a2[1]):pk2(b2_[0],b2_[1]), (g==0)?pk2(a2[2],a2[3]):pk2(b2_[2],b2_[3]),
                   (g<2)?pk2(a3[0],a3[1]):pk2(b3[0],b3[1]), (g==0)?pk2(a3[2],a3[3]):pk2(b3[2],b3[3])};
    const unsigned u0=pk2(o4[0],o4[1]), u1=pk2(o4[2],o4[3]);
    auto r0=__builtin_amdgcn_permlane16_swap(u0,u0,false,false);
    auto r1=__builtin_amdgcn_permlane16_swap(u1,u1,false,false);
    const unsigned ob=rt*WS_OT+wave*WS_OM;
    __builtin_amdgcn_raw_buffer_store_b128(t01,wsr,ob+2*grp*1024+lane*16,0,16);
    __builtin_amdgcn_raw_buffer_store_b128(t23,wsr,ob+2*grp*1024+1024+lane*16,0,16);
    const v4u t4={(unsigned)r0[0],(unsigned)r1[0],(unsigned)r0[1],(unsigned)r1[1]};
    unsigned hs=sum5(sum5(0,t01),t23);
    { auto x=__builtin_amdgcn_permlane16_swap(hs,hs,false,false); hs=x[0]+x[1];
      auto y=__builtin_amdgcn_permlane32_swap(hs,hs,false,false); hs=y[0]+y[1]; }
    if(g==0){
      __builtin_amdgcn_raw_buffer_store_b128(t4,wsr,ob+16*1024+grp*256+lane*16,0,16);
      __builtin_amdgcn_raw_buffer_store_b64((v2u){sum5(hs,t4)+tag,tag},wsr,ob+18*1024+grp*128+lane*8,0,16);
    }
    WAITV(4);
  }
  STAMP(4);
  BAR;
  STAMP(5);
  {
    const int m=(wave<4)?wave:4+((wave-4)>>1);
    const unsigned wa=(8*g+2*(i16>>2))*144+(lane&3)*8;
    const unsigned ob=rt*WS_OT+m*WS_OM+lane*16, rb=rt*WS_OT+m*WS_OM+18*1024+lane*8;
    const unsigned red=A_IMG+((wave>>1)&1)*5120+lane*16, rf=A_ZERO+272+((wave>>1)&1)*4;
    int l9=lane/9, c9=lane-l9*9;
    asm volatile("":"+v"(l9),"+v"(c9));
    f4 acc[5];
    #define OLD(ks) __builtin_amdgcn_raw_buffer_load_b128(wsr,ob,(ks)*1024,16)
    #define RLD(st) __builtin_amdgcn_raw_buffer_load_b64(wsr,rb,(st)*512,16)
    #define WFR(a,ks,c) cat(ldtr((a)+(ks)*4608+(c)*32),ldtr((a)+(ks)*4608+(c)*32+144))
    #define MEMF asm volatile("":::"memory")
    if(wave<4){
      constexpr int NPRE=PNPRE, NW=9, NISS=PNISS, NF=PNF;
      #pragma unroll 1
      for(int tries=0;tries<64;tries++){
        v4u wf[NPRE][5], of[18]; v2u r0,r1;
        #pragma unroll
        for(int k=0;k<NPRE;k++){
          #pragma unroll
          for(int c=0;c<5;c++) wf[k][c]=WFR(wa,k,c);
          if(k==NISS){ SB; MEMF; r0=RLD(0); r1=RLD(1); SB;
            #pragma unroll
            for(int j=0;j<NF;j++){ of[j]=OLD(j); SB; } }
        }
        SB;
        #pragma unroll 1
        for(int spin=0;spin<(1<<20);spin++){
          if(__builtin_amdgcn_ballot_w64(r0.y!=tag || r1.y!=tag)==0) break;
          MEMF; r0=RLD(0); r1=RLD(1); SB;
          #pragma unroll
          for(int j=0;j<NF;j++){ of[j]=OLD(j); SB; }
        }
        #pragma unroll
        for(int j=NF;j<NW;j++){ of[j]=OLD(j); SB; }
        STAMP(6);
        #pragma unroll
        for(int c=0;c<5;c++) acc[c]=(f4){0,0,0,0};
        unsigned hs[10];
        #pragma unroll
        for(int k=0;k<18;k++){
          #pragma unroll
          for(int c=0;c<5;c++){
            acc[c]=mfma(wf[k%NPRE][c],of[k],acc[c]);
            if(k+NPRE<18){ wf[k%NPRE][c]=WFR(wa,k+NPRE,c); SGB(0x8,1); SGB(0x100,2); }
          }
          if(k>=16) hs[k-8]=sum5(tag,of[k]); else hs[k>>1]=sum5((k&1)?hs[k>>1]:0u,of[k]);
          if(k==7) hs[0]=rs4(hs[0],hs[1],hs[2],hs[3]);
          if(k==15) hs[4]=rs4(hs[4],hs[5],hs[6],hs[7]);
          SB;
          if(k+NW<18){ of[k+NW]=OLD(k+NW); SB; }
        }
        if(__builtin_amdgcn_ballot_w64(hs[0]+hs[8]!=r0.x || hs[4]+hs[9]!=r1.x)==0) break;
      }
      STAMP(7);
    } else {
      const int hf=wave&1;
      const unsigned wb=wa+hf*8*4608, wc=wa+(16+hf)*4608;
      #pragma unroll 1
      for(int tries=0;tries<64;tries++){
        v4u wf[9][5], of[9]; v2u r0;
        #pragma unroll
        for(int k=0;k<8;k++){
          #pragma unroll
          for(int c=0;c<5;c++) wf[k][c]=WFR(wb,k,c);
          if(k==PNISS){ SB; MEMF; r0=RLD(hf); SB;
            #pragma unroll
            for(int j=0;j<PNF;j++){ of[j]=OLD(8*hf+j); SB; } }
        }
        #pragma unroll
        for(int c=0;c<5;c++) wf[8][c]=WFR(wc,0,c);
        SB;
        #pragma unroll 1
        for(int spin=0;spin<(1<<20);spin++){
          if(__builtin_amdgcn_ballot_w64(r0.y!=tag)==0) break;
          MEMF; r0=RLD(hf); SB;
          #pragma unroll
          for(int j=0;j<PNF;j++){ of[j]=OLD(8*hf+j); SB; }
        }
        #pragma unroll
        for(int j=PNF;j<9;j++){ of[j]=OLD(j<8?8*hf+j:16+hf); SB; }
        __builtin_amdgcn_s_setprio(1);
        STAMP(6);
        #pragma unroll
        for(int c=0;c<5;c++) acc[c]=(f4){0,0,0,0};
        unsigned hs[5];
        #pragma unroll
        for(int k=0;k<9;k++){
          #pragma unroll
          for(int c=0;c<5;c++) acc[c]=mfma(wf[k][c],of[k],acc[c]);
          hs[k>>1]=sum5((k==8)?tag:((k&1)?hs[k>>1]:0u),of[k]);
          SB;
        }
        if(__builtin_amdgcn_ballot_w64(rs4(hs[0],hs[1],hs[2],hs[3])+hs[4]!=r0.x)==0) break;
      }
      if(hf){
        #pragma unroll
        for(int c=0;c<5;c++) st128(red+c*1024,__builtin_bit_cast(v4u,acc[c]));
        WAITL;
        *(volatile LAS unsigned*)rf=1u;
      } else {
        for(int spin=0;spin<(1<<24);spin++) if(__builtin_amdgcn_readfirstlane(*(volatile LAS unsigned*)rf)) break;
        asm volatile("":::"memory");
        #pragma unroll
        for(int c=0;c<5;c++) acc[c]+=__builtin_bit_cast(f4,ld128(red+c*1024));
      }
      STAMP(7);
    }
    if(wave<4 || !(wave&1)){
      const unsigned tb=A_IMG+10240+wave*2560;
      #pragma unroll
      for(int c=0;c<5;c++) st64(tb+i16*160+g*8+c*32,(v2u){pk2(acc[c][0],acc[c][1]),pk2(acc[c][2],acc[c][3])});
      #pragma unroll
      for(int i=0;i<3;i++){
        const int r=7*i+l9;
        if(lane<63 && r<16 && m*16+r<nrow) { const v4u ov=ld128(tb+r*160+c9*16); asm volatile("global_store_dwordx4 %0, %1, off" STA "\n\ts_nop 1"::"v"(p.out+(size_t)(row0+m*16+r)*2304+grp*144+c9*16),"v"(ov):"memory"); }
      }
    }
    asm volatile(".Lcpf1:");
    if(tid==0 && grp==0) __hip_atomic_store(epoch,tag,__ATOMIC_RELAXED,__HIP_MEMORY_SCOPE_AGENT);
  }
  STAMP(8);
}

NOPK __global__ __launch_bounds__(512,2) void fused(Params p){
  STAMPDECL;
  STAMP(0);
  const int b=__builtin_amdgcn_workgroup_id_x(), xcd=b&7, j=b>>3, pr=xcd>>1, half=xcd&1;
  const int nA=(pr<3)?24:20;
  {
    u64 pc; unsigned len;
    asm volatile("s_getpc_b64 %0\n.Lcpf0:\n\ts_mov_b32 %1, .Lcpf1-.Lcpf0":"=s"(pc),"=s"(len));
    const unsigned t=__builtin_amdgcn_workitem_id_x();
    if(t<40){ unsigned o=((j&7)+t*8)*128; o=o<len?o:len;
      asm volatile("s_mov_b32 m0, 0x27f00\n\ts_nop 0\n\tglobal_load_lds_dword %0, %1"::"v"(o&~3u),"s"(pc):"memory"); }
  }
  if(j>=nA){
    const int jb=j-nA;
    role_b(p, jb<8? xcd*8+jb : 64+half*4+(jb-8) STAMPPASS);
  } else
    role_a(p, 6*pr+(j>>2), half*4+(j&3), ((j>>2)+j)&1 STAMPPASS);
  asm volatile("s_waitcnt vmcnt(0)":::"memory");
  STAMP(15);
  if(__builtin_amdgcn_workgroup_id_x()==PROBEBLK && (__builtin_amdgcn_workitem_id_x()>>6)==PROBEWAVE){ u64 d=(PROBE==0)?0:(g_stamp[1]-g_stamp[0]); if(d>5000) d=5000; u64 until=g_stamp[0]+10000+10*d; while(__builtin_amdgcn_s_memrealtime()<until) __builtin_amdgcn_s_sleep(1); }
}

constexpr int LDS_BYTES=160*1024;
extern "C" void kernel_launch(void* const* d_in, const int* in_sizes, int n_in, void* d_out, int out_size, void* d_ws, size_t ws_size, hipStream_t stream){
  Params p;
  p.x=(const char*)d_in[0]; p.wqkv=(const char*)d_in[1]; p.wo=(const char*)d_in[2];
  p.q=(const char*)d_in[3]; p.k=(const char*)d_in[4]; p.v=(const char*)d_in[5];
  p.sk=(const char*)d_in[6]; p.hk=(const char*)d_in[7]; p.dv=(const char*)d_in[8];
  p.out=(char*)d_out; p.ws=(char*)d_ws;
  (void)hipFuncSetAttribute((const void*)fused,hipFuncAttributeMaxDynamicSharedMemorySize,LDS_BYTES);
  fused<<<256,512,LDS_BYTES,stream>>>(p);
}
```

```cpp
#include <hip/hip_runtime.h>
#include <hip/hip_bf16.h>

typedef unsigned short u16;
typedef unsigned long long u64;
typedef unsigned v4u __attribute__((ext_vector_type(4)));
typedef unsigned v2u __attribute__((ext_vector_type(2)));
typedef short s4 __attribute__((ext_vector_type(4)));
typedef __bf16 b2 __attribute__((ext_vector_type(2)));
typedef __bf16 b8 __attribute__((ext_vector_type(8)));
typedef float f4 __attribute__((ext_vector_type(4)));
typedef float f2 __attribute__((ext_vector_type(2)));
#define LAS __attribute__((address_space(3)))
#define GAS __attribute__((address_space(1)))
#if defined(__HIP_DEVICE_COMPILE__)
#define NOPK __attribute__((target("no-packed-fp32-ops")))
#else
#define NOPK
#endif
#define DEV __device__ __forceinline__ NOPK
#define SB __builtin_amdgcn_sched_barrier(0)
#define SGB(m,n) __builtin_amdgcn_sched_group_barrier(m,n,0)
#pragma clang diagnostic ignored "-Wint-to-pointer-cast"

#define STAMP(i)
#define STAMPDECL
#define STAMPARG
#define STAMPPASS
#define STAMPOUT

#define PNPRE 6
#define PNISS 3
#define PNF 6
#define ALOOK 2
#define NOAT
#define ROT 22
#define NOPR
#define BV 1
#define CPFN 32
#define NTB " nt"
#define NTQ ""
#define NTX ""
#define NTW ""
#define NTO ""
#define BSTAMP(i)
#define NSTAMP(i) STAMP(i)
#define BSLP 16
#define MTS 0
#define WOV 2
#define STV 1
#define NH 4
struct Params {
  const char *x, *wqkv, *wo, *q, *k, *v, *sk, *hk, *dv;
  char *out, *ws;
};

DEV float lo2f(unsigned d){ return __builtin_bit_cast(float,d<<16); }
DEV float hi2f(unsigned d){ return __builtin_bit_cast(float,d&0xffff0000u); }
DEV unsigned pk2(float lo, float hi){ f2 p={lo,hi}; return __builtin_bit_cast(unsigned,__builtin_convertvector(p,b2)); }
DEV unsigned pk2a(float lo, float hi){ unsigned d; asm("v_cvt_pk_bf16_f32 %0, %1, %2":"=v"(d):"v"(lo),"v"(hi)); return d; }
DEV v4u ld128(unsigned a){ return *(LAS v4u*)a; }
DEV v2u ld64v(unsigned a){ return *(volatile LAS v2u*)a; }
DEV unsigned ld32(unsigned a){ return *(LAS unsigned*)a; }
DEV void st128(unsigned a, v4u v){ *(LAS v4u*)a=v; }
DEV void st64(unsigned a, v2u v){ *(LAS v2u*)a=v; }
DEV void st64v(unsigned a, v2u v){ *(volatile LAS v2u*)a=v; }
DEV void st32(unsigned a, unsigned v){ *(LAS unsigned*)a=v; }
DEV v2u ldtr(unsigned a){ return __builtin_bit_cast(v2u,__builtin_amdgcn_ds_read_tr16_b64_v4i16((LAS s4*)a)); }
DEV f4 mfma(v4u a, v4u b, f4 c){ return __builtin_amdgcn_mfma_f32_16x16x32_bf16(__builtin_bit_cast(b8,a),__builtin_bit_cast(b8,b),c,0,0,0); }
DEV v4u cat(v2u a, v2u b){ return (v4u){a.x,a.y,b.x,b.y}; }
DEV v4u gld128(const char* p){ return *(const GAS v4u*)p; }
DEV void gloadq(v4u& d, const char* sbase, unsigned voff){ asm volatile("global_load_dwordx4 %0, %1, %2" NTQ:"=v"(d):"v"(voff),"s"(sbase):"memory"); }
DEV void gload(v4u& d, const char* sbase, unsigned voff){ asm volatile("global_load_dwordx4 %0, %1, %2" NTB:"=v"(d):"v"(voff),"s"(sbase):"memory"); }
DEV void greload(v4u& d, const char* sbase, unsigned voff){
 asm volatile("global_load_dwordx4 %0, %1, %2":"+v"(d):"v"(voff),"s"(sbase):"memory"); }
#define WAIT1(n,a) asm volatile("s_waitcnt vmcnt(" #n ")":"+v"(a)::"memory")
#define WAIT2(n,a,b) asm volatile("s_waitcnt vmcnt(" #n ")":"+v"(a),"+v"(b)::"memory")
#define WAIT3(n,a,b,c) asm volatile("s_waitcnt vmcnt(" #n ")":"+v"(a),"+v"(b),"+v"(c)::"memory")
#define WAIT4(n,a,b,c,d) asm volatile("s_waitcnt vmcnt(" #n ")":"+v"(a),"+v"(b),"+v"(c),"+v"(d)::"memory")
#define WAITL asm volatile("":::"memory"); __builtin_amdgcn_s_waitcnt(0xC07F); asm volatile("":::"memory")
DEV void ldsbar(){ WAITL; __builtin_amdgcn_s_barrier(); asm volatile("":::"memory"); }
DEV int imin(int a,int b){ return a<b?a:b; }
DEV int imax(int a,int b){ return a>b?a:b; }
#define WAITV(n) asm volatile("s_waitcnt vmcnt(" #n ")":::"memory")
#define BAR do{ asm volatile("":::"memory"); __builtin_amdgcn_s_barrier(); asm volatile("":::"memory"); }while(0)
template<int NR,int NM> DEV void interleave(){
  constexpr int PER=(NR+NM-1)/NM, NG=(NR+PER-1)/PER;
  #pragma unroll
  for(int i=0;i<NG;i++){ SGB(0x8,1); SGB(0x100,PER); }
  if(NM>NG) SGB(0x8,NM-NG);
}

#define AMM(acc,a,b) asm volatile("v_mfma_f32_16x16x32_bf16 %0, %1, %2, %0":"+v"(acc):"v"(a),"v"(b))
#define ARD(d,a,off) asm volatile("ds_read_b128 %0, %1 offset:%2":"=v"(d):"v"(a),"i"(off):"memory")
#define ATR(d,a,off) asm volatile("ds_read_b64_tr_b16 %0, %1 offset:%2":"=v"(d):"v"(a),"i"(off):"memory")
#define AADD(v,s) asm volatile("v_add_u32 %0, %1, %0":"+v"(v):"s"(s))
#define AR64(d,a,off) asm volatile("ds_read_b64 %0, %1 offset:%2":"=v"(d):"v"(a),"i"(off):"memory")
constexpr int B_HA=448, B_HB=4576+448, B_X0=9120, B_X1=13344, B_ST=17520, B_PITCH=19696, B_SCR=8*B_PITCH;

template<int BLO,int NB> struct PF { v4u h[8-BLO]; v2u x[NB][2]; };
template<int BLO,int NB,int OFF> DEV void p_rd(int i, PF<BLO,NB>& f, unsigned hb, unsigned xb){
  #pragma unroll
  for(int a=0;a<8-BLO;a++) if(i==2+a) ARD(f.h[a],hb,512*a+OFF);
  #pragma unroll
  for(int b=0;b<NB;b++){ const int q=b?8-BLO+2*b:0; if(i==q) AR64(f.x[b][0],xb,512*(7-BLO-b)+OFF); if(i==q+1) AR64(f.x[b][1],xb,512*(7-BLO-b)+8+OFF); }
}
template<int BLO,int NB> DEV void p_mm(int i, f4 (&acc)[8], PF<BLO,NB>& f){
  int n=0;
  #pragma unroll
  for(int b=0;b<NB;b++){
    #pragma unroll
    for(int a=0;a<8-BLO-b;a++){ if(n==i){ v4u x=cat(f.x[b][0],f.x[b][1]); AMM(acc[a+b+BLO],f.h[a],x); } n++; }
  }
}
template<int N,int BLO,int NB> DEV void p_wait(PF<BLO,NB>& f){
  asm volatile("s_waitcnt lgkmcnt(%0)"::"i"(N):"memory");
  #pragma unroll
  for(int a=0;a<8-BLO;a++) asm volatile("":"+v"(f.h[a]));
  #pragma unroll
  for(int b=0;b<NB;b++) asm volatile("":"+v"(f.x[b][0]),"+v"(f.x[b][1]));
}
template<int BLO,int NB,int OFF> DEV void p_block(f4 (&acc)[8], PF<BLO,NB>& fc, PF<BLO,NB>& fn, unsigned hb, unsigned xb){
  constexpr int NR=8-BLO+2*NB, NM=NB*(8-BLO)-NB*(NB-1)/2, EX=(NR>NM)?NR-NM:0;
  constexpr int I2=(NB==2? 6-BLO : (NB-2)*(8-BLO)-(NB-2)*(NB-3)/2);
  p_wait<4>(fc);
  #pragma unroll
  for(int i=0;i<NM;i++){
    if(i==I2) asm volatile("s_waitcnt lgkmcnt(%0)"::"i"(I2+EX<NR?I2+EX:NR):"memory");
    p_mm<BLO,NB>(i,acc,fc);
    if(i<EX){ p_rd<BLO,NB,OFF>(2*i,fn,hb,xb); p_rd<BLO,NB,OFF>(2*i+1,fn,hb,xb); }
    else if(i+EX<NR) p_rd<BLO,NB,OFF>(i+EX,fn,hb,xb);
  }
}
template<int BLO,int NB> DEV void b_seg(f4 (&acc)[8], PF<BLO,NB>& f0, unsigned hb, unsigned xb, int next){
  PF<BLO,NB> f1;
  #pragma unroll 1
  for(int r=0;r<4;r++){
    p_block<BLO,NB,64>(acc,f0,f1,hb,xb);
    const int d=(r==3)? next-384 : 128;
    AADD(hb,d); AADD(xb,d);
    p_block<BLO,NB,0>(acc,f1,f0,hb,xb);
  }
}
template<int JLO,int NJ> DEV void b_zout(f4 (&acc)[8], unsigned za){
  asm volatile("s_nop 7\n\ts_nop 3":::"memory");
  #pragma unroll
  for(int J=JLO;J<JLO+NJ;J++){
    asm volatile("":"+v"(acc[J]));
    st32(za+512*(7-J)+64,pk2a(acc[J][1],acc[J][0])); st32(za+512*(7-J),pk2a(acc[J][3],acc[J][2]));
  }
}
template<int BLO,int NB> DEV void b_stage(f4 (&a0)[8], f4 (&a1)[8], unsigned hb, unsigned xb, unsigned za){
  PF<BLO,NB> f;
  #pragma unroll
  for(int i=2;i<10-BLO;i++) p_rd<BLO,NB,0>(i,f,hb,xb);
  ldsbar();
  p_rd<BLO,NB,0>(0,f,hb,xb); p_rd<BLO,NB,0>(1,f,hb,xb);
  #pragma unroll
  for(int i=10-BLO;i<8-BLO+2*NB;i++) p_rd<BLO,NB,0>(i,f,hb,xb);
  asm volatile("s_waitcnt lgkmcnt(%0)"::"i"(2*NB-2):"memory");
  b_seg<BLO,NB>(a0,f,hb,xb,B_PITCH);
  b_zout<BLO,NB>(a0,za);
  b_seg<BLO,NB>(a1,f,hb+B_PITCH,xb+B_PITCH,0);
  b_zout<BLO,NB>(a1,za+B_PITCH);
  p_wait<0>(f);
}

DEV unsigned sum5(unsigned m, v4u v){ return (m+v.x+v.y)+v.z+v.w; }
DEV void b_prep2(unsigned ta, unsigned tv, v4u sl0, v4u sh0, v4u sl1, v4u sh1, v4u i01, v4u i23, v4u& oa, v4u& ob){
  v4u a[2][4], vr[2];
  #pragma unroll
  for(int t=0;t<2;t++){ vr[t]=ld128(tv+t*B_PITCH); a[t][0]=ld128(ta+t*B_PITCH); a[t][2]=ld128(ta+t*B_PITCH+64); }
  #pragma unroll
  for(int t=0;t<2;t++){ a[t][1]=ld128(ta+t*B_PITCH+512); a[t][3]=ld128(ta+t*B_PITCH+576); }
  SB;
  const f4 z={0,0,0,0};
  f4 v1[2],v2[2],d1[2],d2[2];
  #pragma unroll
  for(int t=0;t<2;t++){ v1[t]=mfma(vr[t],i01,z); v2[t]=mfma(vr[t],i23,z); d1[t]=mfma(a[t][0],t?sl1:sl0,z); d2[t]=mfma(a[t][2],t?sl1:sl0,z); }
  #pragma unroll
  for(int t=0;t<2;t++){ d1[t]=mfma(a[t][1],t?sh1:sh0,d1[t]); d2[t]=mfma(a[t][3],t?sh1:sh0,d2[t]); }
  SB;
  #pragma unroll
  for(int t=0;t<2;t++){
    const v4u o={pk2(d2[t][3]*v2[t][3],d2[t][2]*v2[t][2]),pk2(d2[t][1]*v2[t][1],d2[t][0]*v2[t][0]),pk2(d1[t][3]*v1[t][3],d1[t][2]*v1[t][2]),pk2(d1[t][1]*v1[t][1],d1[t][0]*v1[t][0])};
    if(t) ob=o; else oa=o;
  }
}
DEV void b_put(unsigned a, v4u o){
  st128(a+B_X0,o);
  st32(a+B_X1+4,o.x); st64(a+B_X1+8,(v2u){o.y,o.z}); st32(a+B_X1+16,o.w);
}
DEV v4u b_diag(int di, unsigned e){ return (v4u){di==0?e:0u,di==1?e:0u,di==2?e:0u,di==3?e:0u}; }
constexpr int WS_B=4<<20, WS_BT=1280;
DEV void b_gate(unsigned ga, unsigned gb, v4u q, const float (&dd)[8], char* outp){
  const v2u y0=ldtr(ga), x0=ldtr(ga+B_X0-B_HA), y1=ldtr(gb), x1=ldtr(gb+B_X0-B_HA);
  #define G2(Q,Y,X,i) pk2(lo2f(Q)*__builtin_fmaf(lo2f(X),dd[i],lo2f(Y)), hi2f(Q)*__builtin_fmaf(hi2f(X),dd[i+1],hi2f(Y)))
  const v4u o={G2(q.x,y0.x,x0.x,0),G2(q.y,y0.y,x0.y,2),G2(q.z,y1.x,x1.x,4),G2(q.w,y1.y,x1.y,6)};
  *(GAS v4u*)outp=o;
}

DEV void role_b(const Params& p, int cg STAMPARG){
  const int tid=__builtin_amdgcn_workitem_id_x(), lane=tid&63, wave=__builtin_amdgcn_readfirstlane(tid>>6);
  const int i16=lane&15, g=lane>>4;
  constexpr unsigned BLK=512*1152;
  const int w=wave&3, ql=lane^3;
  const unsigned rq=(w*128+ql)*1152+cg*16;
  unsigned ga,gx; char* outp; float dd[8];
  #define GCONST { const v4u dw=*(const __attribute__((address_space(4))) v4u*)(p.dv+cg*16); \
    dd[0]=lo2f(dw.x); dd[1]=hi2f(dw.x); dd[2]=lo2f(dw.y); dd[3]=hi2f(dw.y); dd[4]=lo2f(dw.z); dd[5]=hi2f(dw.z); dd[6]=lo2f(dw.w); dd[7]=hi2f(dw.w); \
    ga=(i16>>2)*B_PITCH+B_HA+(2044-1536-64-128*w-16*g-4*(lane&3))*2; gx=ga+4*B_PITCH; outp=p.out+(size_t)(w*128+ql)*2304+1152+cg*16; \
    asm volatile("":"+v"(ga),"+v"(gx),"+v"(outp)); }
  #define GATE(b,Q0,Q1) { b_gate(ga+1024*(3-(b))+128,gx+1024*(3-(b))+128,Q0,dd,outp+(size_t)(b)*512*2304); b_gate(ga+1024*(3-(b)),gx+1024*(3-(b)),Q1,dd,outp+(size_t)((b)*512+64)*2304); }
  if(wave<4){
    v4u hr[8];
    BAR;
    #pragma unroll
    for(int i=0;i<8;i++) gload(hr[i],p.hk,(cg*8+2*wave)*4096+i*1024+lane*16);
    const unsigned cb=2*wave*B_PITCH;
    #pragma unroll
    for(int c=0;c<2;c++){
      if(lane<56) st128(cb+c*B_PITCH+(lane<28? lane*16 : 4576+(lane-28)*16),(v4u){0,0,0,0});
      if(lane<31) st32(cb+c*B_PITCH+(lane<16? B_X0+4096+lane*4 : B_X1+4100+(lane-16)*4),0u);
    }
    unsigned hb=cb+((i16&1)?B_HA:B_HB)+(32*(i16>>1)+8*g)*2-448;
    unsigned xb=cb+((i16&1)? B_X0+(2046-2*i16+8*g)*2 : B_X1+(2048-2*i16+8*g)*2)-512*7-448;
    unsigned za=cb+B_HA+(2046-64*g-2*i16)*2-64-512*7;
    asm volatile("":"+v"(hb),"+v"(xb),"+v"(za));
    GCONST
    f4 a0[8],a1[8];
    #pragma unroll
    for(int j=0;j<8;j++){ a0[j]=(f4){0,0,0,0}; a1[j]=(f4){0,0,0,0}; asm volatile("":"+v"(a0[j]),"+v"(a1[j])); }
    #pragma unroll
    for(int c=0;c<2;c++){
      if(c==0) WAIT4(4,hr[0],hr[1],hr[2],hr[3]); else WAIT4(0,hr[4],hr[5],hr[6],hr[7]);
      #pragma unroll
      for(int i=0;i<4;i++){
        const v4u h=hr[4*c+i];
        const unsigned first=i? __builtin_amdgcn_readlane(hr[4*c+i-1].w,63) : 0u;
        const unsigned pv=__builtin_amdgcn_update_dpp(first,h.w,0x138,0xf,0xf,false);
        const v4u s={__builtin_amdgcn_alignbit(h.x,pv,16),__builtin_amdgcn_alignbit(h.y,h.x,16),__builtin_amdgcn_alignbit(h.z,h.y,16),__builtin_amdgcn_alignbit(h.w,h.z,16)};
        st128(cb+c*B_PITCH+B_HA+i*1024+lane*16,h);
        st128(cb+c*B_PITCH+B_HB+i*1024+lane*16,s);
      }
    }
    STAMP(1);
    __builtin_amdgcn_s_setprio(1);
    b_stage<0,2>(a0,a1,hb,xb,za);
    STAMP(3);
    b_stage<2,2>(a0,a1,hb,xb,za);
    STAMP(4);
    v4u qa,qb;
    gloadq(qa,p.q+3*BLK,rq); gloadq(qb,p.q+3*BLK+64*1152,rq);
    b_stage<4,4>(a0,a1,hb,xb,za);
    STAMP(5);
    WAIT2(0,qa,qb);
    ldsbar();
    GATE(3,qa,qb)
    STAMP(7);
  } else {
    const int j=cg&7, cp=(cg&~7)+2*w;
    const unsigned rk=(j*64+w*8+(lane>>3))*1152+(cg>>3)*128+(lane&7)*16;
    v4u k0[4],k1[4],v0[4],v1[4],q0[3],q1[3],halo; unsigned sk0,sk1,ep;
    #define LOADKV(b) { gload(k0[b],p.k+(b)*BLK,rk); gload(k1[b],p.k+(b)*BLK+32*1152,rk); gload(v0[b],p.v+(b)*BLK,rk); gload(v1[b],p.v+(b)*BLK+32*1152,rk); }
    #define LOADQ(b) { gloadq(q0[b],p.q+(b)*BLK,rq); gloadq(q1[b],p.q+(b)*BLK+64*1152,rq); }
    unsigned junk=0;
    #define TOUCH(P,O) asm volatile("global_load_dword %0, %1, %2":"+v"(junk):"v"(O),"s"(P):"memory")
    asm volatile("global_load_ushort %0, %2, %3\n\tglobal_load_ushort %1, %2, %3 offset:64":"=&v"(sk0),"=&v"(sk1):"v"(cp*64+(lane&7)*8+g*2),"s"(p.sk):"memory");
    GAS unsigned* epoch=(GAS unsigned*)(p.ws+(32+(cg>>3))*256);
    asm volatile("global_load_dword %0, %1, %2 sc1":"=v"(ep):"v"(0),"s"(epoch):"memory");
    LOADKV(0)
    const int ht=512*((lane>>3)&3)+64*j-4+w;
    gload(halo,p.k,imax(ht,0)*1152+(cg>>3)*128+(lane&7)*16);
    const unsigned po=(cp*32+j)*WS_BT, co=(cg*32+2*w)*WS_BT;
    st32(B_SCR+4*w,0u);
    ldsbar();
    TOUCH(p.ws+WS_B,co+imin(lane,19)*128);
    LOADKV(1)
    NSTAMP(10);
    GCONST
    #define KO(sl) ((sl)?B_ST+64:B_X1)
    #define VO(sl) ((sl)?B_ST+1088:B_X0)
    unsigned sw=(lane&7)*B_PITCH+(w*8+(lane>>3))*16, sh=(lane&7)*B_PITCH+w*16;
    unsigned ta=2*w*B_PITCH+(4+8*(i16>>2)+(lane&3)-g)*16, tv=2*w*B_PITCH+(32*(g&1)+8*(i16>>2)+4*(g>>1)+(lane&3))*16;
    unsigned xw=(lane&7)*B_PITCH+(2040-1536-64-128*w-32*(i16>>3)-8*g)*2;
    const int di=(lane&7)>>1;
    const unsigned one=0x3f80u<<(16*(lane&1));
    v4u i01=b_diag(di,(g==(i16>>3))?one:0u), i23=b_diag(di,(g==(i16>>3)+2)?one:0u);
    asm volatile("":"+v"(sw),"+v"(sh),"+v"(ta),"+v"(tv),"+v"(xw),"+v"(i01),"+v"(i23));
    const __amdgpu_buffer_rsrc_t xr=__builtin_amdgcn_make_buffer_rsrc(p.ws+WS_B,0,72*32*WS_BT,0x00020000);
    WAIT3(10,sk0,sk1,ep);
    unsigned tag=ep*2891336453u+1013904223u;
    sk0<<=16*(lane&1); sk1<<=16*(lane&1);
    v4u sl0=b_diag(di,(i16<8)?sk0:0u), sh0=b_diag(di,(i16<8)?0u:sk0), sl1=b_diag(di,(i16<8)?sk1:0u), sh1=b_diag(di,(i16<8)?0u:sk1);
    asm volatile("":"+v"(tag),"+v"(sl0),"+v"(sh0),"+v"(sl1),"+v"(sh1));
    WAIT1(5,halo);
    NSTAMP(11);
    if(ht<0) halo=(v4u){0,0,0,0};
    #define STAGE(b,sl,n) { WAIT4(n,k0[b],k1[b],v0[b],v1[b]); \
        st128(sw+KO(sl),k0[b]); st128(sw+KO(sl)+512,k1[b]); st128(sw+VO(sl),v0[b]); st128(sw+VO(sl)+512,v1[b]); \
        if((lane>>3)==(b)) st128(sh+KO(sl)-64,halo); }
    #define SYNC(n) { WAITL; *(volatile LAS unsigned*)(B_SCR+4*w)=(n); \
        _Pragma("unroll 1") for(int spin=0;spin<(1<<24);spin++){ if(__builtin_amdgcn_ballot_w64(*(volatile LAS unsigned*)(B_SCR+4*(lane&3))<(n))==0) break; __builtin_amdgcn_s_sleep(1); } asm volatile("":::"memory"); }
    #define PROD(b,sl) { \
        v4u oa,ob; b_prep2(ta+KO(sl)-64,tv+VO(sl),sl0,sh0,sl1,sh1,i01,i23,oa,ob); \
        __builtin_amdgcn_raw_buffer_store_b128(oa,xr,lane*16+po+(b)*8*WS_BT,0,16); __builtin_amdgcn_raw_buffer_store_b32(sum5(tag,oa),xr,1024+lane*4,po+(b)*8*WS_BT,16); \
        __builtin_amdgcn_raw_buffer_store_b128(ob,xr,lane*16+po+(32+(b)*8)*WS_BT,0,16); __builtin_amdgcn_raw_buffer_store_b32(sum5(tag,ob),xr,1024+lane*4,po+(32+(b)*8)*WS_BT,16); }
    #define RECV(b) { v4u oa,ob; _Pragma("unroll 1") for(int spin=0;spin<(1<<20);spin++){ asm volatile("":::"memory"); \
          oa=__builtin_amdgcn_raw_buffer_load_b128(xr,lane*16,co+(b)*8*WS_BT,16); ob=__builtin_amdgcn_raw_buffer_load_b128(xr,lane*16+WS_BT,co+(b)*8*WS_BT,16); \
          const unsigned ca=__builtin_amdgcn_raw_buffer_load_b32(xr,1024+lane*4,co+(b)*8*WS_BT,16), cb=__builtin_amdgcn_raw_buffer_load_b32(xr,1024+lane*4+WS_BT,co+(b)*8*WS_BT,16); \
          if(__builtin_amdgcn_ballot_w64(sum5(tag,oa)!=ca || sum5(tag,ob)!=cb)==0) break; __builtin_amdgcn_s_sleep(4); } \
        b_put(xw+1024*(3-(b))+128,oa); b_put(xw+1024*(3-(b)),ob); }
    __builtin_amdgcn_s_setprio(2);
    STAGE(0,1,5)
    SYNC(1)
    PROD(0,1)
    NSTAMP(12);
    __builtin_amdgcn_s_sleep(BSLP);
    RECV(0)
    NSTAMP(1);
    ldsbar();
    STAMP(2);
    LOADKV(2) LOADKV(3)
    BSTAMP(10);
    STAGE(1,0,8)
    SYNC(2)
    BSTAMP(11);
    PROD(1,0)
    BSTAMP(12);
    SYNC(3)
    STAGE(2,1,8)
    STAGE(3,0,4)
    BSTAMP(1);
    RECV(1)
    STAMP(13);
    TOUCH(p.q+w*BLK,(lane*8+j)*1152+cg*16);
    SYNC(4)
    PROD(2,1)
    PROD(3,0)
    STAMP(8);
    ldsbar();
    STAMP(3);
    LOADQ(0)
    BSTAMP(6);
    RECV(2)
    BSTAMP(7);
    RECV(3)
    BSTAMP(5);
    asm volatile(""::"v"(junk));
    LOADQ(1) LOADQ(2)
    WAIT2(4,q0[0],q1[0]);
    GATE(0,q0[0],q1[0])
    STAMP(9);
    ldsbar();
    STAMP(4);
    if(tid==256 && j==0) __hip_atomic_store(epoch,tag,__ATOMIC_RELAXED,__HIP_MEMORY_SCOPE_AGENT);
    WAIT4(2,q0[1],q1[1],q0[2],q1[2]);
    GATE(1,q0[1],q1[1])
    NSTAMP(5);
    ldsbar();
    NSTAMP(6);
    GATE(2,q0[2],q1[2])
    NSTAMP(7);
  }
}

constexpr int A_TM=90, A_SLOT=41984, A_WOFF=14336, A_IMG=83968, A_IROW=464, A_ZERO=136192, A_TAG=A_ZERO+288, A_CST=136704;
constexpr int WS_O=65536, WS_OM=19*1024, WS_OT=6*WS_OM;
typedef __amdgpu_buffer_rsrc_t rsrc_t;
DEV void dma16o(const v4u& srd, unsigned voff, unsigned soff, unsigned lds){
  asm volatile("s_mov_b32 m0, %3\n\ts_nop 0\n\tbuffer_load_dwordx4 %0, %1, %2 offen" NTO " lds"::"v"(voff),"s"(srd),"s"(soff),"s"(lds):"memory");
}
DEV void dma16(const v4u& srd, unsigned voff, unsigned soff, unsigned lds){
  asm volatile("s_mov_b32 m0, %3\n\ts_nop 0\n\tbuffer_load_dwordx4 %0, %1, %2 offen" NTX " lds"::"v"(voff),"s"(srd),"s"(soff),"s"(lds):"memory");
}
#define DW1 "s_add_u32 m0, m0, 3456\n\ts_add_u32 %0, %0, 27648\n\tbuffer_load_dwordx4 %1, %2, %0 offen lds\n\t"
DEV void dma_w4(const v4u& srd, unsigned voff, unsigned soff, unsigned lds){
  asm volatile("s_lshr_b64 exec, exec, 10\n\ts_mov_b32 m0, %3\n\ts_nop 0\n\tbuffer_load_dwordx4 %1, %2, %0 offen lds\n\t"
               DW1 DW1 DW1 "s_mov_b64 exec, -1"
               :"+s"(soff):"v"(voff),"s"(srd),"s"(lds):"memory","scc");
}
DEV v4u mksrd(const char* p, unsigned bytes){
  const u64 a=(u64)p; return (v4u){(unsigned)a,(unsigned)(a>>32)&0xffffu,bytes,0x00020000u};
}
struct AFr { v4u x[7]; v2u w[4][2]; };
struct AOp { v4u x[7], w[4]; };
template<int WO> DEV void a_rd(int i, AFr& f, unsigned xe, unsigned x0, unsigned xo, unsigned wa, unsigned we){
  switch(i){
    case 0: ATR(f.w[3][0],we,WO); break;      case 1: ATR(f.w[3][1],we,WO+432); break;
    case 2: ARD(f.x[0],xe,0); break;          case 3: ARD(f.x[1],xe,2048); break;   case 4: ARD(f.x[2],xe,4096); break;
    case 5: ATR(f.w[0][0],wa,WO); break;      case 6: ATR(f.w[0][1],wa,WO+432); break;
    case 7: ATR(f.w[1][0],wa,WO+32); break;   case 8: ATR(f.w[1][1],wa,WO+464); break;
    case 9: ATR(f.w[2][0],wa,WO+64); break;   case 10: ATR(f.w[2][1],wa,WO+496); break;
    case 11: ARD(f.x[3],x0,0); break;
    case 12: ARD(f.x[4],xo,0); break;         case 13: ARD(f.x[5],xo,2048); break;  case 14: ARD(f.x[6],xo,4096); break;
  }
}
DEV void a_wait(AFr& f, AOp& o){
  asm volatile("s_waitcnt lgkmcnt(0)":"+v"(f.x[0]),"+v"(f.x[1]),"+v"(f.x[2]),"+v"(f.x[3]),"+v"(f.x[4]),"+v"(f.x[5]),"+v"(f.x[6]),
     "+v"(f.w[0][0]),"+v"(f.w[0][1]),"+v"(f.w[1][0]),"+v"(f.w[1][1]),"+v"(f.w[2][0]),"+v"(f.w[2][1]),"+v"(f.w[3][0]),"+v"(f.w[3][1])::"memory");
  #pragma unroll
  for(int m=0;m<7;m++) o.x[m]=f.x[m];
  #pragma unroll
  for(int c=0;c<4;c++) o.w[c]=cat(f.w[c][0],f.w[c][1]);
}
DEV void a_mm(int i, f4 (&acc)[3][7], f4 (&ace)[3], const AOp& f){
  if(i<3) AMM(ace[i],f.w[3],f.x[i]); else AMM(acc[(i-3)%3][(i-3)/3],f.w[(i-3)%3],f.x[(i-3)/3]);
}
DEV float max3(float a,float b,float c){ float d; asm("v_max3_f32 %0, %1, %2, %3":"=v"(d):"v"(a),"v"(b),"v"(c)); return d; }
DEV float max2(float a,float b){ float d; asm("v_max_f32 %0, %1, %2":"=v"(d):"v"(a),"v"(b)); return d; }
DEV void tr4(unsigned (&a)[4]){
  auto r0=__builtin_amdgcn_permlane16_swap(a[0],a[1],false,false);
  auto r1=__builtin_amdgcn_permlane16_swap(a[2],a[3],false,false);
  auto s0=__builtin_amdgcn_permlane32_swap(r0[0],r1[0],false,false);
  auto s1=__builtin_amdgcn_permlane32_swap(r0[1],r1[1],false,false);
  a[0]=s0[0]; a[1]=s1[0]; a[2]=s0[1]; a[3]=s1[1];
}

DEV unsigned rs4(unsigned a, unsigned b, unsigned c, unsigned d){
  auto x=__builtin_amdgcn_permlane16_swap(a,b,false,false);
  auto y=__builtin_amdgcn_permlane16_swap(c,d,false,false);
  auto z=__builtin_amdgcn_permlane32_swap(x[0]+x[1],y[0]+y[1],false,false);
  return z[0]+z[1];
}
DEV void role_a(const Params& p, int rt, int grp, int rot STAMPARG){
  const int tid=__builtin_amdgcn_workitem_id_x(), lane=tid&63, wave=__builtin_amdgcn_readfirstlane(tid>>6);
  const int row0=rt*A_TM, nrow=imin(A_TM,2048-row0);
  const int i16=lane&15, g=lane>>4;
  GAS unsigned* epoch=(GAS unsigned*)(p.ws+rt*256);
  unsigned ka,kb,qb,va,sel0,sel1,sel2,sel3; v4u vl=(v4u){0,0,0,0}; unsigned vl4=0;
  const int par=i16&1; const bool cur0=(4*g+par<=i16), cur1=(4*g+par+2<=i16);
  const rsrc_t wsr=__builtin_amdgcn_make_buffer_rsrc(p.ws+WS_O,0,23*WS_OT,0x00020000);
  if(wave>=4){
    const int lw=wave-4;
    const v4u sx=mksrd(p.x,2048*1152), sw=mksrd(p.wqkv,576*3456), so=mksrd(p.wo,576*1152);
    int st=rot, cnt=0;
    unsigned sl=lw*1024;
    const int l27=lane>=27? lane-27:lane, seg=(l27>=18)?2:(l27>=9?1:0);
    const unsigned vw=(lane>=27?4*3456:0)+seg*(1152-144)+l27*16+grp*144+lw*3456;
    dma_w4(sw,vw,st*64*3456,sl+A_WOFF-lw*160);
    __builtin_amdgcn_s_setprio(3);
    unsigned vx[4];
    #pragma unroll
    for(int i=0;i<4;i++){
      const int ir=8*(lw+4*i)+(lane>>3); int tok=row0-16+ir; tok=imax(tok,imax(row0-14,0)); tok=imin(tok,row0+nrow-1);
      vx[i]=tok*1152+(((lane&7)^((ir>>1)&7))<<4);
    }
    #define A_XA() do{ dma16(sx,vx[0],st*128,sl); dma16(sx,vx[1],st*128,sl+4096); }while(0)
    #define A_HA() do{ dma_w4(sw,vw,st*64*3456,sl+A_WOFF-lw*160); A_XA(); }while(0)
    #define A_HB() do{ dma16(sx,vx[2],st*128,sl+8192); if(lw<2) dma16(sx,vx[3],st*128,sl+12288); dma_w4(sw,vw,(st*64+32)*3456,sl+A_WOFF-lw*160+13824); \
      cnt++; st=(cnt==8)?8:(cnt^rot); sl=(sl>=2*A_SLOT)?sl-2*A_SLOT:sl+A_SLOT; }while(0)
    A_XA(); A_HB();
    {
      const int pr=rt/6, id=(rt-6*pr)*4+(grp&3), u=tid&127; const char* a;
      if(lw<2){
        const int per=(pr==3)?108:90, pw=(pr==3)?36:30;
        const int l=id*per+u, r=l/15, c=l-r*15, m=imin(id*pw+imin(u-per,pw-1),719), ro=m/5;
        a=(u<per)? p.wqkv+((pr*144+r)*3456+(c/5)*1152+(c%5)*128) : p.wo+((pr*144+ro)*1152+(m-ro*5)*128);
        a+=(grp>>2)*512;
      } else a=p.x+imin((row0+grp*12)*1152+imin(u,107)*128,2048*1152-128);
      asm volatile("s_mov_b32 m0, 0x27e00\n\ts_nop 0\n\tglobal_load_lds_dword %0, off"::"v"(a):"memory");
    }
    A_HA();
    WAITV(7);
    BAR;
    #define LSTAMP(n)
    #define LUN _Pragma("unroll 1")
    LUN
    for(int n=1;n<=NH;n++){ A_HB(); A_HA(); WAITV(6); BAR; LSTAMP(n) }
    A_HB();
    LUN
    for(int n=NH+1;n<8;n++){ A_HA(); A_HB(); if(lw<2) WAITV(12); else WAITV(11); BAR; LSTAMP(n) }
    const v4u c0=ld128(A_CST+tid*16-4096), c1=ld128(A_CST+tid*16), c2=ld128(A_CST+tid*16+4096);
    vl=ld128(A_CST+tid*16+8192);
    ka=c0.x; kb=c0.y; qb=c0.z; va=c0.w; sel0=c1.x; sel1=c1.y; sel2=c1.z; sel3=c1.w; vl4=c2.w;
    #define A_ISSUE_WO(P0,P1) do{ _Pragma("unroll 1") for(int P=(P0);P<(P1);P++){ \
        dma16o(so,c2.x,P*72*1152,P*9216+lw*1024); dma16o(so,c2.y,P*72*1152,P*9216+lw*1024+4096); \
        if((P&3)==lw) dma16o(so,c2.z,P*72*1152,P*9216+8192); } }while(0)
    A_ISSUE_WO(0,2);
    if(lw<2) WAITV(5); else WAITV(4);
    BAR;
    STAMP(13);
    A_ISSUE_WO(2,5);
    BAR;
    A_ISSUE_WO(5,8);
    __builtin_amdgcn_s_setprio(0);
    STAMP(2);
  } else {
    unsigned ep;
    asm volatile("global_load_dword %0, %1, %2 sc1":"=v"(ep):"v"(0),"s"(epoch):"memory");
    { const unsigned plo=par?0x01000c0cu:0x0c0c0100u, phi=par?0x03020c0cu:0x0c0c0302u, pzz=0x0c0c0c0cu;
      sel0=cur0?pzz:plo; sel1=cur1?pzz:phi; sel2=cur0?plo:pzz; sel3=cur1?phi:pzz;
      ka=A_IMG+(16*wave+i16)*A_IROW+8*g; kb=(g==0)?ka+32:A_ZERO-144; qb=(g==0)?ka+32+16*A_IROW:A_ZERO;
      va=A_IMG+(16*wave+4*g+(i16>>2))*A_IROW+288+(lane&3)*8;
      const unsigned d=64*A_IROW, dz=(g==0)?d:0u;
      st128(A_CST+tid*16,(v4u){ka+d,kb+dz,qb+dz,va+d}); st128(A_CST+tid*16+4096,(v4u){sel0,sel1,sel2,sel3});
      unsigned vo[3], vv[5];
      #pragma unroll
      for(int i=0;i<3;i++){
        const int ci=(i<2? wave+4*i : 8)*64+lane, rho=ci/9, cc=ci-rho*9;
        vo[i]=(32*(rho>>5)+16*(rho&1)+4*((rho>>3)&3)+((rho>>1)&3))*1152+grp*144+cc*16;
      }
      #pragma unroll
      for(int i=0;i<5;i++){
        const int ci=imin((wave&1)+2*i,8)*64+lane, rho=ci/9, cc=ci-rho*9;
        vv[i]=(72*(rho>>3)+64+4*(rho&1)+((rho>>1)&3))*1152+grp*144+cc*16;
      }
      st128(A_CST+tid*16+8192,(v4u){vo[0],vo[1],vo[2],vv[4]}); st128(A_CST+tid*16+12288,(v4u){vv[0],vv[1],vv[2],vv[3]});
      asm volatile("":"+v"(ka),"+v"(kb),"+v"(qb),"+v"(va),"+v"(sel0),"+v"(sel1),"+v"(sel2),"+v"(sel3)); }
    f4 acc[3][7], ace[3];
    #pragma unroll
    for(int c=0;c<3;c++){ ace[c]=(f4){0,0,0,0};
      #pragma unroll
      for(int m=0;m<7;m++) acc[c][m]=(f4){0,0,0,0}; }
    const int e0=(wave&1)?4:1, o0=(wave&1)?1:4;
    const unsigned xl=i16*128+((g^(i16>>1))<<4);
    const unsigned wl=A_WOFF+(8*g+2*(i16>>2))*432+(lane&3)*8;
    unsigned ae0=xl+e0*2048, az0=xl, ao0=xl+o0*2048, ae1=ae0^64, az1=az0^64, ao1=ao0^64, wa=wl+(2+3*wave)*32, we=wl+(wave>>1)*32;
    const unsigned ia=A_IMG+i16*A_IROW+g*8;
    unsigned ie=ia+e0*16*A_IROW+(2+3*wave)*32, iz=ia+(2+3*wave)*32, io=ia+o0*16*A_IROW+(2+3*wave)*32, ix=ia+e0*16*A_IROW+(wave>>1)*32;
    asm volatile("":"+v"(ae0),"+v"(az0),"+v"(ao0),"+v"(ae1),"+v"(az1),"+v"(ao1),"+v"(wa),"+v"(we),"+v"(ie),"+v"(iz),"+v"(io),"+v"(ix));
    #pragma unroll
    for(int c=0;c<3;c++){ asm volatile("":"+v"(ace[c]));
      #pragma unroll
      for(int m=0;m<7;m++) asm volatile("":"+v"(acc[c][m])); }
    if(tid<72) st32(A_ZERO+tid*4,0u);
    WAIT1(0,ep);
    if(tid==0) st32(A_TAG,ep*2891336453u+1013904223u);
    int slot=0;
    AFr f0,f1; AOp g0,g1;
    ldsbar();
    STAMP(1);
    #pragma unroll
    for(int i=0;i<15;i++) a_rd<0>(i,f0,ae0,az0,ao0,wa,we);
    a_wait(f0,g0);
    #pragma unroll 1
    for(int s=0;s<8;s++){
      const int dl=(slot==2)?-2*A_SLOT:A_SLOT; slot=(slot==2)?0:slot+1;
      #pragma unroll
      for(int i=0;i<24;i++){
        a_mm(i,acc,ace,g0);
        if(i<15) a_rd<13824>(i,f1,ae1,az1,ao1,wa,we);
        if(i==15) AADD(ae0,dl); if(i==16) AADD(az0,dl); if(i==17) AADD(ao0,dl); if(i==18) AADD(wa,dl); if(i==19) AADD(we,dl);
      }
      a_wait(f1,g1);
      BAR;

      #pragma unroll
      for(int i=0;i<24;i++){
        a_mm(i,acc,ace,g1);
        if(i<15) a_rd<0>(i,f0,ae0,az0,ao0,wa,we);
        if(i==15) AADD(ae1,dl); if(i==16) AADD(az1,dl); if(i==17) AADD(ao1,dl);
      }
      a_wait(f0,g0);
    }
    #pragma unroll
    for(int i=0;i<24;i++){
      a_mm(i,acc,ace,g0);
      if(i<15) a_rd<13824>(i,f1,ae1,az1,ao1,wa,we);
    }
    a_wait(f1,g1);
    BAR;
    SB;
    #pragma unroll
    for(int m=0;m<3;m++) ace[m]=mfma(g1.w[3],g1.x[m],ace[m]);
    #pragma unroll
    for(int m=0;m<7;m++){
      #pragma unroll
      for(int c=0;c<3;c++) acc[c][m]=mfma(g1.w[c],g1.x[m],acc[c][m]);
    }
    #pragma unroll
    for(int m=0;m<3;m++) st64(ix+m*16*A_IROW,(v2u){pk2(ace[m][0],ace[m][1]),pk2(ace[m][2],ace[m][3])});
    #pragma unroll
    for(int m=0;m<7;m++){
      const unsigned ib=(m<3)?ie+m*16*A_IROW:(m==3?iz:io+(m-4)*16*A_IROW);
      #pragma unroll
      for(int c=0;c<3;c++) st64v(ib+c*32,(v2u){pk2(acc[c][m][0],acc[c][m][1]),pk2(acc[c][m][2],acc[c][m][3])});
    }
    SGB(0x8,3);
    #pragma unroll
    for(int i=0;i<21;i++){ SGB(0x8,1); SGB(0x2,2); SGB(0x200,1); }
    SB;
    STAMP(2);
  }
  ldsbar();
  STAMP(3);
  const unsigned tag=ld32(A_TAG);
  if(wave>=6){
    const v4u so=mksrd(p.wo,576*1152);
    #pragma unroll
    for(int i=0;i<5;i++) if(i<4 || wave==6) dma16o(so,i<4?vl[i]:vl4,0,8*9216+(wave&1)*1024+i*2048);
    WAITV(0);
  } else {
    v4u qf[4],kp[4],kc[4],vf[5];
    #pragma unroll
    for(int h=0;h<4;h++){
      const int hb=36*h-4*(h&1);
      qf[h]=cat(ld64v(ka+16*A_IROW+hb),ld64v(qb+hb));
      kp[h]=cat(ld64v(ka+144+hb),ld64v(kb+144+hb));
      kc[h]=cat(ld64v(ka+16*A_IROW+144+hb),ld64v(qb+144+hb));
    }
    #pragma unroll
    for(int c=0;c<5;c++) vf[c]=cat(ldtr(va+32*c),ldtr(va+16*A_IROW+32*c));
    SB;
    f4 sp[4],sc[4];
    #pragma unroll
    for(int h=0;h<4;h++){
      v4u q=qf[h];
      if(h&1) q.x=(g==0)?0u:q.x; else q.w=0u;
      sp[h]=mfma(kp[h],q,(f4){0,0,0,0}); sc[h]=mfma(kc[h],q,(f4){0,0,0,0});
    }
    SB;
    unsigned a0[4],a1[4];
    #pragma unroll
    for(int h=0;h<4;h++){
      const float c0=sc[h][0],c1=sc[h][1],c2=sc[h][2],c3=sc[h][3],p0=sp[h][0],p1=sp[h][1],p2=sp[h][2],p3=sp[h][3];
      const float ca=par?c1:c0, pa=par?p1:p0, cb=par?c3:c2, pb=par?p3:p2;
      a0[h]=__builtin_bit_cast(unsigned,cur0?ca:pa);
      a1[h]=__builtin_bit_cast(unsigned,cur1?cb:pb);
    }
    if(row0+16*wave==0){
      const unsigned mk=__builtin_bit_cast(unsigned,-1e9f/0.23570226039551584f);
      #pragma unroll
      for(int h=0;h<4;h++){ a0[h]=cur0?a0[h]:mk; a1[h]=cur1?a1[h]:mk; }
    }
    tr4(a0); tr4(a1);
    float l[8];
    #pragma unroll
    for(int i=0;i<4;i++){ l[2*i]=__builtin_bit_cast(float,a0[i]); l[2*i+1]=__builtin_bit_cast(float,a1[i]); }
    constexpr float SC=0.23570226039551584f*1.4426950408889634f;
    const float mx=-SC*max3(max3(l[0],l[1],l[2]),max3(l[3],l[4],l[5]),max2(l[6],l[7]));
    #pragma unroll
    for(int i=0;i<8;i++) l[i]=__builtin_amdgcn_exp2f(__builtin_fmaf(l[i],SC,mx));
    const float inv=__builtin_amdgcn_rcpf(((l[0]+l[1])+(l[2]+l[3]))+((l[4]+l[5])+(l[6]+l[7])));
    unsigned pp[4];
    #pragma unroll
    for(int i=0;i<4;i++) pp[i]=pk2(l[2*i]*inv,l[2*i+1]*inv);
    tr4(pp);
    v4u pf[4];
    #pragma unroll
    for(int h=0;h<4;h++) pf[h]=(v4u){__builtin_amdgcn_perm(0u,pp[h],sel0),__builtin_amdgcn_perm(0u,pp[h],sel1),__builtin_amdgcn_perm(0u,pp[h],sel2),__builtin_amdgcn_perm(0u,pp[h],sel3)};
    const f4 z={0,0,0,0};
    const f4 o0=mfma(vf[0],pf[0],z), a1_=mfma(vf[1],pf[0],z), b1=mfma(vf[1],pf[1],z), a2=mfma(vf[2],pf[1],z),
             b2_=mfma(vf[2],pf[2],z), a3=mfma(vf[3],pf[2],z), b3=mfma(vf[3],pf[3],z), o4=mfma(vf[4],pf[3],z);
    const v4u t01={pk2(o0[0],o0[1]),pk2(o0[2],o0[3]), (g==0)?pk2(a1_[0],a1_[1]):pk2(b1[0],b1[1]), pk2(b1[2],b1[3])};
    const v4u t23={(g==0)?pk2(a2[0],a2[1]):pk2(b2_[0],b2_[1]), (g==0)?pk2(a2[2],a2[3]):pk2(b2_[2],b2_[3]),
                   (g<2)?pk2(a3[0],a3[1]):pk2(b3[0],b3[1]), (g==0)?pk2(a3[2],a3[3]):pk2(b3[2],b3[3])};
    const unsigned u0=pk2(o4[0],o4[1]), u1=pk2(o4[2],o4[3]);
    auto r0=__builtin_amdgcn_permlane16_swap(u0,u0,false,false);
    auto r1=__builtin_amdgcn_permlane16_swap(u1,u1,false,false);
    const unsigned ob=rt*WS_OT+wave*WS_OM;
    __builtin_amdgcn_raw_buffer_store_b128(t01,wsr,ob+2*grp*1024+lane*16,0,16);
    __builtin_amdgcn_raw_buffer_store_b128(t23,wsr,ob+2*grp*1024+1024+lane*16,0,16);
    const v4u t4={(unsigned)r0[0],(unsigned)r1[0],(unsigned)r0[1],(unsigned)r1[1]};
    unsigned hs=sum5(sum5(0,t01),t23);
    { auto x=__builtin_amdgcn_permlane16_swap(hs,hs,false,false); hs=x[0]+x[1];
      auto y=__builtin_amdgcn_permlane32_swap(hs,hs,false,false); hs=y[0]+y[1]; }
    if(g==0){
      __builtin_amdgcn_raw_buffer_store_b128(t4,wsr,ob+16*1024+grp*256+lane*16,0,16);
      __builtin_amdgcn_raw_buffer_store_b64((v2u){sum5(hs,t4)+tag,tag},wsr,ob+18*1024+grp*128+lane*8,0,16);
    }
    WAITV(4);
  }
  STAMP(4);
  BAR;
  STAMP(5);
  {
    const int hf=wave&1, mo=(wave<4)?wave:4+((wave-4)>>1), m0=(wave<4)?(wave^1):mo;
    const unsigned wa=(8*g+2*(i16>>2))*144+(lane&3)*8, wb=wa+hf*8*4608, wc=wa+(16+hf)*4608;
    const v4u so=mksrd(p.ws+WS_O,23*WS_OT);
    const unsigned vo=lane*16;
    const int s0=rt*WS_OT+m0*WS_OM+hf*8192, s1=rt*WS_OT+mo*WS_OM+hf*8192, dl=16384-hf*7168, dr=18432-hf*7680;
    #define WFR(a,ks,c) cat(ldtr((a)+(ks)*4608+(c)*32),ldtr((a)+(ks)*4608+(c)*32+144))
    #define OLD(d,S,k) asm volatile("buffer_load_dwordx4 %0, %1, %2, %3 offen offset:%4 sc1":"=v"(d):"v"(vo),"s"(so),"s"((S)+((k)==8?dl:((k)>>2)*4096)),"i"(((k)&3)*1024):"memory")
    #define RLD(d,S) asm volatile("buffer_load_dwordx2 %0, %1, %2, %3 offen sc1":"=v"(d):"v"(vo>>1),"s"(so),"s"((S)+dr):"memory")
    v4u wf[9][5], of[9]; v2u r0,r1; f4 acc[5];
    #define ATT { RLD(r0,s0); RLD(r1,s1); _Pragma("unroll") for(int k_=0;k_<PNF;k_++) OLD(of[k_],s0,k_); }
    #pragma unroll
    for(int k=0;k<9;k++){
      #pragma unroll
      for(int c=0;c<5;c++) wf[k][c]=(k<8)?WFR(wb,k,c):WFR(wc,0,c);
      if(k==PNISS){ SB; ATT SB; }
    }
    WAITL;
    #pragma unroll
    for(int k=0;k<9;k++){
      #pragma unroll
      for(int c=0;c<5;c++) asm volatile("":"+v"(wf[k][c]));
    }
    STAMP(12);
    const unsigned red=A_IMG+lane*16, rf=A_ZERO+260;
    if(wave>=4) __builtin_amdgcn_s_setprio(1);
    #define UNIT(NEXT,R,OK) { unsigned hs[5]; \
      _Pragma("unroll") for(int k=0;k<9;k++){ \
        asm volatile("s_waitcnt vmcnt(%1)":"+v"(of[k]):"i"((NEXT)?8:8-k):"memory"); \
        _Pragma("unroll") for(int c=0;c<5;c++){ \
          if(k==0) asm volatile("v_mfma_f32_16x16x32_bf16 %0, %1, %2, 0":"=&v"(acc[c]):"v"(wf[k][c]),"v"(of[k])); else AMM(acc[c],wf[k][c],of[k]); \
          if(c==0){ if(k&1) asm volatile("v_add3_u32 %0, %0, %1, %2":"+v"(hs[k>>1]):"v"(of[k].x),"v"(of[k].y)); \
                    else asm volatile("v_add_u32 %0, %1, %2":"=v"(hs[k>>1]):"v"(of[k].x),"v"(of[k].y)); } \
          if(c==1) asm volatile("v_add3_u32 %0, %0, %1, %2":"+v"(hs[k>>1]):"v"(of[k].z),"v"(of[k].w)); } \
        if(NEXT) OLD(of[k],s1,k); } \
      asm volatile("s_nop 7\n\ts_nop 3":::"memory"); SB; \
      OK=__builtin_amdgcn_ballot_w64(rs4(hs[0],hs[1],hs[2],hs[3])+hs[4]+tag!=R.x)==0; }
    #define SEND(m) { _Pragma("unroll") for(int c=0;c<5;c++) st128(red+(m)*5120+c*1024,__builtin_bit_cast(v4u,acc[c])); asm volatile("":::"memory"); *(volatile LAS unsigned*)(rf+4*(m))=1u; }
    #define PWAIT(m) { _Pragma("unroll 1") for(int spin=0;spin<(1<<24);spin++) if(__builtin_amdgcn_readfirstlane(*(volatile LAS unsigned*)(rf+4*(m)))) break; asm volatile("":::"memory"); }
    #pragma unroll 1
    for(int tries=0;tries<64;tries++){
      #pragma unroll 1
      for(int spin=0;spin<(1<<20);spin++){
        asm volatile("s_waitcnt vmcnt(%2)":"+v"(r0),"+v"(r1):"i"(PNF):"memory");
        if(__builtin_amdgcn_ballot_w64(r0.y!=tag || r1.y!=tag)==0) break;
        WAITV(0);
        ATT
      }
      #pragma unroll
      for(int k=PNF;k<9;k++) OLD(of[k],s0,k);
      STAMP(6);
      bool ok0=true, ok1;
      if(wave<4){
        UNIT(1,r0,ok0)
        STAMP(10);
        if(ok0) SEND(m0)
      }
      STAMP(11);
      UNIT(0,r1,ok1)
      if(ok0 && ok1) break;
      ATT
    }
    if(wave>=4 && hf) SEND(mo)
    else { PWAIT(mo)
      #pragma unroll
      for(int c=0;c<5;c++) acc[c]+=__builtin_bit_cast(f4,ld128(red+mo*5120+c*1024)); }
    STAMP(7);
    if(wave<4 || !hf){
      const unsigned tb=A_IMG+30720+mo*2560;
      #pragma unroll
      for(int c=0;c<5;c++) st64(tb+i16*160+g*8+c*32,(v2u){pk2(acc[c][0],acc[c][1]),pk2(acc[c][2],acc[c][3])});
      const int l9=(lane*57)>>9, c9=lane-l9*9;
      const rsrc_t orr=__builtin_amdgcn_make_buffer_rsrc(p.out+(size_t)(row0+mo*16)*2304+grp*144,0,__builtin_amdgcn_readfirstlane(imax(imin(nrow-mo*16,16),0)*2304),0x00020000);
      const unsigned oo=(lane<63)? l9*2304+c9*16 : 0x40000000u;
      #pragma unroll
      for(int i=0;i<3;i++) __builtin_amdgcn_raw_buffer_store_b128(ld128(tb+(7*i+l9)*160+c9*16),orr,oo+i*7*2304,0,0);
    }
    asm volatile(".Lcpf1:");
    if(tid==0 && grp==0) __hip_atomic_store(epoch,tag,__ATOMIC_RELAXED,__HIP_MEMORY_SCOPE_AGENT);
  }
  STAMP(8);
}

NOPK __global__ __launch_bounds__(512,2) void fused(Params p){
  STAMPDECL;
  STAMP(0);
  const int b=__builtin_amdgcn_workgroup_id_x(), xcd=b&7, j=b>>3, pr=xcd>>1, half=xcd&1;
  const int nA=(pr<3)?24:20;
  {
    u64 pc; unsigned len;
    asm volatile("s_getpc_b64 %0\n.Lcpf0:\n\ts_mov_b32 %1, .Lcpf1-.Lcpf0":"=s"(pc),"=s"(len));
    const unsigned t=__builtin_amdgcn_workitem_id_x();
    if(t<40){ unsigned o=((j&7)+t*8)*128; o=o<len?o:len;
      asm volatile("s_mov_b32 m0, 0x27f00\n\ts_nop 0\n\tglobal_load_lds_dword %0, %1"::"v"(o&~3u),"s"(pc):"memory"); }
  }
  if(j>=nA){
    const int jb=j-nA;
    role_b(p, jb<8? xcd*8+jb : 64+half*4+(jb-8) STAMPPASS);
  } else
    role_a(p, 6*pr+(j>>2), half*4+(j&3), ((j>>2)+j)&1 STAMPPASS);
}

constexpr int LDS_BYTES=160*1024;
extern "C" void kernel_launch(void* const* d_in, const int* in_sizes, int n_in, void* d_out, int out_size, void* d_ws, size_t ws_size, hipStream_t stream){
  Params p;
  p.x=(const char*)d_in[0]; p.wqkv=(const char*)d_in[1]; p.wo=(const char*)d_in[2];
  p.q=(const char*)d_in[3]; p.k=(const char*)d_in[4]; p.v=(const char*)d_in[5];
  p.sk=(const char*)d_in[6]; p.hk=(const char*)d_in[7]; p.dv=(const char*)d_in[8];
  p.out=(char*)d_out; p.ws=(char*)d_ws;
  (void)hipFuncSetAttribute((const void*)fused,hipFuncAttributeMaxDynamicSharedMemorySize,LDS_BYTES);
  fused<<<256,512,LDS_BYTES,stream>>>(p);
}
```

```cpp
#include <hip/hip_runtime.h>
#include <hip/hip_bf16.h>

typedef unsigned short u16;
typedef unsigned long long u64;
typedef unsigned v4u __attribute__((ext_vector_type(4)));
typedef unsigned v2u __attribute__((ext_vector_type(2)));
typedef short s4 __attribute__((ext_vector_type(4)));
typedef __bf16 b2 __attribute__((ext_vector_type(2)));
typedef __bf16 b8 __attribute__((ext_vector_type(8)));
typedef float f4 __attribute__((ext_vector_type(4)));
typedef float f2 __attribute__((ext_vector_type(2)));
#define LAS __attribute__((address_space(3)))
#define GAS __attribute__((address_space(1)))
#if defined(__HIP_DEVICE_COMPILE__)
#define NOPK __attribute__((target("no-packed-fp32-ops")))
#else
#define NOPK
#endif
#define DEV __device__ __forceinline__ NOPK
#define SB __builtin_amdgcn_sched_barrier(0)
#define SGB(m,n) __builtin_amdgcn_sched_group_barrier(m,n,0)
#pragma clang diagnostic ignored "-Wint-to-pointer-cast"

#define STAMP(i)
#define CSTAMP(i)
#define STAMPDECL
#define STAMPARG
#define STAMPPASS
#define STAMPOUT

#define PNPRE 6
#define PNISS 3
#define PNF 6
#define ALOOK 2
#define NOAT
#define ROT 22
#define NOPR
#define BV 1
#define CPFN 32
#define NTB " nt"
#define NTQ ""
#define NTX ""
#define NTW ""
#define NTO ""
#define BSTAMP(i)
#define NSTAMP(i) STAMP(i)
#define BSLP 16
#define MTS 0
#define WOV 2
#define STV 1
#define NH 1
#define B1L
#define HSL 20
#define TS1 4
#define T2N 2
#define BS 2
#define IS_B
static int is_b(int b){ int j=b>>3,pr=(b&7)>>1; if(pr==0) j-=4; if(j<0) return 2; return j>=((pr==0||pr==3)?20:24); }
struct Params {
  const char *x, *wqkv, *wo, *q, *k, *v, *sk, *hk, *dv;
  char *out, *ws;
};

DEV float lo2f(unsigned d){ return __builtin_bit_cast(float,d<<16); }
DEV float hi2f(unsigned d){ return __builtin_bit_cast(float,d&0xffff0000u); }
DEV unsigned pk2(float lo, float hi){ f2 p={lo,hi}; return __builtin_bit_cast(unsigned,__builtin_convertvector(p,b2)); }
DEV unsigned pk2a(float lo, float hi){ unsigned d; asm("v_cvt_pk_bf16_f32 %0, %1, %2":"=v"(d):"v"(lo),"v"(hi)); return d; }
DEV v4u ld128(unsigned a){ return *(LAS v4u*)a; }
DEV v2u ld64v(unsigned a){ return *(volatile LAS v2u*)a; }
DEV unsigned ld32(unsigned a){ return *(LAS unsigned*)a; }
DEV void st128(unsigned a, v4u v){ *(LAS v4u*)a=v; }
DEV void st64(unsigned a, v2u v){ *(LAS v2u*)a=v; }
DEV void st64v(unsigned a, v2u v){ *(volatile LAS v2u*)a=v; }
DEV void st32(unsigned a, unsigned v){ *(LAS unsigned*)a=v; }
DEV v2u ldtr(unsigned a){ return __builtin_bit_cast(v2u,__builtin_amdgcn_ds_read_tr16_b64_v4i16((LAS s4*)a)); }
DEV f4 mfma(v4u a, v4u b, f4 c){ return __builtin_amdgcn_mfma_f32_16x16x32_bf16(__builtin_bit_cast(b8,a),__builtin_bit_cast(b8,b),c,0,0,0); }
DEV v4u cat(v2u a, v2u b){ return (v4u){a.x,a.y,b.x,b.y}; }
DEV v4u gld128(const char* p){ return *(const GAS v4u*)p; }
DEV void gloadq(v4u& d, const char* sbase, unsigned voff){ asm volatile("global_load_dwordx4 %0, %1, %2" NTQ:"=v"(d):"v"(voff),"s"(sbase):"memory"); }
DEV void gload(v4u& d, const char* sbase, unsigned voff){ asm volatile("global_load_dwordx4 %0, %1, %2" NTB:"=v"(d):"v"(voff),"s"(sbase):"memory"); }
DEV void greload(v4u& d, const char* sbase, unsigned voff){
 asm volatile("global_load_dwordx4 %0, %1, %2":"+v"(d):"v"(voff),"s"(sbase):"memory"); }
#define WAIT1(n,a) asm volatile("s_waitcnt vmcnt(" #n ")":"+v"(a)::"memory")
#define WAIT2(n,a,b) asm volatile("s_waitcnt vmcnt(" #n ")":"+v"(a),"+v"(b)::"memory")
#define WAIT3(n,a,b,c) asm volatile("s_waitcnt vmcnt(" #n ")":"+v"(a),"+v"(b),"+v"(c)::"memory")
#define WAIT4(n,a,b,c,d) asm volatile("s_waitcnt vmcnt(" #n ")":"+v"(a),"+v"(b),"+v"(c),"+v"(d)::"memory")
#define WAITL asm volatile("":::"memory"); __builtin_amdgcn_s_waitcnt(0xC07F); asm volatile("":::"memory")
DEV void ldsbar(){ WAITL; __builtin_amdgcn_s_barrier(); asm volatile("":::"memory"); }
DEV int imin(int a,int b){ return a<b?a:b; }
DEV int imax(int a,int b){ return a>b?a:b; }
#define WAITV(n) asm volatile("s_waitcnt vmcnt(" #n ")":::"memory")
#define BAR do{ asm volatile("":::"memory"); __builtin_amdgcn_s_barrier(); asm volatile("":::"memory"); }while(0)
template<int NR,int NM> DEV void interleave(){
  constexpr int PER=(NR+NM-1)/NM, NG=(NR+PER-1)/PER;
  #pragma unroll
  for(int i=0;i<NG;i++){ SGB(0x8,1); SGB(0x100,PER); }
  if(NM>NG) SGB(0x8,NM-NG);
}

#define AMM(acc,a,b) asm volatile("v_mfma_f32_16x16x32_bf16 %0, %1, %2, %0":"+v"(acc):"v"(a),"v"(b))
#define ARD(d,a,off) asm volatile("ds_read_b128 %0, %1 offset:%2":"=v"(d):"v"(a),"i"(off):"memory")
#define ATR(d,a,off) asm volatile("ds_read_b64_tr_b16 %0, %1 offset:%2":"=v"(d):"v"(a),"i"(off):"memory")
#define AADD(v,s) asm volatile("v_add_u32 %0, %1, %0":"+v"(v):"s"(s))
#define AR64(d,a,off) asm volatile("ds_read_b64 %0, %1 offset:%2":"=v"(d):"v"(a),"i"(off):"memory")
constexpr int B_HA=448, B_HB=4576+448, B_X0=9120, B_X1=13344, B_ST=17520, B_PITCH=19696, B_SCR=8*B_PITCH;

template<int BLO,int NB> struct PF { v4u h[8-BLO]; v2u x[NB][2]; };
template<int BLO,int NB,int OFF> DEV void p_rd(int i, PF<BLO,NB>& f, unsigned hb, unsigned xb){
  #pragma unroll
  for(int a=0;a<8-BLO;a++) if(i==2+a) ARD(f.h[a],hb,512*a+OFF);
  #pragma unroll
  for(int b=0;b<NB;b++){ const int q=b?8-BLO+2*b:0; if(i==q) AR64(f.x[b][0],xb,512*(7-BLO-b)+OFF); if(i==q+1) AR64(f.x[b][1],xb,512*(7-BLO-b)+8+OFF); }
}
template<int BLO,int NB> DEV void p_mm(int i, f4 (&acc)[8], PF<BLO,NB>& f){
  int n=0;
  #pragma unroll
  for(int b=0;b<NB;b++){
    #pragma unroll
    for(int a=0;a<8-BLO-b;a++){ if(n==i){ v4u x=cat(f.x[b][0],f.x[b][1]); AMM(acc[a+b+BLO],f.h[a],x); } n++; }
  }
}
template<int N,int BLO,int NB> DEV void p_wait(PF<BLO,NB>& f){
  asm volatile("s_waitcnt lgkmcnt(%0)"::"i"(N):"memory");
  #pragma unroll
  for(int a=0;a<8-BLO;a++) asm volatile("":"+v"(f.h[a]));
  #pragma unroll
  for(int b=0;b<NB;b++) asm volatile("":"+v"(f.x[b][0]),"+v"(f.x[b][1]));
}
template<int BLO,int NB,int OFF> DEV void p_block(f4 (&acc)[8], PF<BLO,NB>& fc, PF<BLO,NB>& fn, unsigned hb, unsigned xb){
  constexpr int NR=8-BLO+2*NB, NM=NB*(8-BLO)-NB*(NB-1)/2, EX=(NR>NM)?NR-NM:0;
  constexpr int I2=(NB==2? 6-BLO : (NB-2)*(8-BLO)-(NB-2)*(NB-3)/2);
  p_wait<4>(fc);
  #pragma unroll
  for(int i=0;i<NM;i++){
    if(i==I2) asm volatile("s_waitcnt lgkmcnt(%0)"::"i"(I2+EX<15?I2+EX:15):"memory");
    p_mm<BLO,NB>(i,acc,fc);
    if(i<EX){ p_rd<BLO,NB,OFF>(2*i,fn,hb,xb); p_rd<BLO,NB,OFF>(2*i+1,fn,hb,xb); }
    else if(i+EX<NR) p_rd<BLO,NB,OFF>(i+EX,fn,hb,xb);
  }
}
template<int BLO,int NB> DEV void b_seg(f4 (&acc)[8], PF<BLO,NB>& f0, unsigned hb, unsigned xb, int next){
  PF<BLO,NB> f1;
  #pragma unroll 1
  for(int r=0;r<4;r++){
    p_block<BLO,NB,64>(acc,f0,f1,hb,xb);
    const int d=(r==3)? next-384 : 128;
    AADD(hb,d); AADD(xb,d);
    p_block<BLO,NB,0>(acc,f1,f0,hb,xb);
  }
}
template<int JLO,int NJ> DEV void b_zout(f4 (&acc)[8], unsigned za){
  asm volatile("s_nop 7\n\ts_nop 3":::"memory");
  #pragma unroll
  for(int J=JLO;J<JLO+NJ;J++){
    asm volatile("":"+v"(acc[J]));
    st32(za+512*(7-J)+64,pk2a(acc[J][1],acc[J][0])); st32(za+512*(7-J),pk2a(acc[J][3],acc[J][2]));
  }
}
DEV void flag_wait(unsigned a, unsigned n){
  #pragma unroll 1
  for(int spin=0;spin<(1<<24);spin++){ if(__builtin_amdgcn_ballot_w64(*(volatile LAS unsigned*)a<n)==0) break; __builtin_amdgcn_s_sleep(1); }
  asm volatile("":::"memory");
}
template<int BLO,int NB> DEV void b_stage(f4 (&a0)[8], f4 (&a1)[8], unsigned hb, unsigned xb, unsigned za){
  PF<BLO,NB> f;
  #pragma unroll
  for(int i=2;i<10-BLO;i++) p_rd<BLO,NB,0>(i,f,hb,xb);
  ldsbar();
  p_rd<BLO,NB,0>(0,f,hb,xb); p_rd<BLO,NB,0>(1,f,hb,xb);
  #pragma unroll
  for(int i=10-BLO;i<8-BLO+2*NB;i++) p_rd<BLO,NB,0>(i,f,hb,xb);
  asm volatile("s_waitcnt lgkmcnt(%0)"::"i"(2*NB-2):"memory");
  b_seg<BLO,NB>(a0,f,hb,xb,B_PITCH);
  b_zout<BLO,NB>(a0,za);
  b_seg<BLO,NB>(a1,f,hb+B_PITCH,xb+B_PITCH,0);
  b_zout<BLO,NB>(a1,za+B_PITCH);
  p_wait<0>(f);
}

DEV unsigned sum5(unsigned m, v4u v){ return (m+v.x+v.y)+v.z+v.w; }
DEV void b_prep2(unsigned ta, unsigned tv, v4u sl0, v4u sh0, v4u sl1, v4u sh1, v4u i01, v4u i23, v4u& oa, v4u& ob){
  v4u a[2][4], vr[2];
  #pragma unroll
  for(int t=0;t<2;t++){ vr[t]=ld128(tv+t*B_PITCH); a[t][0]=ld128(ta+t*B_PITCH); a[t][2]=ld128(ta+t*B_PITCH+64); }
  #pragma unroll
  for(int t=0;t<2;t++){ a[t][1]=ld128(ta+t*B_PITCH+512); a[t][3]=ld128(ta+t*B_PITCH+576); }
  SB;
  const f4 z={0,0,0,0};
  f4 v1[2],v2[2],d1[2],d2[2];
  #pragma unroll
  for(int t=0;t<2;t++){ v1[t]=mfma(vr[t],i01,z); v2[t]=mfma(vr[t],i23,z); d1[t]=mfma(a[t][0],t?sl1:sl0,z); d2[t]=mfma(a[t][2],t?sl1:sl0,z); }
  #pragma unroll
  for(int t=0;t<2;t++){ d1[t]=mfma(a[t][1],t?sh1:sh0,d1[t]); d2[t]=mfma(a[t][3],t?sh1:sh0,d2[t]); }
  SB;
  #pragma unroll
  for(int t=0;t<2;t++){
    const v4u o={pk2(d2[t][3]*v2[t][3],d2[t][2]*v2[t][2]),pk2(d2[t][1]*v2[t][1],d2[t][0]*v2[t][0]),pk2(d1[t][3]*v1[t][3],d1[t][2]*v1[t][2]),pk2(d1[t][1]*v1[t][1],d1[t][0]*v1[t][0])};
    if(t) ob=o; else oa=o;
  }
}
DEV void b_put(unsigned a, v4u o){
  st128(a+B_X0,o);
  st32(a+B_X1+4,o.x); st64(a+B_X1+8,(v2u){o.y,o.z}); st32(a+B_X1+16,o.w);
}
DEV v4u b_diag(int di, unsigned e){ return (v4u){di==0?e:0u,di==1?e:0u,di==2?e:0u,di==3?e:0u}; }
constexpr int WS_B=4<<20, WS_BT=1280;
DEV void b_gate(unsigned ga, unsigned gb, v4u q, char* outp){
  const v2u y0=ldtr(ga), y1=ldtr(gb);
  #define G2(Q,Y) pk2(lo2f(Q)*lo2f(Y), hi2f(Q)*hi2f(Y))
  const v4u o={G2(q.x,y0.x),G2(q.y,y0.y),G2(q.z,y1.x),G2(q.w,y1.y)};
  *(GAS v4u*)outp=o;
}

DEV void role_b(const Params& p, int cg STAMPARG){
  const int tid=__builtin_amdgcn_workitem_id_x(), lane=tid&63, wave=__builtin_amdgcn_readfirstlane(tid>>6);
  const int i16=lane&15, g=lane>>4;
  constexpr unsigned BLK=512*1152;
  const int w=wave&3, ql=lane^3;
  const unsigned rq=(w*128+ql)*1152+cg*16;
  unsigned ga,gx; char* outp;
  #define GCONST { \
    ga=(i16>>2)*B_PITCH+B_HA+(2044-1536-64-128*w-16*g-4*(lane&3))*2; gx=ga+4*B_PITCH; outp=p.out+(size_t)(w*128+ql)*2304+1152+cg*16; \
    asm volatile("":"+v"(ga),"+v"(gx),"+v"(outp)); }
  #define GATE(b,Q0,Q1) { b_gate(ga+1024*(3-(b))+128,gx+1024*(3-(b))+128,Q0,outp+(size_t)(b)*512*2304); b_gate(ga+1024*(3-(b)),gx+1024*(3-(b)),Q1,outp+(size_t)((b)*512+64)*2304); }
  const unsigned cb=(2*w+(wave>>2))*B_PITCH;
  unsigned hb,xb,za;
  #define FCONST { hb=cb+((i16&1)?B_HA:B_HB)+(32*(i16>>1)+8*g)*2-448; \
    xb=cb+((i16&1)? B_X0+(2046-2*i16+8*g)*2 : B_X1+(2048-2*i16+8*g)*2)-512*7-448; \
    za=cb+B_HA+(2046-64*g-2*i16)*2-64-512*7; asm volatile("":"+v"(hb),"+v"(xb),"+v"(za)); }
  f4 a0[8];
  #define PADDR(i) (2*w*B_PITCH+lane*16+((i)>>1)*B_PITCH+B_ST+((i)&1)*1024)
  if(wave<4){
    f4 a1[8];
    v4u hr[8];
    BAR;
    {
      const int u=tid&127, lx=imin(cg*(29*BS)+imin(u,29*BS-1),2048*BS-1), rx=lx/BS;
      const char* t1=(wave<2)? p.wqkv+(128*27+cg*(24*BS)+imin(u,24*BS-1))*128 : p.x+(rx*1152+(2+lx-rx*BS)*128);
      if((wave&1)*64<29*BS) asm volatile("s_mov_b32 m0, 0x27e00\n\ts_nop 0\n\tglobal_load_lds_dword %0, off"::"v"(t1):"memory"); }
    __builtin_amdgcn_s_sleep(HSL);
    #pragma unroll
    for(int i=0;i<8;i++) gload(hr[i],p.hk,(cg*8+2*wave)*4096+i*1024+lane*16);
    #pragma unroll
    for(int c=0;c<2;c++){
      if(lane<56) st128(cb+c*B_PITCH+(lane<28? lane*16 : 4576+(lane-28)*16),(v4u){0,0,0,0});
      if(lane<31) st32(cb+c*B_PITCH+(lane<16? B_X0+4096+lane*4 : B_X1+4100+(lane-16)*4),0u);
    }
    FCONST
    GCONST
    const unsigned dw=*(const __attribute__((address_space(4))) unsigned*)(p.dv+cg*16+wave*4);
    #pragma unroll
    for(int j=0;j<8;j++){ a0[j]=(f4){0,0,0,0}; a1[j]=(f4){0,0,0,0}; asm volatile("":"+v"(a0[j]),"+v"(a1[j])); }
    #pragma unroll
    for(int c=0;c<2;c++){
      if(c==0) WAIT4(4,hr[0],hr[1],hr[2],hr[3]); else WAIT4(0,hr[4],hr[5],hr[6],hr[7]);
      if(lane==0) hr[4*c].x=pk2(lo2f(hr[4*c].x)+(c?hi2f(dw):lo2f(dw)),hi2f(hr[4*c].x));
      #pragma unroll
      for(int i=0;i<4;i++){
        const v4u h=hr[4*c+i];
        const unsigned first=i? __builtin_amdgcn_readlane(hr[4*c+i-1].w,63) : 0u;
        const unsigned pv=__builtin_amdgcn_update_dpp(first,h.w,0x138,0xf,0xf,false);
        const v4u s={__builtin_amdgcn_alignbit(h.x,pv,16),__builtin_amdgcn_alignbit(h.y,h.x,16),__builtin_amdgcn_alignbit(h.z,h.y,16),__builtin_amdgcn_alignbit(h.w,h.z,16)};
        st128(cb+c*B_PITCH+B_HA+i*1024+lane*16,h);
        st128(cb+c*B_PITCH+B_HB+i*1024+lane*16,s);
      }
    }
    STAMP(1);
    __builtin_amdgcn_s_setprio(1);
    b_stage<0,2>(a0,a1,hb,xb,za);
    STAMP(3);
    b_stage<2,2>(a0,a1,hb,xb,za);
    STAMP(4);
    #pragma unroll
    for(int i=0;i<4;i++) st128(PADDR(i),__builtin_bit_cast(v4u,a1[4+i]));
    ldsbar();
  } else {
    const int j=cg&7, cp=(cg&~7)+2*w;
    const unsigned rk=(j*64+w*8+(lane>>3))*1152+(cg>>3)*128+(lane&7)*16;
    v4u k0[4],k1[4],v0[4],v1[4],q0[2],q1[2],halo; unsigned sk0,sk1,ep;
    #define LOADKV(b) { gload(k0[b],p.k+(b)*BLK,rk); gload(k1[b],p.k+(b)*BLK+32*1152,rk); gload(v0[b],p.v+(b)*BLK,rk); gload(v1[b],p.v+(b)*BLK+32*1152,rk); }
    #define LOADQ(b) { gloadq(q0[b],p.q+(b)*BLK,rq); gloadq(q1[b],p.q+(b)*BLK+64*1152,rq); }
    unsigned junk=0;
    #define TOUCH(P,O) asm volatile("global_load_dword %0, %1, %2":"+v"(junk):"v"(O),"s"(P):"memory")
    asm volatile("global_load_ushort %0, %2, %3\n\tglobal_load_ushort %1, %2, %3 offset:64":"=&v"(sk0),"=&v"(sk1):"v"(cp*64+(lane&7)*8+g*2),"s"(p.sk):"memory");
    GAS unsigned* epoch=(GAS unsigned*)(p.ws+(32+(cg>>3))*256);
    asm volatile("global_load_dword %0, %1, %2 sc1":"=v"(ep):"v"(0),"s"(epoch):"memory");
    LOADKV(0)
    const int ht=512*((lane>>3)&3)+64*j-4+w;
    gload(halo,p.k,imax(ht,0)*1152+(cg>>3)*128+(lane&7)*16);
    const unsigned po=(cp*32+j)*WS_BT, co=(cg*32+2*w)*WS_BT;
    st32(B_SCR+4*w,0u);
    ldsbar();
    TOUCH(p.ws+WS_B,co+imin(lane,19)*128);
#define WN(a,b) b
    NSTAMP(10);
    GCONST FCONST
    #define KO(sl) ((sl)?B_ST+64:B_X1)
    #define VO(sl) ((sl)?B_ST+1088:B_X0)
    unsigned sw=(lane&7)*B_PITCH+(w*8+(lane>>3))*16, sh=(lane&7)*B_PITCH+w*16;
    unsigned ta=2*w*B_PITCH+(4+8*(i16>>2)+(lane&3)-g)*16, tv=2*w*B_PITCH+(32*(g&1)+8*(i16>>2)+4*(g>>1)+(lane&3))*16;
    unsigned xw=(lane&7)*B_PITCH+(2040-1536-64-128*w-32*(i16>>3)-8*g)*2;
    const int di=(lane&7)>>1;
    const unsigned one=0x3f80u<<(16*(lane&1));
    v4u i01=b_diag(di,(g==(i16>>3))?one:0u), i23=b_diag(di,(g==(i16>>3)+2)?one:0u);
    asm volatile("":"+v"(sw),"+v"(sh),"+v"(ta),"+v"(tv),"+v"(xw),"+v"(i01),"+v"(i23));
    const __amdgpu_buffer_rsrc_t xr=__builtin_amdgcn_make_buffer_rsrc(p.ws+WS_B,0,72*32*WS_BT,0x00020000);
    WAIT3(6,sk0,sk1,ep);
    unsigned tag=ep*2891336453u+1013904223u;
    sk0<<=16*(lane&1); sk1<<=16*(lane&1);
    v4u sl0=b_diag(di,(i16<8)?sk0:0u), sh0=b_diag(di,(i16<8)?0u:sk0), sl1=b_diag(di,(i16<8)?sk1:0u), sh1=b_diag(di,(i16<8)?0u:sk1);
    asm volatile("":"+v"(tag),"+v"(sl0),"+v"(sh0),"+v"(sl1),"+v"(sh1));
    WAIT1(1,halo);
    NSTAMP(11);
    if(ht<0) halo=(v4u){0,0,0,0};
    #define STAGE(b,sl,n) { WAIT4(n,k0[b],k1[b],v0[b],v1[b]); \
        st128(sw+KO(sl),k0[b]); st128(sw+KO(sl)+512,k1[b]); st128(sw+VO(sl),v0[b]); st128(sw+VO(sl)+512,v1[b]); \
        if((lane>>3)==(b)) st128(sh+KO(sl)-64,halo); }
    #define SYNC(n) { WAITL; *(volatile LAS unsigned*)(B_SCR+4*w)=(n); flag_wait(B_SCR+4*(lane&3),n); }
    #define PROD(b,sl) { \
        v4u oa,ob; b_prep2(ta+KO(sl)-64,tv+VO(sl),sl0,sh0,sl1,sh1,i01,i23,oa,ob); \
        __builtin_amdgcn_raw_buffer_store_b128(oa,xr,lane*16+po+(b)*8*WS_BT,0,16); __builtin_amdgcn_raw_buffer_store_b32(sum5(tag,oa),xr,1024+lane*4,po+(b)*8*WS_BT,16); \
        __builtin_amdgcn_raw_buffer_store_b128(ob,xr,lane*16+po+(32+(b)*8)*WS_BT,0,16); __builtin_amdgcn_raw_buffer_store_b32(sum5(tag,ob),xr,1024+lane*4,po+(32+(b)*8)*WS_BT,16); }
    #define RECV(b) { v4u oa,ob; _Pragma("unroll 1") for(int spin=0;spin<(1<<20);spin++){ asm volatile("":::"memory"); \
          oa=__builtin_amdgcn_raw_buffer_load_b128(xr,lane*16,co+(b)*8*WS_BT,16); ob=__builtin_amdgcn_raw_buffer_load_b128(xr,lane*16+WS_BT,co+(b)*8*WS_BT,16); \
          const unsigned ca=__builtin_amdgcn_raw_buffer_load_b32(xr,1024+lane*4,co+(b)*8*WS_BT,16), cb=__builtin_amdgcn_raw_buffer_load_b32(xr,1024+lane*4+WS_BT,co+(b)*8*WS_BT,16); \
          if(__builtin_amdgcn_ballot_w64(sum5(tag,oa)!=ca || sum5(tag,ob)!=cb)==0) break; __builtin_amdgcn_s_sleep(4); } \
        b_put(xw+1024*(3-(b))+128,oa); b_put(xw+1024*(3-(b)),ob); }
    __builtin_amdgcn_s_setprio(2);
    STAGE(0,1,5)
    SYNC(1)
    PROD(0,1)
    NSTAMP(12);
    __builtin_amdgcn_s_sleep(BSLP);
    RECV(0)
    NSTAMP(1);
    ldsbar();
    STAMP(2);
    LOADKV(1)
    LOADKV(2) LOADKV(3)
    BSTAMP(10);
    STAGE(1,0,8)
    SYNC(2)
    BSTAMP(11);
    PROD(1,0)
    BSTAMP(12);
    SYNC(3)
    STAGE(2,1,8)
    STAGE(3,0,4)
    SYNC(4)
    PROD(2,1)
    STAMP(13);
    RECV(1)
    TOUCH(p.q+w*BLK,(lane*8+j)*1152+cg*16);
    STAMP(8);
    ldsbar();
    STAMP(3);
    PROD(3,0)
    RECV(2)
    asm volatile(""::"v"(junk));
    LOADQ(0) LOADQ(1)
    SYNC(5)
    RECV(3)
    WAIT2(2,q0[0],q1[0]);
    GATE(0,q0[0],q1[0])
    STAMP(9);
    ldsbar();
    STAMP(4);
    if(tid==256 && j==0) __hip_atomic_store(epoch,tag,__ATOMIC_RELAXED,__HIP_MEMORY_SCOPE_AGENT);
    #pragma unroll
    for(int i=0;i<4;i++){ a0[i]=(f4){0,0,0,0}; a0[4+i]=__builtin_bit_cast(f4,ld128(PADDR(i))); }
    WAIT2(2,q0[1],q1[1]);
    GATE(1,q0[1],q1[1])
    __builtin_amdgcn_s_setprio(1);
  }
  {
    PF<4,4> f;
    #pragma unroll
    for(int i=0;i<12;i++) p_rd<4,4,0>(i,f,hb,xb);
    v4u qa,qb; const unsigned rr=rq+(wave>>2)*64*1152;
    gloadq(qa,p.q+2*BLK,rr); gloadq(qb,p.q+3*BLK,rr);
    b_seg<4,4>(a0,f,hb,xb,0);
    b_zout<4,4>(a0,za);
    p_wait<0>(f);
    STAMP(5);
    WAIT2(0,qa,qb);
    ldsbar();
    STAMP(6);
    const unsigned go=(wave<4)?128:0; char* op=outp+(size_t)(wave>>2)*64*2304;
    b_gate(ga+go+1024,gx+go+1024,qa,op+(size_t)1024*2304);
    b_gate(ga+go,gx+go,qb,op+(size_t)1536*2304);
    STAMP(7);
  }
}

constexpr int A_TM=96, A_SLOT=41984, A_WOFF=14336, A_IMG=83968, A_IROW=464, A_ZERO=136192, A_TAG=A_ZERO+288, A_CST=136704;
constexpr int WS_O=65536, WS_OM=19*1024, WS_OT=6*WS_OM;
typedef __amdgpu_buffer_rsrc_t rsrc_t;
DEV void dma16o(const v4u& srd, unsigned voff, unsigned soff, unsigned lds){
  asm volatile("s_mov_b32 m0, %3\n\ts_nop 0\n\tbuffer_load_dwordx4 %0, %1, %2 offen" NTO " lds"::"v"(voff),"s"(srd),"s"(soff),"s"(lds):"memory");
}
DEV void dma16(const v4u& srd, unsigned voff, unsigned soff, unsigned lds){
  asm volatile("s_mov_b32 m0, %3\n\ts_nop 0\n\tbuffer_load_dwordx4 %0, %1, %2 offen" NTX " lds"::"v"(voff),"s"(srd),"s"(soff),"s"(lds):"memory");
}
#define DW1 "s_add_u32 m0, m0, 3456\n\ts_add_u32 %0, %0, 27648\n\tbuffer_load_dwordx4 %1, %2, %0 offen lds\n\t"
DEV void dma_w4(const v4u& srd, unsigned voff, unsigned soff, unsigned lds){
  asm volatile("s_lshr_b64 exec, exec, 10\n\ts_mov_b32 m0, %3\n\ts_nop 0\n\tbuffer_load_dwordx4 %1, %2, %0 offen lds\n\t"
               DW1 DW1 DW1 "s_mov_b64 exec, -1"
               :"+s"(soff):"v"(voff),"s"(srd),"s"(lds):"memory","scc");
}
DEV v4u mksrd(const char* p, unsigned bytes){
  const u64 a=(u64)p; return (v4u){(unsigned)a,(unsigned)(a>>32)&0xffffu,bytes,0x00020000u};
}
struct AFr { v4u x[7]; v2u w[4][2]; };
struct AOp { v4u x[7], w[4]; };
template<int WO> DEV void a_rd(int i, AFr& f, unsigned xe, unsigned x0, unsigned xo, unsigned wa, unsigned we){
  switch(i){
    case 0: ATR(f.w[3][0],we,WO); break;      case 1: ATR(f.w[3][1],we,WO+432); break;
    case 2: ARD(f.x[0],xe,0); break;          case 3: ARD(f.x[1],xe,2048); break;   case 4: ARD(f.x[2],xe,4096); break;
    case 5: ATR(f.w[0][0],wa,WO); break;      case 6: ATR(f.w[0][1],wa,WO+432); break;
    case 7: ATR(f.w[1][0],wa,WO+32); break;   case 8: ATR(f.w[1][1],wa,WO+464); break;
    case 9: ATR(f.w[2][0],wa,WO+64); break;   case 10: ATR(f.w[2][1],wa,WO+496); break;
    case 11: ARD(f.x[3],x0,0); break;
    case 12: ARD(f.x[4],xo,0); break;         case 13: ARD(f.x[5],xo,2048); break;  case 14: ARD(f.x[6],xo,4096); break;
  }
}
DEV void a_wait(AFr& f, AOp& o){
  asm volatile("s_waitcnt lgkmcnt(0)":"+v"(f.x[0]),"+v"(f.x[1]),"+v"(f.x[2]),"+v"(f.x[3]),"+v"(f.x[4]),"+v"(f.x[5]),"+v"(f.x[6]),
     "+v"(f.w[0][0]),"+v"(f.w[0][1]),"+v"(f.w[1][0]),"+v"(f.w[1][1]),"+v"(f.w[2][0]),"+v"(f.w[2][1]),"+v"(f.w[3][0]),"+v"(f.w[3][1])::"memory");
  #pragma unroll
  for(int m=0;m<7;m++) o.x[m]=f.x[m];
  #pragma unroll
  for(int c=0;c<4;c++) o.w[c]=cat(f.w[c][0],f.w[c][1]);
}
DEV void a_mm(int i, f4 (&acc)[3][7], f4 (&ace)[3], const AOp& f){
  if(i<3) AMM(ace[i],f.w[3],f.x[i]); else AMM(acc[(i-3)%3][(i-3)/3],f.w[(i-3)%3],f.x[(i-3)/3]);
}
DEV float max3(float a,float b,float c){ float d; asm("v_max3_f32 %0, %1, %2, %3":"=v"(d):"v"(a),"v"(b),"v"(c)); return d; }
DEV float max2(float a,float b){ float d; asm("v_max_f32 %0, %1, %2":"=v"(d):"v"(a),"v"(b)); return d; }
DEV void tr4(unsigned (&a)[4]){
  auto r0=__builtin_amdgcn_permlane16_swap(a[0],a[1],false,false);
  auto r1=__builtin_amdgcn_permlane16_swap(a[2],a[3],false,false);
  auto s0=__builtin_amdgcn_permlane32_swap(r0[0],r1[0],false,false);
  auto s1=__builtin_amdgcn_permlane32_swap(r0[1],r1[1],false,false);
  a[0]=s0[0]; a[1]=s1[0]; a[2]=s0[1]; a[3]=s1[1];
}

DEV unsigned rs4(unsigned a, unsigned b, unsigned c, unsigned d){
  auto x=__builtin_amdgcn_permlane16_swap(a,b,false,false);
  auto y=__builtin_amdgcn_permlane16_swap(c,d,false,false);
  auto z=__builtin_amdgcn_permlane32_swap(x[0]+x[1],y[0]+y[1],false,false);
  return z[0]+z[1];
}
DEV void role_a(const Params& p, int rt, int grp, int rot STAMPARG){
  const int tid=__builtin_amdgcn_workitem_id_x(), lane=tid&63, wave=__builtin_amdgcn_readfirstlane(tid>>6);
  const int row0=rt*A_TM, nrow=imin(A_TM,2048-row0);
  const int i16=lane&15, g=lane>>4;
  GAS unsigned* epoch=(GAS unsigned*)(p.ws+rt*256);
  unsigned ka,kb,qb,va,sel0,sel1,sel2,sel3; v4u vl=(v4u){0,0,0,0}; unsigned vl4=0;
  const int par=i16&1; const bool cur0=(4*g+par<=i16), cur1=(4*g+par+2<=i16);
  const rsrc_t wsr=__builtin_amdgcn_make_buffer_rsrc(p.ws+WS_O,0,22*WS_OT,0x00020000);
  if(wave>=4){
    const int lw=wave-4;
    const v4u sx=mksrd(p.x,2048*1152), sw=mksrd(p.wqkv,576*3456), so=mksrd(p.wo,576*1152);
    int st=rot, cnt=0;
    unsigned sl=lw*1024;
    const int l27=lane>=27? lane-27:lane, seg=(l27>=18)?2:(l27>=9?1:0);
    const unsigned vw=(lane>=27?4*3456:0)+seg*(1152-144)+l27*16+grp*144+lw*3456;
    dma_w4(sw,vw,st*64*3456,sl+A_WOFF-lw*160);
    __builtin_amdgcn_s_setprio(3);
    unsigned vx[4];
    #pragma unroll
    for(int i=0;i<4;i++){
      const int ir=8*(lw+4*i)+(lane>>3); int tok=row0-16+ir;
      tok=imax(tok,imax(row0-14,0)); tok=imin(tok,row0+nrow-1);
      vx[i]=tok*1152+(((lane&7)^((ir>>1)&7))<<4);
    }
    #define A_XA() do{ dma16(sx,vx[0],st*128,sl); dma16(sx,vx[1],st*128,sl+4096); }while(0)
    #define A_HA() do{ dma_w4(sw,vw,st*64*3456,sl+A_WOFF-lw*160); A_XA(); }while(0)
    #define A_HB() do{ dma16(sx,vx[2],st*128,sl+8192); if(lw<2) dma16(sx,vx[3],st*128,sl+12288); dma_w4(sw,vw,(st*64+32)*3456,sl+A_WOFF-lw*160+13824); \
      cnt++; st=(cnt==8)?8:(cnt^rot); sl=(sl>=2*A_SLOT)?sl-2*A_SLOT:sl+A_SLOT; }while(0)
    A_XA(); A_HB();
    A_HA();
    WAITV(6);
    BAR;
    #define LSTAMP(n)
    #define LUN _Pragma("unroll 1")
    LUN
    for(int n=1;n<=NH;n++){ A_HB(); A_HA(); WAITV(6); BAR; LSTAMP(n) }
    A_HB();
    LUN
    for(int n=NH+1;n<8;n++){ A_HA(); A_HB(); if(lw<2) WAITV(12); else WAITV(11); BAR; LSTAMP(n) }
    const v4u c0=ld128(A_CST+tid*16-4096), c1=ld128(A_CST+tid*16), c2=ld128(A_CST+tid*16+4096);
    vl=ld128(A_CST+tid*16+8192);
    ka=c0.x; kb=c0.y; qb=c0.z; va=c0.w; sel0=c1.x; sel1=c1.y; sel2=c1.z; sel3=c1.w; vl4=c2.w;
    #define A_ISSUE_WO(P0,P1) do{ _Pragma("unroll 1") for(int P=(P0);P<(P1);P++){ \
        dma16o(so,c2.x,P*72*1152,P*9216+lw*1024); dma16o(so,c2.y,P*72*1152,P*9216+lw*1024+4096); \
        if((P&3)==lw) dma16o(so,c2.z,P*72*1152,P*9216+8192); } }while(0)
    A_ISSUE_WO(0,2);
    if(lw<2) WAITV(5); else WAITV(4);
    BAR;
    STAMP(13);
    A_ISSUE_WO(2,5);
    BAR;
    A_ISSUE_WO(5,8);
    __builtin_amdgcn_s_setprio(0);
    STAMP(2);
  } else {
    unsigned ep;
    asm volatile("global_load_dword %0, %1, %2 sc1":"=v"(ep):"v"(0),"s"(epoch):"memory");
    { const unsigned plo=par?0x01000c0cu:0x0c0c0100u, phi=par?0x03020c0cu:0x0c0c0302u, pzz=0x0c0c0c0cu;
      sel0=cur0?pzz:plo; sel1=cur1?pzz:phi; sel2=cur0?plo:pzz; sel3=cur1?phi:pzz;
      ka=A_IMG+(16*wave+i16)*A_IROW+8*g; kb=(g==0)?ka+32:A_ZERO-144; qb=(g==0)?ka+32+16*A_IROW:A_ZERO;
      va=A_IMG+(16*wave+4*g+(i16>>2))*A_IROW+288+(lane&3)*8;
      const unsigned d=64*A_IROW, dz=(g==0)?d:0u;
      st128(A_CST+tid*16,(v4u){ka+d,kb+dz,qb+dz,va+d}); st128(A_CST+tid*16+4096,(v4u){sel0,sel1,sel2,sel3});
      unsigned vo[3], vv[5];
      #pragma unroll
      for(int i=0;i<3;i++){
        const int ci=(i<2? wave+4*i : 8)*64+lane, rho=ci/9, cc=ci-rho*9;
        vo[i]=(32*(rho>>5)+16*(rho&1)+4*((rho>>3)&3)+((rho>>1)&3))*1152+grp*144+cc*16;
      }
      #pragma unroll
      for(int i=0;i<5;i++){
        const int ci=imin((wave&1)+2*i,8)*64+lane, rho=ci/9, cc=ci-rho*9;
        vv[i]=(72*(rho>>3)+64+4*(rho&1)+((rho>>1)&3))*1152+grp*144+cc*16;
      }
      st128(A_CST+tid*16+8192,(v4u){vo[0],vo[1],vo[2],vv[4]}); st128(A_CST+tid*16+12288,(v4u){vv[0],vv[1],vv[2],vv[3]});
      asm volatile("":"+v"(ka),"+v"(kb),"+v"(qb),"+v"(va),"+v"(sel0),"+v"(sel1),"+v"(sel2),"+v"(sel3)); }
    f4 acc[3][7], ace[3];
    #pragma unroll
    for(int c=0;c<3;c++){ ace[c]=(f4){0,0,0,0};
      #pragma unroll
      for(int m=0;m<7;m++) acc[c][m]=(f4){0,0,0,0}; }
    const int e0=(wave&1)?4:1, o0=(wave&1)?1:4;
    const unsigned xl=i16*128+((g^(i16>>1))<<4);
    const unsigned wl=A_WOFF+(8*g+2*(i16>>2))*432+(lane&3)*8;
    unsigned ae0=xl+e0*2048, az0=xl, ao0=xl+o0*2048, ae1=ae0^64, az1=az0^64, ao1=ao0^64, wa=wl+(2+3*wave)*32, we=wl+(wave>>1)*32;
    const unsigned ia=A_IMG+i16*A_IROW+g*8;
    unsigned ie=ia+e0*16*A_IROW+(2+3*wave)*32, iz=ia+(2+3*wave)*32, io=ia+o0*16*A_IROW+(2+3*wave)*32, ix=ia+e0*16*A_IROW+(wave>>1)*32;
    asm volatile("":"+v"(ae0),"+v"(az0),"+v"(ao0),"+v"(ae1),"+v"(az1),"+v"(ao1),"+v"(wa),"+v"(we),"+v"(ie),"+v"(iz),"+v"(io),"+v"(ix));
    #pragma unroll
    for(int c=0;c<3;c++){ asm volatile("":"+v"(ace[c]));
      #pragma unroll
      for(int m=0;m<7;m++) asm volatile("":"+v"(acc[c][m])); }
    if(tid<72) st32(A_ZERO+tid*4,0u);
    WAIT1(0,ep);
    if(tid==0) st32(A_TAG,ep*2891336453u+1013904223u);
    int slot=0;
    AFr f0={},f1={}; AOp g0,g1;
    ldsbar();
    STAMP(1);
    #pragma unroll
    for(int i=0;i<15;i++) a_rd<0>(i,f0,ae0,az0,ao0,wa,we);
    a_wait(f0,g0);
    #pragma unroll 1
    for(int s=0;s<8;s++){
      const int dl=(slot==2)?-2*A_SLOT:A_SLOT; slot=(slot==2)?0:slot+1;
      #pragma unroll
      for(int i=0;i<24;i++){
        a_mm(i,acc,ace,g0);
        if(i<15) a_rd<13824>(i,f1,ae1,az1,ao1,wa,we);
        if(i==15) AADD(ae0,dl); if(i==16) AADD(az0,dl); if(i==17) AADD(ao0,dl); if(i==18) AADD(wa,dl); if(i==19) AADD(we,dl);
      }
      a_wait(f1,g1);
      BAR;

      #pragma unroll
      for(int i=0;i<24;i++){
        a_mm(i,acc,ace,g1);
        if(i<15) a_rd<0>(i,f0,ae0,az0,ao0,wa,we);
        if(i==15) AADD(ae1,dl); if(i==16) AADD(az1,dl); if(i==17) AADD(ao1,dl);
      }
      a_wait(f0,g0);
    }
    #pragma unroll
    for(int i=0;i<24;i++){
      a_mm(i,acc,ace,g0);
      if(i<15) a_rd<13824>(i,f1,ae1,az1,ao1,wa,we);
    }
    a_wait(f1,g1);
    BAR;
    SB;
    #pragma unroll
    for(int m=0;m<3;m++) ace[m]=mfma(g1.w[3],g1.x[m],ace[m]);
    #pragma unroll
    for(int m=0;m<7;m++){
      #pragma unroll
      for(int c=0;c<3;c++) acc[c][m]=mfma(g1.w[c],g1.x[m],acc[c][m]);
    }
    #pragma unroll
    for(int m=0;m<3;m++) st64(ix+m*16*A_IROW,(v2u){pk2(ace[m][0],ace[m][1]),pk2(ace[m][2],ace[m][3])});
    #pragma unroll
    for(int m=0;m<7;m++){
      const unsigned ib=(m<3)?ie+m*16*A_IROW:(m==3?iz:io+(m-4)*16*A_IROW);
      #pragma unroll
      for(int c=0;c<3;c++) st64v(ib+c*32,(v2u){pk2(acc[c][m][0],acc[c][m][1]),pk2(acc[c][m][2],acc[c][m][3])});
    }
    SGB(0x8,3);
    #pragma unroll
    for(int i=0;i<21;i++){ SGB(0x8,1); SGB(0x2,2); SGB(0x200,1); }
    SB;
    STAMP(2);
  }
  ldsbar();
  STAMP(3);
  const unsigned tag=ld32(A_TAG);
  if(wave>=6){
    const v4u so=mksrd(p.wo,576*1152);
    #pragma unroll
    for(int i=0;i<5;i++) if(i<4 || wave==6) dma16o(so,i<4?vl[i]:vl4,0,8*9216+(wave&1)*1024+i*2048);
    WAITV(0);
  } else {
    v4u qf[4],kp[4],kc[4],vf[5];
    #pragma unroll
    for(int h=0;h<4;h++){
      const int hb=36*h-4*(h&1);
      qf[h]=cat(ld64v(ka+16*A_IROW+hb),ld64v(qb+hb));
      kp[h]=cat(ld64v(ka+144+hb),ld64v(kb+144+hb));
      kc[h]=cat(ld64v(ka+16*A_IROW+144+hb),ld64v(qb+144+hb));
    }
    #pragma unroll
    for(int c=0;c<5;c++) vf[c]=cat(ldtr(va+32*c),ldtr(va+16*A_IROW+32*c));
    SB;
    f4 sp[4],sc[4];
    #pragma unroll
    for(int h=0;h<4;h++){
      v4u q=qf[h];
      if(h&1) q.x=(g==0)?0u:q.x; else q.w=0u;
      sp[h]=mfma(kp[h],q,(f4){0,0,0,0}); sc[h]=mfma(kc[h],q,(f4){0,0,0,0});
    }
    SB;
    unsigned a0[4],a1[4];
    #pragma unroll
    for(int h=0;h<4;h++){
      const float c0=sc[h][0],c1=sc[h][1],c2=sc[h][2],c3=sc[h][3],p0=sp[h][0],p1=sp[h][1],p2=sp[h][2],p3=sp[h][3];
      const float ca=par?c1:c0, pa=par?p1:p0, cb=par?c3:c2, pb=par?p3:p2;
      a0[h]=__builtin_bit_cast(unsigned,cur0?ca:pa);
      a1[h]=__builtin_bit_cast(unsigned,cur1?cb:pb);
    }
    if(row0+16*wave==0){
      const unsigned mk=__builtin_bit_cast(unsigned,-1e9f/0.23570226039551584f);
      #pragma unroll
      for(int h=0;h<4;h++){ a0[h]=cur0?a0[h]:mk; a1[h]=cur1?a1[h]:mk; }
    }
    tr4(a0); tr4(a1);
    float l[8];
    #pragma unroll
    for(int i=0;i<4;i++){ l[2*i]=__builtin_bit_cast(float,a0[i]); l[2*i+1]=__builtin_bit_cast(float,a1[i]); }
    constexpr float SC=0.23570226039551584f*1.4426950408889634f;
    const float mx=-SC*max3(max3(l[0],l[1],l[2]),max3(l[3],l[4],l[5]),max2(l[6],l[7]));
    #pragma unroll
    for(int i=0;i<8;i++) l[i]=__builtin_amdgcn_exp2f(__builtin_fmaf(l[i],SC,mx));
    const float inv=__builtin_amdgcn_rcpf(((l[0]+l[1])+(l[2]+l[3]))+((l[4]+l[5])+(l[6]+l[7])));
    unsigned pp[4];
    #pragma unroll
    for(int i=0;i<4;i++) pp[i]=pk2(l[2*i]*inv,l[2*i+1]*inv);
    tr4(pp);
    v4u pf[4];
    #pragma unroll
    for(int h=0;h<4;h++) pf[h]=(v4u){__builtin_amdgcn_perm(0u,pp[h],sel0),__builtin_amdgcn_perm(0u,pp[h],sel1),__builtin_amdgcn_perm(0u,pp[h],sel2),__builtin_amdgcn_perm(0u,pp[h],sel3)};
    const f4 z={0,0,0,0};
    const f4 o0=mfma(vf[0],pf[0],z), a1_=mfma(vf[1],pf[0],z), b1=mfma(vf[1],pf[1],z), a2=mfma(vf[2],pf[1],z),
             b2_=mfma(vf[2],pf[2],z), a3=mfma(vf[3],pf[2],z), b3=mfma(vf[3],pf[3],z), o4=mfma(vf[4],pf[3],z);
    const v4u t01={pk2(o0[0],o0[1]),pk2(o0[2],o0[3]), (g==0)?pk2(a1_[0],a1_[1]):pk2(b1[0],b1[1]), pk2(b1[2],b1[3])};
    const v4u t23={(g==0)?pk2(a2[0],a2[1]):pk2(b2_[0],b2_[1]), (g==0)?pk2(a2[2],a2[3]):pk2(b2_[2],b2_[3]),
                   (g<2)?pk2(a3[0],a3[1]):pk2(b3[0],b3[1]), (g==0)?pk2(a3[2],a3[3]):pk2(b3[2],b3[3])};
    const unsigned u0=pk2(o4[0],o4[1]), u1=pk2(o4[2],o4[3]);
    auto r0=__builtin_amdgcn_permlane16_swap(u0,u0,false,false);
    auto r1=__builtin_amdgcn_permlane16_swap(u1,u1,false,false);
    const unsigned ob=rt*WS_OT+wave*WS_OM;
    __builtin_amdgcn_raw_buffer_store_b128(t01,wsr,ob+2*grp*1024+lane*16,0,16);
    __builtin_amdgcn_raw_buffer_store_b128(t23,wsr,ob+2*grp*1024+1024+lane*16,0,16);
    const v4u t4={(unsigned)r0[0],(unsigned)r1[0],(unsigned)r0[1],(unsigned)r1[1]};
    unsigned hs=sum5(sum5(0,t01),t23);
    { auto x=__builtin_amdgcn_permlane16_swap(hs,hs,false,false); hs=x[0]+x[1];
      auto y=__builtin_amdgcn_permlane32_swap(hs,hs,false,false); hs=y[0]+y[1]; }
    if(g==0){
      __builtin_amdgcn_raw_buffer_store_b128(t4,wsr,ob+16*1024+grp*256+lane*16,0,16);
      __builtin_amdgcn_raw_buffer_store_b64((v2u){sum5(hs,t4)+tag,tag},wsr,ob+18*1024+grp*128+lane*8,0,16);
    }
    WAITV(4);
  }
  STAMP(4);
  BAR;
  STAMP(5);
  {
    const int hf=wave&1, mo=(wave<4)?wave:4+((wave-4)>>1), m0=(wave<4)?(wave^1):mo;
    const unsigned wa=(8*g+2*(i16>>2))*144+(lane&3)*8, wb=wa+hf*8*4608, wc=wa+(16+hf)*4608;
    const v4u so=mksrd(p.ws+WS_O,22*WS_OT);
    const unsigned vo=lane*16;
    const int s0=rt*WS_OT+m0*WS_OM+hf*8192, s1=rt*WS_OT+mo*WS_OM+hf*8192, dl=16384-hf*7168, dr=18432-hf*7680;
    #define WFR(a,ks,c) cat(ldtr((a)+(ks)*4608+(c)*32),ldtr((a)+(ks)*4608+(c)*32+144))
    #define OLD(d,S,k) asm volatile("buffer_load_dwordx4 %0, %1, %2, %3 offen offset:%4 sc1":"=v"(d):"v"(vo),"s"(so),"s"((S)+((k)==8?dl:((k)>>2)*4096)),"i"(((k)&3)*1024):"memory")
    #define RLD(d,S) asm volatile("buffer_load_dwordx2 %0, %1, %2, %3 offen sc1":"=v"(d):"v"(vo>>1),"s"(so),"s"((S)+dr):"memory")
    v4u wf[9][5], of[9]; v2u r0,r1; f4 acc[5];
    #define ATT { RLD(r0,s0); RLD(r1,s1); _Pragma("unroll") for(int k_=0;k_<PNF;k_++) OLD(of[k_],s0,k_); }
    #pragma unroll
    for(int k=0;k<9;k++){
      #pragma unroll
      for(int c=0;c<5;c++) wf[k][c]=(k<8)?WFR(wb,k,c):WFR(wc,0,c);
      if(k==PNISS){ SB; ATT SB; }
    }
    WAITL;
    #pragma unroll
    for(int k=0;k<9;k++){
      #pragma unroll
      for(int c=0;c<5;c++) asm volatile("":"+v"(wf[k][c]));
    }
    STAMP(12);
    const unsigned red=A_IMG+lane*16, rf=A_ZERO+260;
    if(wave>=4) __builtin_amdgcn_s_setprio(1);
    #define UNIT(NEXT,R,OK) { unsigned hs[5]; \
      _Pragma("unroll") for(int k=0;k<9;k++){ \
        asm volatile("s_waitcnt vmcnt(%1)":"+v"(of[k]):"i"((NEXT)?8:8-k):"memory"); \
        _Pragma("unroll") for(int c=0;c<5;c++){ \
          if(k==0) asm volatile("v_mfma_f32_16x16x32_bf16 %0, %1, %2, 0":"=&v"(acc[c]):"v"(wf[k][c]),"v"(of[k])); else AMM(acc[c],wf[k][c],of[k]); \
          if(c==0){ if(k&1) asm volatile("v_add3_u32 %0, %0, %1, %2":"+v"(hs[k>>1]):"v"(of[k].x),"v"(of[k].y)); \
                    else asm volatile("v_add_u32 %0, %1, %2":"=v"(hs[k>>1]):"v"(of[k].x),"v"(of[k].y)); } \
          if(c==1) asm volatile("v_add3_u32 %0, %0, %1, %2":"+v"(hs[k>>1]):"v"(of[k].z),"v"(of[k].w)); } \
        if(NEXT) OLD(of[k],s1,k); } \
      asm volatile("s_nop 7\n\ts_nop 3":::"memory"); SB; \
      OK=__builtin_amdgcn_ballot_w64(rs4(hs[0],hs[1],hs[2],hs[3])+hs[4]+tag!=R.x)==0; }
    #define SEND(m) { _Pragma("unroll") for(int c=0;c<5;c++) st128(red+(m)*5120+c*1024,__builtin_bit_cast(v4u,acc[c])); asm volatile("":::"memory"); *(volatile LAS unsigned*)(rf+4*(m))=1u; }
    #define PWAIT(m) { _Pragma("unroll 1") for(int spin=0;spin<(1<<24);spin++) if(__builtin_amdgcn_readfirstlane(*(volatile LAS unsigned*)(rf+4*(m)))) break; asm volatile("":::"memory"); }
    #pragma unroll 1
    for(int tries=0;tries<64;tries++){
      #pragma unroll 1
      for(int spin=0;spin<(1<<20);spin++){
        asm volatile("s_waitcnt vmcnt(%2)":"+v"(r0),"+v"(r1):"i"(PNF):"memory");
        if(__builtin_amdgcn_ballot_w64(r0.y!=tag || r1.y!=tag)==0) break;
        WAITV(0);
        ATT
      }
      #pragma unroll
      for(int k=PNF;k<9;k++) OLD(of[k],s0,k);
      STAMP(6);
      bool ok0=true, ok1;
      if(wave<4){
        UNIT(1,r0,ok0)
        STAMP(10);
        if(ok0) SEND(m0)
      }
      STAMP(11);
      UNIT(0,r1,ok1)
      if(ok0 && ok1) break;
      ATT
    }
    if(wave>=4 && hf) SEND(mo)
    else { PWAIT(mo)
      #pragma unroll
      for(int c=0;c<5;c++) acc[c]+=__builtin_bit_cast(f4,ld128(red+mo*5120+c*1024)); }
    STAMP(7);
    if(wave<4 || !hf){
      const unsigned tb=A_IMG+30720+mo*2560;
      #pragma unroll
      for(int c=0;c<5;c++) st64(tb+i16*160+g*8+c*32,(v2u){pk2(acc[c][0],acc[c][1]),pk2(acc[c][2],acc[c][3])});
      const int l9=(lane*57)>>9, c9=lane-l9*9;
      const rsrc_t orr=__builtin_amdgcn_make_buffer_rsrc(p.out+(size_t)(row0+mo*16)*2304+grp*144,0,__builtin_amdgcn_readfirstlane(imax(imin(nrow-mo*16,16),0)*2304),0x00020000);
      const unsigned oo=(lane<63)? l9*2304+c9*16 : 0x40000000u;
      #pragma unroll
      for(int i=0;i<3;i++) __builtin_amdgcn_raw_buffer_store_b128(ld128(tb+(7*i+l9)*160+c9*16),orr,oo+i*7*2304,0,0);
    }
    asm volatile(".Lcpf1:");
    if(tid==0 && grp==0) __hip_atomic_store(epoch,tag,__ATOMIC_RELAXED,__HIP_MEMORY_SCOPE_AGENT);
  }
  STAMP(8);
}

NOPK __global__ __launch_bounds__(512,2) void fused(Params p){
  STAMPDECL;
  STAMP(0);
  const int b=__builtin_amdgcn_workgroup_id_x(), xcd=b&7, j=b>>3, pr=xcd>>1, half=xcd&1;
  const int nA=(pr==0||pr==3)?20:24;
  {
    u64 pc; unsigned len;
    asm volatile("s_getpc_b64 %0\n.Lcpf0:\n\ts_mov_b32 %1, .Lcpf1-.Lcpf0":"=s"(pc),"=s"(len));
    const unsigned t=__builtin_amdgcn_workitem_id_x();
    if(t<40){ unsigned o=((j&7)+t*8)*128; o=o<len?o:len;
      asm volatile("s_mov_b32 m0, 0x27f00\n\ts_nop 0\n\tglobal_load_lds_dword %0, %1"::"v"(o&~3u),"s"(pc):"memory"); }
  }
  const int ja=(pr==0)? j-4 : j;
  if(ja<0){
    const int t=(half*4+j)*512+__builtin_amdgcn_workitem_id_x();
    #pragma unroll 1
    for(int st=2+BS;st<9;st++){
      const char* a=(t<2048)? p.x+(t*1152+st*128) : p.wqkv+(st*1728+imin(t-2048,1727))*128;
      if(t<3776) asm volatile("s_mov_b32 m0, 0x27e00\n\ts_nop 0\n\tglobal_load_lds_dword %0, off"::"v"(a):"memory");
    }
    asm volatile("s_mov_b32 m0, 0x27e00\n\ts_nop 0\n\tglobal_load_lds_dword %0, off"::"v"(p.wo+t*128):"memory");
    if(t<5184-4096) asm volatile("s_mov_b32 m0, 0x27e00\n\ts_nop 0\n\tglobal_load_lds_dword %0, off"::"v"(p.wo+(4096+t)*128):"memory");
  } else if(ja>=nA){
    const int jb=ja-nA;
    role_b(p, jb<8? xcd*8+jb : 64+half*4+(jb-8) STAMPPASS);
  } else role_a(p, (pr?6*pr-1:0)+(ja>>2), half*4+(ja&3), ((ja>>2)+ja)&1 STAMPPASS);
}

constexpr int LDS_BYTES=160*1024;
extern "C" void kernel_launch(void* const* d_in, const int* in_sizes, int n_in, void* d_out, int out_size, void* d_ws, size_t ws_size, hipStream_t stream){
  Params p;
  p.x=(const char*)d_in[0]; p.wqkv=(const char*)d_in[1]; p.wo=(const char*)d_in[2];
  p.q=(const char*)d_in[3]; p.k=(const char*)d_in[4]; p.v=(const char*)d_in[5];
  p.sk=(const char*)d_in[6]; p.hk=(const char*)d_in[7]; p.dv=(const char*)d_in[8];
  p.out=(char*)d_out; p.ws=(char*)d_ws;
  (void)hipFuncSetAttribute((const void*)fused,hipFuncAttributeMaxDynamicSharedMemorySize,LDS_BYTES);
  fused<<<256,512,LDS_BYTES,stream>>>(p);
}
```

```cpp
#include <hip/hip_runtime.h>
#include <hip/hip_bf16.h>

typedef unsigned short u16;
typedef unsigned long long u64;
typedef unsigned v4u __attribute__((ext_vector_type(4)));
typedef unsigned v2u __attribute__((ext_vector_type(2)));
typedef short s4 __attribute__((ext_vector_type(4)));
typedef __bf16 b2 __attribute__((ext_vector_type(2)));
typedef __bf16 b8 __attribute__((ext_vector_type(8)));
typedef float f4 __attribute__((ext_vector_type(4)));
typedef float f2 __attribute__((ext_vector_type(2)));
#define LAS __attribute__((address_space(3)))
#define GAS __attribute__((address_space(1)))
#if defined(__HIP_DEVICE_COMPILE__)
#define NOPK __attribute__((target("no-packed-fp32-ops")))
#else
#define NOPK
#endif
#define DEV __device__ __forceinline__ NOPK
#define SB __builtin_amdgcn_sched_barrier(0)
#pragma clang diagnostic ignored "-Wint-to-pointer-cast"

#define STAMP(i)
#define CSTAMP(i)
#define STAMPDECL
#define STAMPARG
#define STAMPPASS
#define STAMPOUT

#define HSL 20
#define BSLP 16
#define TSL 4
#define BS 3
constexpr int NF0=13, PNISS=3, PNF=6, SPIN_G=1<<18, SPIN_L=1<<22;
struct Params {
  const char *x, *wqkv, *wo, *q, *k, *v, *sk, *hk, *dv;
  char *out, *ws;
};

DEV float lo2f(unsigned d){ return __builtin_bit_cast(float,d<<16); }
DEV float hi2f(unsigned d){ return __builtin_bit_cast(float,d&0xffff0000u); }
DEV unsigned pk2(float lo, float hi){ f2 p={lo,hi}; return __builtin_bit_cast(unsigned,__builtin_convertvector(p,b2)); }
DEV unsigned pk2a(float lo, float hi){ unsigned d; asm("v_cvt_pk_bf16_f32 %0, %1, %2":"=v"(d):"v"(lo),"v"(hi)); return d; }
DEV v4u ld128(unsigned a){ return *(LAS v4u*)a; }
DEV v2u ld64v(unsigned a){ return *(volatile LAS v2u*)a; }
DEV unsigned ld32(unsigned a){ return *(LAS unsigned*)a; }
DEV void st128(unsigned a, v4u v){ *(LAS v4u*)a=v; }
DEV void st64(unsigned a, v2u v){ *(LAS v2u*)a=v; }
DEV void st64v(unsigned a, v2u v){ *(volatile LAS v2u*)a=v; }
DEV void st32(unsigned a, unsigned v){ *(LAS unsigned*)a=v; }
DEV v2u ldtr(unsigned a){ return __builtin_bit_cast(v2u,__builtin_amdgcn_ds_read_tr16_b64_v4i16((LAS s4*)a)); }
DEV f4 mfma(v4u a, v4u b, f4 c){ return __builtin_amdgcn_mfma_f32_16x16x32_bf16(__builtin_bit_cast(b8,a),__builtin_bit_cast(b8,b),c,0,0,0); }
DEV v4u cat(v2u a, v2u b){ return (v4u){a.x,a.y,b.x,b.y}; }
DEV void gloadq(v4u& d, const char* sbase, unsigned voff){ asm volatile("global_load_dwordx4 %0, %1, %2":"=v"(d):"v"(voff),"s"(sbase):"memory"); }
DEV void gload(v4u& d, const char* sbase, unsigned voff){ asm volatile("global_load_dwordx4 %0, %1, %2 nt":"=v"(d):"v"(voff),"s"(sbase):"memory"); }
#define WAIT1(n,a) asm volatile("s_waitcnt vmcnt(" #n ")":"+v"(a)::"memory")
#define WAIT2(n,a,b) asm volatile("s_waitcnt vmcnt(" #n ")":"+v"(a),"+v"(b)::"memory")
#define WAIT3(n,a,b,c) asm volatile("s_waitcnt vmcnt(" #n ")":"+v"(a),"+v"(b),"+v"(c)::"memory")
#define WAIT4(n,a,b,c,d) asm volatile("s_waitcnt vmcnt(" #n ")":"+v"(a),"+v"(b),"+v"(c),"+v"(d)::"memory")
#define WAITL asm volatile("":::"memory"); __builtin_amdgcn_s_waitcnt(0xC07F); asm volatile("":::"memory")
DEV void ldsbar(){ WAITL; __builtin_amdgcn_s_barrier(); asm volatile("":::"memory"); }
DEV unsigned nexttag(unsigned e){ return e*2891336453u+1013904223u; }
DEV int imin(int a,int b){ return a<b?a:b; }
DEV int imax(int a,int b){ return a>b?a:b; }
#define WAITV(n) asm volatile("s_waitcnt vmcnt(" #n ")":::"memory")
#define BAR do{ asm volatile("":::"memory"); __builtin_amdgcn_s_barrier(); asm volatile("":::"memory"); }while(0)
#define LTOUCH(a) asm volatile("s_mov_b32 m0, 0x27e00\n\ts_nop 0\n\tglobal_load_lds_dword %0, off"::"v"(a):"memory")
#define AMM(acc,a,b) asm volatile("v_mfma_f32_16x16x32_bf16 %0, %1, %2, %0":"+v"(acc):"v"(a),"v"(b))
#define ARD(d,a,off) asm volatile("ds_read_b128 %0, %1 offset:%2":"=v"(d):"v"(a),"i"(off):"memory")
#define ATR(d,a,off) asm volatile("ds_read_b64_tr_b16 %0, %1 offset:%2":"=v"(d):"v"(a),"i"(off):"memory")
#define AADD(v,s) asm volatile("v_add_u32 %0, %1, %0":"+v"(v):"s"(s))
#define AR64(d,a,off) asm volatile("ds_read_b64 %0, %1 offset:%2":"=v"(d):"v"(a),"i"(off):"memory")
constexpr int B_HA=448, B_HB=4576+448, B_X0=9120, B_X1=13344, B_ST=17520, B_PITCH=19696, B_SCR=8*B_PITCH;

template<int BLO,int NB> struct PF { v4u h[8-BLO]; v2u x[NB][2]; };
template<int BLO,int NB,int OFF> DEV void p_rd(int i, PF<BLO,NB>& f, unsigned hb, unsigned xb){
  #pragma unroll
  for(int a=0;a<8-BLO;a++) if(i==2+a) ARD(f.h[a],hb,512*a+OFF);
  #pragma unroll
  for(int b=0;b<NB;b++){ const int q=b?8-BLO+2*b:0; if(i==q) AR64(f.x[b][0],xb,512*(7-BLO-b)+OFF); if(i==q+1) AR64(f.x[b][1],xb,512*(7-BLO-b)+8+OFF); }
}
template<int BLO,int NB> DEV void p_mm(int i, f4 (&acc)[8], PF<BLO,NB>& f){
  int n=0;
  #pragma unroll
  for(int b=0;b<NB;b++){
    #pragma unroll
    for(int a=0;a<8-BLO-b;a++){ if(n==i){ v4u x=cat(f.x[b][0],f.x[b][1]); AMM(acc[a+b+BLO],f.h[a],x); } n++; }
  }
}
template<int N,int BLO,int NB> DEV void p_wait(PF<BLO,NB>& f){
  asm volatile("s_waitcnt lgkmcnt(%0)"::"i"(N):"memory");
  #pragma unroll
  for(int a=0;a<8-BLO;a++) asm volatile("":"+v"(f.h[a]));
  #pragma unroll
  for(int b=0;b<NB;b++) asm volatile("":"+v"(f.x[b][0]),"+v"(f.x[b][1]));
}
template<int OFF,int BLO,int NB,int BL2,int NB2> DEV void p_block(f4 (&acc)[8], PF<BLO,NB>& fc, PF<BL2,NB2>& fn, unsigned hb, unsigned xb){
  constexpr int NR=8-BL2+2*NB2, NM=NB*(8-BLO)-NB*(NB-1)/2, EX=(NR>NM)?NR-NM:0;
  constexpr int I2=(NB==2? 6-BLO : (NB-2)*(8-BLO)-(NB-2)*(NB-3)/2);
  static_assert(I2>=EX && I2+EX<=NR && I2+EX<=15,"");
  p_wait<4>(fc);
  #pragma unroll
  for(int i=0;i<NM;i++){
    if(i==I2) asm volatile("s_waitcnt lgkmcnt(%0)"::"i"(I2+EX):"memory");
    p_mm<BLO,NB>(i,acc,fc);
    if(i<EX){ p_rd<BL2,NB2,OFF>(2*i,fn,hb,xb); p_rd<BL2,NB2,OFF>(2*i+1,fn,hb,xb); }
    else if(i+EX<NR) p_rd<BL2,NB2,OFF>(i+EX,fn,hb,xb);
  }
}
DEV void flag_wait(unsigned a, unsigned n){
  #pragma unroll 1
  for(int spin=0;spin<SPIN_L;spin++){ if(__builtin_amdgcn_ballot_w64(*(volatile LAS unsigned*)a<n)==0) break; __builtin_amdgcn_s_sleep(1); }
  asm volatile("":::"memory");
}
template<int BLO,int NB> DEV void b_seg(f4 (&acc)[8], PF<BLO,NB>& f0, unsigned hb, unsigned xb, int next){
  PF<BLO,NB> f1;
  #pragma unroll 1
  for(int r=0;r<4;r++){
    p_block<64>(acc,f0,f1,hb,xb);
    const int d=(r==3)? next-384 : 128;
    AADD(hb,d); AADD(xb,d);
    p_block<0>(acc,f1,f0,hb,xb);
  }
}
template<int BLO,int NB,int BL2,int NB2> DEV void b_segx(f4 (&acc)[8], PF<BLO,NB>& f0, PF<BL2,NB2>& fn, unsigned hb, unsigned xb, unsigned fa, unsigned need){
  PF<BLO,NB> f1; unsigned h=hb+B_PITCH, x=xb+B_PITCH;
  #pragma unroll 1
  for(int r=0;r<3;r++){
    p_block<64>(acc,f0,f1,h,x);
    AADD(h,128); AADD(x,128);
    p_block<0>(acc,f1,f0,h,x);
  }
  unsigned fl;
  asm volatile("ds_read_b32 %0, %1":"=v"(fl):"v"(fa):"memory");
  p_block<64>(acc,f0,f1,h,x);
  asm volatile("":"+v"(fl));
  if(__builtin_amdgcn_ballot_w64(fl<need)!=0) flag_wait(fa,need);
  p_block<0>(acc,f1,fn,hb,xb);
}
template<int JLO,int NJ> DEV void b_zout(f4 (&acc)[8], unsigned za){
  asm volatile("s_nop 7\n\ts_nop 3":::"memory");
  #pragma unroll
  for(int J=JLO;J<JLO+NJ;J++){
    asm volatile("":"+v"(acc[J]));
    st32(za+512*(7-J)+64,pk2a(acc[J][1],acc[J][0])); st32(za+512*(7-J),pk2a(acc[J][3],acc[J][2]));
  }
}

DEV unsigned sum5(unsigned m, v4u v){ return (m+v.x+v.y)+v.z+v.w; }
DEV void b_prep2(unsigned ta, unsigned tv, v4u sl0, v4u sh0, v4u sl1, v4u sh1, v4u i01, v4u i23, v4u& oa, v4u& ob){
  v4u a[2][4], vr[2];
  #pragma unroll
  for(int t=0;t<2;t++){ vr[t]=ld128(tv+t*B_PITCH); a[t][0]=ld128(ta+t*B_PITCH); a[t][2]=ld128(ta+t*B_PITCH+64); }
  #pragma unroll
  for(int t=0;t<2;t++){ a[t][1]=ld128(ta+t*B_PITCH+512); a[t][3]=ld128(ta+t*B_PITCH+576); }
  SB;
  const f4 z={0,0,0,0};
  f4 v1[2],v2[2],d1[2],d2[2];
  #pragma unroll
  for(int t=0;t<2;t++){ v1[t]=mfma(vr[t],i01,z); v2[t]=mfma(vr[t],i23,z); d1[t]=mfma(a[t][0],t?sl1:sl0,z); d2[t]=mfma(a[t][2],t?sl1:sl0,z); }
  #pragma unroll
  for(int t=0;t<2;t++){ d1[t]=mfma(a[t][1],t?sh1:sh0,d1[t]); d2[t]=mfma(a[t][3],t?sh1:sh0,d2[t]); }
  SB;
  #pragma unroll
  for(int t=0;t<2;t++){
    const v4u o={pk2(d2[t][3]*v2[t][3],d2[t][2]*v2[t][2]),pk2(d2[t][1]*v2[t][1],d2[t][0]*v2[t][0]),pk2(d1[t][3]*v1[t][3],d1[t][2]*v1[t][2]),pk2(d1[t][1]*v1[t][1],d1[t][0]*v1[t][0])};
    if(t) ob=o; else oa=o;
  }
}
DEV void b_put(unsigned a, v4u o){
  st128(a+B_X0,o);
  st32(a+B_X1+4,o.x); st64(a+B_X1+8,(v2u){o.y,o.z}); st32(a+B_X1+16,o.w);
}
DEV v4u b_diag(int di, unsigned e){ return (v4u){di==0?e:0u,di==1?e:0u,di==2?e:0u,di==3?e:0u}; }
constexpr int WS_B=4<<20, WS_BT=1280;
DEV void b_gate(unsigned ga, unsigned gb, v4u q, char* outp){
  const v2u y0=ldtr(ga), y1=ldtr(gb);
  #define G2(Q,Y) pk2(lo2f(Q)*lo2f(Y), hi2f(Q)*hi2f(Y))
  const v4u o={G2(q.x,y0.x),G2(q.y,y0.y),G2(q.z,y1.x),G2(q.w,y1.y)};
  *(GAS v4u*)outp=o;
}

DEV void role_b(const Params& p, int cg STAMPARG){
  const int tid=__builtin_amdgcn_workitem_id_x(), lane=tid&63, wave=__builtin_amdgcn_readfirstlane(tid>>6);
  const int i16=lane&15, g=lane>>4;
  constexpr unsigned BLK=512*1152;
  const int w=wave&3, ql=lane^3;
  const unsigned rq=(w*128+ql)*1152+cg*16;
  unsigned ga,gx; char* outp;
  #define GCONST { \
    ga=(i16>>2)*B_PITCH+B_HA+(2044-1536-64-128*w-16*g-4*(lane&3))*2; gx=ga+4*B_PITCH; outp=p.out+(size_t)(w*128+ql)*2304+1152+cg*16; \
    asm volatile("":"+v"(ga),"+v"(gx),"+v"(outp)); }
  #define GATE(b,Q0,Q1) { b_gate(ga+1024*(3-(b))+128,gx+1024*(3-(b))+128,Q0,outp+(size_t)(b)*512*2304); b_gate(ga+1024*(3-(b)),gx+1024*(3-(b)),Q1,outp+(size_t)((b)*512+64)*2304); }
  if(wave<4){
    v4u hr[8]; f4 a0[8],a1[8];
    const unsigned cb=2*w*B_PITCH;
    {
      const int u=tid&127, lx=imin(cg*(29*BS)+imin(u,29*BS-1),2048*BS-1), rx=lx/BS;
      const char* t1=(wave<2)? p.wqkv+(128*27+cg*(24*BS)+imin(u,24*BS-1))*128 : p.x+(rx*1152+(2+lx-rx*BS)*128);
      asm volatile("":"+v"(t1));
      BAR;
      if((wave&1)*64<29*BS) LTOUCH(t1); }
    __builtin_amdgcn_s_sleep(HSL);
    #pragma unroll
    for(int i=0;i<8;i++) gload(hr[i],p.hk,(cg*8+2*wave)*4096+i*1024+lane*16);
    #pragma unroll
    for(int c=0;c<2;c++){
      if(lane<56) st128(cb+c*B_PITCH+(lane<28? lane*16 : 4576+(lane-28)*16),(v4u){0,0,0,0});
      if(lane<31) st32(cb+c*B_PITCH+(lane<16? B_X0+4096+lane*4 : B_X1+4100+(lane-16)*4),0u);
    }
    unsigned hb=cb+((i16&1)?B_HA:B_HB)+(32*(i16>>1)+8*g)*2-448;
    unsigned xb=cb+((i16&1)? B_X0+(2046-2*i16+8*g)*2 : B_X1+(2048-2*i16+8*g)*2)-512*7-448;
    unsigned za=cb+B_HA+(2046-64*g-2*i16)*2-64-512*7, fx=B_SCR+16+4*(lane&3);
    asm volatile("":"+v"(hb),"+v"(xb),"+v"(za),"+v"(fx));
    GCONST
    const unsigned dw=*(const __attribute__((address_space(4))) unsigned*)(p.dv+cg*16+wave*4);
    #pragma unroll
    for(int j=0;j<8;j++){ a0[j]=(f4){0,0,0,0}; a1[j]=(f4){0,0,0,0}; asm volatile("":"+v"(a0[j]),"+v"(a1[j])); }
    #pragma unroll
    for(int c=0;c<2;c++){
      if(c==0) WAIT4(4,hr[0],hr[1],hr[2],hr[3]); else WAIT4(0,hr[4],hr[5],hr[6],hr[7]);
      if(lane==0) hr[4*c].x=pk2(lo2f(hr[4*c].x)+(c?hi2f(dw):lo2f(dw)),hi2f(hr[4*c].x));
      #pragma unroll
      for(int i=0;i<4;i++){
        const v4u h=hr[4*c+i];
        const unsigned first=i? __builtin_amdgcn_readlane(hr[4*c+i-1].w,63) : 0u;
        const unsigned pv=__builtin_amdgcn_update_dpp(first,h.w,0x138,0xf,0xf,false);
        const v4u s={__builtin_amdgcn_alignbit(h.x,pv,16),__builtin_amdgcn_alignbit(h.y,h.x,16),__builtin_amdgcn_alignbit(h.z,h.y,16),__builtin_amdgcn_alignbit(h.w,h.z,16)};
        st128(cb+c*B_PITCH+B_HA+i*1024+lane*16,h);
        st128(cb+c*B_PITCH+B_HB+i*1024+lane*16,s);
      }
    }
    STAMP(1);
    __builtin_amdgcn_s_setprio(1);
    PF<0,2> f0; PF<2,2> f1; PF<4,4> f2;
    #pragma unroll
    for(int i=2;i<10;i++) p_rd<0,2,0>(i,f0,hb,xb);
    ldsbar();
    p_rd<0,2,0>(0,f0,hb,xb); p_rd<0,2,0>(1,f0,hb,xb); p_rd<0,2,0>(10,f0,hb,xb); p_rd<0,2,0>(11,f0,hb,xb);
    asm volatile("s_waitcnt lgkmcnt(2)":::"memory");
    b_seg(a0,f0,hb,xb,B_PITCH);   b_zout<0,2>(a0,za);
    b_segx(a1,f0,f1,hb,xb,fx,1);  b_zout<0,2>(a1,za+B_PITCH);
    asm volatile("":::"memory"); *(volatile LAS unsigned*)(B_SCR+32+4*w)=1u;
    STAMP(3);
    b_seg(a0,f1,hb,xb,B_PITCH);   b_zout<2,2>(a0,za);
    b_segx(a1,f1,f2,hb,xb,fx,2);  b_zout<2,2>(a1,za+B_PITCH);
    asm volatile("":::"memory"); *(volatile LAS unsigned*)(B_SCR+32+4*w)=2u;
    STAMP(4);
    b_seg(a0,f2,hb,xb,B_PITCH);   b_zout<4,4>(a0,za);
    v4u qa,qb;
    gloadq(qa,p.q+3*BLK,rq); gloadq(qb,p.q+3*BLK+64*1152,rq);
    b_seg(a1,f2,hb+B_PITCH,xb+B_PITCH,0); b_zout<4,4>(a1,za+B_PITCH);
    p_wait<0>(f2);
    STAMP(5);
    WAIT2(0,qa,qb);
    ldsbar();
    STAMP(6);
    GATE(3,qa,qb)
    STAMP(7);
  } else {
    const int j=cg&7, cp=(cg&~7)+2*w;
    const unsigned rk=(j*64+w*8+(lane>>3))*1152+(cg>>3)*128+(lane&7)*16;
    v4u k0[4],k1[4],v0[4],v1[4],q0[3],q1[3],halo; unsigned sk0,sk1,ep;
    #define LOADKV(b) { gload(k0[b],p.k+(b)*BLK,rk); gload(k1[b],p.k+(b)*BLK+32*1152,rk); gload(v0[b],p.v+(b)*BLK,rk); gload(v1[b],p.v+(b)*BLK+32*1152,rk); }
    #define LOADQ(b) { gloadq(q0[b],p.q+(b)*BLK,rq); gloadq(q1[b],p.q+(b)*BLK+64*1152,rq); }
    unsigned junk=0;
    #define TOUCH(P,O) asm volatile("global_load_dword %0, %1, %2":"+v"(junk):"v"(O),"s"(P):"memory")
    asm volatile("global_load_ushort %0, %2, %3\n\tglobal_load_ushort %1, %2, %3 offset:64":"=&v"(sk0),"=&v"(sk1):"v"(cp*64+(lane&7)*8+g*2),"s"(p.sk):"memory");
    GAS unsigned* epoch=(GAS unsigned*)(p.ws+(32+(cg>>3))*256);
    asm volatile("global_load_dword %0, %1, %2 sc1":"=v"(ep):"v"(0),"s"(epoch):"memory");
    LOADKV(0)
    const int ht=512*((lane>>3)&3)+64*j-4+w;
    gload(halo,p.k,imax(ht,0)*1152+(cg>>3)*128+(lane&7)*16);
    const unsigned po=(cp*32+j)*WS_BT, co=(cg*32+2*w)*WS_BT;
    st32(B_SCR+4*w,0u); st32(B_SCR+16+4*w,0u); st32(B_SCR+32+4*w,0u);
    ldsbar();
    asm volatile("global_load_dword %0, %1, %2 sc1":"+v"(junk):"v"(co+imin(lane,19)*128),"s"(p.ws+WS_B):"memory");
    STAMP(10);
    GCONST
    #define KO(sl) ((sl)?B_ST+64:B_X1)
    #define VO(sl) ((sl)?B_ST+1088:B_X0)
    unsigned sw=(lane&7)*B_PITCH+(w*8+(lane>>3))*16, sh=(lane&7)*B_PITCH+w*16;
    unsigned ta=2*w*B_PITCH+(4+8*(i16>>2)+(lane&3)-g)*16, tv=2*w*B_PITCH+(32*(g&1)+8*(i16>>2)+4*(g>>1)+(lane&3))*16;
    unsigned xw=(lane&7)*B_PITCH+(2040-1536-64-128*w-32*(i16>>3)-8*g)*2;
    const int di=(lane&7)>>1;
    const unsigned one=0x3f80u<<(16*(lane&1));
    v4u i01=b_diag(di,(g==(i16>>3))?one:0u), i23=b_diag(di,(g==(i16>>3)+2)?one:0u);
    asm volatile("":"+v"(sw),"+v"(sh),"+v"(ta),"+v"(tv),"+v"(xw),"+v"(i01),"+v"(i23));
    const __amdgpu_buffer_rsrc_t xr=__builtin_amdgcn_make_buffer_rsrc(p.ws+WS_B,0,72*32*WS_BT,0x00020000);
    WAIT3(6,sk0,sk1,ep);
    unsigned tag=nexttag(ep);
    sk0<<=16*(lane&1); sk1<<=16*(lane&1);
    v4u sl0=b_diag(di,(i16<8)?sk0:0u), sh0=b_diag(di,(i16<8)?0u:sk0), sl1=b_diag(di,(i16<8)?sk1:0u), sh1=b_diag(di,(i16<8)?0u:sk1);
    asm volatile("":"+v"(tag),"+v"(sl0),"+v"(sh0),"+v"(sl1),"+v"(sh1));
    WAIT1(1,halo);
    STAMP(11);
    if(ht<0) halo=(v4u){0,0,0,0};
    #define STAGE(b,sl,n) { WAIT4(n,k0[b],k1[b],v0[b],v1[b]); \
        st128(sw+KO(sl),k0[b]); st128(sw+KO(sl)+512,k1[b]); st128(sw+VO(sl),v0[b]); st128(sw+VO(sl)+512,v1[b]); \
        if((lane>>3)==(b)) st128(sh+KO(sl)-64,halo); }
    #define SYNC(n) { WAITL; *(volatile LAS unsigned*)(B_SCR+4*w)=(n); flag_wait(B_SCR+4*(lane&3),n); }
    #define PROD(b,sl) { \
        v4u oa,ob; b_prep2(ta+KO(sl)-64,tv+VO(sl),sl0,sh0,sl1,sh1,i01,i23,oa,ob); \
        __builtin_amdgcn_raw_buffer_store_b128(oa,xr,lane*16+po+(b)*8*WS_BT,0,16); __builtin_amdgcn_raw_buffer_store_b32(sum5(tag,oa),xr,1024+lane*4,po+(b)*8*WS_BT,16); \
        __builtin_amdgcn_raw_buffer_store_b128(ob,xr,lane*16+po+(32+(b)*8)*WS_BT,0,16); __builtin_amdgcn_raw_buffer_store_b32(sum5(tag,ob),xr,1024+lane*4,po+(32+(b)*8)*WS_BT,16); }
    #define RECV(b) { v4u oa,ob; _Pragma("unroll 1") for(int spin=0;spin<SPIN_G;spin++){ asm volatile("":::"memory"); \
          oa=__builtin_amdgcn_raw_buffer_load_b128(xr,lane*16,co+(b)*8*WS_BT,16); ob=__builtin_amdgcn_raw_buffer_load_b128(xr,lane*16+WS_BT,co+(b)*8*WS_BT,16); \
          const unsigned ca=__builtin_amdgcn_raw_buffer_load_b32(xr,1024+lane*4,co+(b)*8*WS_BT,16), cb=__builtin_amdgcn_raw_buffer_load_b32(xr,1024+lane*4+WS_BT,co+(b)*8*WS_BT,16); \
          if(__builtin_amdgcn_ballot_w64(sum5(tag,oa)!=ca || sum5(tag,ob)!=cb)==0) break; __builtin_amdgcn_s_sleep(4); } \
        b_put(xw+1024*(3-(b))+128,oa); b_put(xw+1024*(3-(b)),ob); }
    __builtin_amdgcn_s_setprio(2);
    STAGE(0,1,1)
    SYNC(1)
    PROD(0,1)
    STAMP(12);
    __builtin_amdgcn_s_sleep(BSLP);
    RECV(0)
    STAMP(1);
    ldsbar();
    STAMP(2);
    LOADKV(1)
    LOADKV(2) LOADKV(3)
    STAGE(1,0,8)
    SYNC(2)
    PROD(1,0)
    SYNC(3)
    STAGE(2,1,8)
    STAGE(3,0,4)
    SYNC(4)
    PROD(2,1)
    STAMP(13);
    RECV(1)
    asm volatile("":::"memory"); *(volatile LAS unsigned*)(B_SCR+16+4*w)=1u;
    TOUCH(p.q+w*BLK,(lane*8+j)*1152+cg*16);
    PROD(3,0)
    STAMP(8);
    RECV(2)
    asm volatile(""::"v"(junk));
    LOADQ(0)
    SYNC(5)
    RECV(3)
    asm volatile("":::"memory"); *(volatile LAS unsigned*)(B_SCR+16+4*w)=2u;
    STAMP(9);
    LOADQ(1) LOADQ(2)
    flag_wait(B_SCR+32+4*(lane&3),1);
    WAIT2(4,q0[0],q1[0]);
    GATE(0,q0[0],q1[0])
    STAMP(4);
    flag_wait(B_SCR+32+4*(lane&3),2);
    if(tid==256 && j==0) __hip_atomic_store(epoch,tag,__ATOMIC_RELAXED,__HIP_MEMORY_SCOPE_AGENT);
    WAIT4(2,q0[1],q1[1],q0[2],q1[2]);
    GATE(1,q0[1],q1[1])
    STAMP(5);
    ldsbar();
    STAMP(6);
    GATE(2,q0[2],q1[2])
    STAMP(7);
  }
}

constexpr int A_TM=96, A_SLOT=41984, A_WOFF=14336, A_IMG=83968, A_IROW=456, A_ZERO=136192, A_TAG=A_ZERO+288, A_CST=136704;
constexpr int WS_O=65536, WS_OM=19*1024, WS_OT=6*WS_OM;
typedef __amdgpu_buffer_rsrc_t rsrc_t;
DEV void dma16(const v4u& srd, unsigned voff, unsigned soff, unsigned lds){
  asm volatile("s_mov_b32 m0, %3\n\ts_nop 0\n\tbuffer_load_dwordx4 %0, %1, %2 offen lds"::"v"(voff),"s"(srd),"s"(soff),"s"(lds):"memory");
}
#define DW1 "s_add_u32 m0, m0, 3456\n\ts_add_u32 %0, %0, 27648\n\tbuffer_load_dwordx4 %1, %2, %0 offen lds\n\t"
DEV void dma_w4(const v4u& srd, unsigned voff, unsigned soff, unsigned lds){
  asm volatile("s_lshr_b64 exec, exec, 10\n\ts_mov_b32 m0, %3\n\ts_nop 0\n\tbuffer_load_dwordx4 %1, %2, %0 offen lds\n\t"
               DW1 DW1 DW1 "s_mov_b64 exec, -1"
               :"+s"(soff):"v"(voff),"s"(srd),"s"(lds):"memory","scc");
}
DEV v4u mksrd(const char* p, unsigned bytes){
  const u64 a=(u64)p; return (v4u){(unsigned)a,(unsigned)(a>>32)&0xffffu,bytes,0x00020000u};
}
struct AFr { v4u x[7]; v2u w[4][2]; };
struct AOp { v4u x[7], w[4]; };
template<int WO> DEV void a_rd(int i, AFr& f, unsigned xe, unsigned x0, unsigned xo, unsigned wa, unsigned we){
  switch(i){
    case 0: ATR(f.w[3][0],we,WO); break;      case 1: ATR(f.w[3][1],we,WO+432); break;
    case 2: ARD(f.x[0],xe,0); break;          case 3: ARD(f.x[1],xe,2048); break;   case 4: ARD(f.x[2],xe,4096); break;
    case 5: ATR(f.w[0][0],wa,WO); break;      case 6: ATR(f.w[0][1],wa,WO+432); break;
    case 7: ATR(f.w[1][0],wa,WO+32); break;   case 8: ATR(f.w[1][1],wa,WO+464); break;
    case 9: ATR(f.w[2][0],wa,WO+64); break;   case 10: ATR(f.w[2][1],wa,WO+496); break;
    case 11: ARD(f.x[3],x0,0); break;
    case 12: ARD(f.x[4],xo,0); break;         case 13: ARD(f.x[5],xo,2048); break;  case 14: ARD(f.x[6],xo,4096); break;
  }
}
DEV void a_wait(AFr& f, AOp& o){
  asm volatile("s_waitcnt lgkmcnt(0)":"+v"(f.x[0]),"+v"(f.x[1]),"+v"(f.x[2]),"+v"(f.x[3]),"+v"(f.x[4]),"+v"(f.x[5]),"+v"(f.x[6]),
     "+v"(f.w[0][0]),"+v"(f.w[0][1]),"+v"(f.w[1][0]),"+v"(f.w[1][1]),"+v"(f.w[2][0]),"+v"(f.w[2][1]),"+v"(f.w[3][0]),"+v"(f.w[3][1])::"memory");
  #pragma unroll
  for(int m=0;m<7;m++) o.x[m]=f.x[m];
  #pragma unroll
  for(int c=0;c<4;c++) o.w[c]=cat(f.w[c][0],f.w[c][1]);
}
DEV void a_mm(int i, f4 (&acc)[3][7], f4 (&ace)[3], const AOp& f){
  if(i<3) AMM(ace[i],f.w[3],f.x[i]); else AMM(acc[(i-3)%3][(i-3)/3],f.w[(i-3)%3],f.x[(i-3)/3]);
}
DEV float max3(float a,float b,float c){ float d; asm("v_max3_f32 %0, %1, %2, %3":"=v"(d):"v"(a),"v"(b),"v"(c)); return d; }
DEV float max2(float a,float b){ float d; asm("v_max_f32 %0, %1, %2":"=v"(d):"v"(a),"v"(b)); return d; }
DEV void tr4(unsigned (&a)[4]){
  auto r0=__builtin_amdgcn_permlane16_swap(a[0],a[1],false,false);
  auto r1=__builtin_amdgcn_permlane16_swap(a[2],a[3],false,false);
  auto s0=__builtin_amdgcn_permlane32_swap(r0[0],r1[0],false,false);
  auto s1=__builtin_amdgcn_permlane32_swap(r0[1],r1[1],false,false);
  a[0]=s0[0]; a[1]=s1[0]; a[2]=s0[1]; a[3]=s1[1];
}

DEV unsigned rs4(unsigned a, unsigned b, unsigned c, unsigned d){
  auto x=__builtin_amdgcn_permlane16_swap(a,b,false,false);
  auto y=__builtin_amdgcn_permlane16_swap(c,d,false,false);
  auto z=__builtin_amdgcn_permlane32_swap(x[0]+x[1],y[0]+y[1],false,false);
  return z[0]+z[1];
}
DEV void role_a(const Params& p, int rt, int grp, int rot STAMPARG){
  const int tid=__builtin_amdgcn_workitem_id_x(), lane=tid&63, wave=__builtin_amdgcn_readfirstlane(tid>>6);
  const int row0=rt*A_TM, nrow=imin(A_TM,2048-row0);
  const int i16=lane&15, g=lane>>4;
  GAS unsigned* epoch=(GAS unsigned*)(p.ws+rt*256);
  unsigned ka,kb,qb,va,sel0,sel1,sel2,sel3; v4u vl=(v4u){0,0,0,0}; unsigned vl4=0;
  const int par=i16&1; const bool cur0=(4*g+par<=i16), cur1=(4*g+par+2<=i16);
  const rsrc_t wsr=__builtin_amdgcn_make_buffer_rsrc(p.ws+WS_O,0,22*WS_OT,0x00020000);
  if(wave>=4){
    const int lw=wave-4;
    const v4u sx=mksrd(p.x,2048*1152), sw=mksrd(p.wqkv,576*3456), so=mksrd(p.wo,576*1152);
    int st=rot, cnt=0;
    unsigned sl=lw*1024;
    const int l27=lane>=27? lane-27:lane, seg=(l27>=18)?2:(l27>=9?1:0);
    const unsigned vw=(lane>=27?4*3456:0)+seg*(1152-144)+l27*16+grp*144+lw*3456;
    __builtin_amdgcn_s_setprio(3);
    unsigned vx[4];
    #pragma unroll
    for(int i=0;i<4;i++){
      const int ir=8*(lw+4*i)+(lane>>3); int tok=row0-16+ir;
      tok=imax(tok,imax(row0-14,0)); tok=imin(tok,row0+nrow-1);
      vx[i]=tok*1152+(((lane&7)^((ir>>1)&7))<<4);
    }
    #define A_XA() do{ dma16(sx,vx[0],st*128,sl); dma16(sx,vx[1],st*128,sl+4096); }while(0)
    #define A_HA() do{ dma_w4(sw,vw,st*64*3456,sl+A_WOFF-lw*160); A_XA(); }while(0)
    #define A_HB() do{ dma16(sx,vx[2],st*128,sl+8192); if(lw<2) dma16(sx,vx[3],st*128,sl+12288); dma_w4(sw,vw,(st*64+32)*3456,sl+A_WOFF-lw*160+13824); \
      cnt++; st=(cnt==8)?8:(cnt^rot); sl=(sl>=2*A_SLOT)?sl-2*A_SLOT:sl+A_SLOT; }while(0)
    A_XA(); dma16(sx,vx[2],st*128,sl+8192); if(lw<2) dma16(sx,vx[3],st*128,sl+12288);
    cnt=1; st=1^rot; sl+=A_SLOT;
    A_HA();
    WAITV(6);
    BAR;
    #define LSTAMP(n)
    #define LUN _Pragma("unroll 1")
    A_HB(); A_HA(); WAITV(6); BAR; LSTAMP(1)
    A_HB();
    LUN
    for(int n=2;n<8;n++){ A_HA(); A_HB(); if(lw<2) WAITV(12); else WAITV(11); BAR; LSTAMP(n) }
    const v4u c0=ld128(A_CST+tid*16-4096), c1=ld128(A_CST+tid*16), c2=ld128(A_CST+tid*16+4096);
    vl=ld128(A_CST+tid*16+8192);
    ka=c0.x; kb=c0.y; qb=c0.z; va=c0.w; sel0=c1.x; sel1=c1.y; sel2=c1.z; sel3=c1.w; vl4=c2.w;
    #define A_ISSUE_WO(P0,P1) do{ _Pragma("unroll 1") for(int P=(P0);P<(P1);P++){ \
        dma16(so,c2.x,P*72*1152,P*9216+lw*1024); dma16(so,c2.y,P*72*1152,P*9216+lw*1024+4096); \
        if((P&3)==lw) dma16(so,c2.z,P*72*1152,P*9216+8192); } }while(0)
    A_ISSUE_WO(0,2);
    if(lw<2) WAITV(5); else WAITV(4);
    BAR;
    STAMP(13);
    A_ISSUE_WO(2,4);
    BAR;
    A_ISSUE_WO(4,8);
    __builtin_amdgcn_s_setprio(0);
    STAMP(2);
  } else {
    unsigned ep;
    asm volatile("global_load_dword %0, %1, %2 sc1":"=v"(ep):"v"(0),"s"(epoch):"memory");
    { const unsigned plo=par?0x01000c0cu:0x0c0c0100u, phi=par?0x03020c0cu:0x0c0c0302u, pzz=0x0c0c0c0cu;
      sel0=cur0?pzz:plo; sel1=cur1?pzz:phi; sel2=cur0?plo:pzz; sel3=cur1?phi:pzz;
      ka=A_IMG+(16*wave+i16)*A_IROW+8*g; kb=(g==0)?ka+32:A_ZERO-144; qb=(g==0)?ka+32+16*A_IROW:A_ZERO;
      va=A_IMG+(16*wave+4*g+(i16>>2))*A_IROW+288+(lane&3)*8;
      const unsigned d=64*A_IROW, dz=(g==0)?d:0u;
      st128(A_CST+tid*16,(v4u){ka+d,kb+dz,qb+dz,va+d}); st128(A_CST+tid*16+4096,(v4u){sel0,sel1,sel2,sel3});
      unsigned vo[3], vv[5];
      #pragma unroll
      for(int i=0;i<3;i++){
        const int ci=(i<2? wave+4*i : 8)*64+lane, rho=ci/9, cc=ci-rho*9;
        vo[i]=(32*(rho>>5)+16*(rho&1)+4*((rho>>3)&3)+((rho>>1)&3))*1152+grp*144+cc*16;
      }
      #pragma unroll
      for(int i=0;i<5;i++){
        const int ci=imin((wave&1)+2*i,8)*64+lane, rho=ci/9, cc=ci-rho*9;
        vv[i]=(72*(rho>>3)+64+4*(rho&1)+((rho>>1)&3))*1152+grp*144+cc*16;
      }
      st128(A_CST+tid*16+8192,(v4u){vo[0],vo[1],vo[2],vv[4]}); st128(A_CST+tid*16+12288,(v4u){vv[0],vv[1],vv[2],vv[3]});
      asm volatile("":"+v"(ka),"+v"(kb),"+v"(qb),"+v"(va),"+v"(sel0),"+v"(sel1),"+v"(sel2),"+v"(sel3)); }
    f4 acc[3][7], ace[3];
    #pragma unroll
    for(int c=0;c<3;c++){ ace[c]=(f4){0,0,0,0};
      #pragma unroll
      for(int m=0;m<7;m++) acc[c][m]=(f4){0,0,0,0}; }
    const int e0=(wave&1)?4:1, o0=(wave&1)?1:4;
    const unsigned xl=i16*128+((g^(i16>>1))<<4);
    const unsigned wl=A_WOFF+(8*g+2*(i16>>2))*432+(lane&3)*8;
    unsigned ae0=xl+e0*2048, az0=xl, ao0=xl+o0*2048, ae1=ae0^64, az1=az0^64, ao1=ao0^64, wa=wl+(2+3*wave)*32, we=wl+(wave>>1)*32;
    const unsigned ia=A_IMG+i16*A_IROW+g*8;
    unsigned ie=ia+e0*16*A_IROW+(2+3*wave)*32, iz=ia+(2+3*wave)*32, io=ia+o0*16*A_IROW+(2+3*wave)*32, ix=ia+e0*16*A_IROW+(wave>>1)*32;
    asm volatile("":"+v"(ae0),"+v"(az0),"+v"(ao0),"+v"(ae1),"+v"(az1),"+v"(ao1),"+v"(wa),"+v"(we),"+v"(ie),"+v"(iz),"+v"(io),"+v"(ix));
    #pragma unroll
    for(int c=0;c<3;c++){ asm volatile("":"+v"(ace[c]));
      #pragma unroll
      for(int m=0;m<7;m++) asm volatile("":"+v"(acc[c][m])); }
    if(tid<72) st32(A_ZERO+tid*4,0u);
    WAIT1(0,ep);
    if(tid==0) st32(A_TAG,nexttag(ep));
    int slot=0;
    AFr f0,f1; AOp g0,g1;
    ldsbar();
    STAMP(1);
    #pragma unroll
    for(int i=0;i<15;i++) a_rd<0>(i,f0,ae0,az0,ao0,wa,we);
    a_wait(f0,g0);
    #pragma unroll 1
    for(int s=0;s<8;s++){
      const int dl=(slot==2)?-2*A_SLOT:A_SLOT; slot=(slot==2)?0:slot+1;
      #pragma unroll
      for(int i=0;i<24;i++){
        a_mm(i,acc,ace,g0);
        if(i<15) a_rd<13824>(i,f1,ae1,az1,ao1,wa,we);
        if(i==15) AADD(ae0,dl); if(i==16) AADD(az0,dl); if(i==17) AADD(ao0,dl); if(i==18) AADD(wa,dl); if(i==19) AADD(we,dl);
      }
      a_wait(f1,g1);
      BAR;

      #pragma unroll
      for(int i=0;i<24;i++){
        a_mm(i,acc,ace,g1);
        if(i<15) a_rd<0>(i,f0,ae0,az0,ao0,wa,we);
        if(i==15) AADD(ae1,dl); if(i==16) AADD(az1,dl); if(i==17) AADD(ao1,dl);
      }
      a_wait(f0,g0);
    }
    #pragma unroll
    for(int i=0;i<NF0;i++){ a_mm(i,acc,ace,g0); a_rd<13824>(2*i,f1,ae1,az1,ao1,wa,we); a_rd<13824>(2*i+1,f1,ae1,az1,ao1,wa,we); }
    a_wait(f1,g1);
    STAMP(9);
    BAR;
    STAMP(14);
    #pragma unroll
    for(int i=0;i<24+3;i++){
      if(i<24){ a_mm(i,acc,ace,g1); }
      if(i>=3){ const int t=i-3;
        if(i==3 || i>=24) asm volatile("s_nop 2":::"memory");
        f4& v=(t<3)?ace[t]:acc[(t-3)%3][(t-3)/3]; const int m=(t-3)/3, c=(t-3)%3;
        unsigned lo,hi;
        asm volatile("v_cvt_pk_bf16_f32 %0, %2, %3\n\tv_cvt_pk_bf16_f32 %1, %4, %5":"=&v"(lo),"=&v"(hi):"v"(v[0]),"v"(v[1]),"v"(v[2]),"v"(v[3]));
        st64v((t<3)? ix+t*16*A_IROW : ((m<3)?ie+m*16*A_IROW:(m==3?iz:io+(m-4)*16*A_IROW))+c*32,(v2u){lo,hi});
      }
      if(i+NF0<24) a_mm(i+NF0,acc,ace,g0);
    }
    SB;
    STAMP(2);
  }
  ldsbar();
  STAMP(3);
  const unsigned tag=ld32(A_TAG);
  if(wave>=6){
    const v4u so=mksrd(p.wo,576*1152);
    #pragma unroll
    for(int i=0;i<5;i++) if(i<4 || wave==6) dma16(so,i<4?vl[i]:vl4,0,8*9216+(wave&1)*1024+i*2048);
    WAITV(0);
  } else {
    v4u qf[4],kp[4],kc[4],vf[5];
    #pragma unroll
    for(int h=0;h<4;h++){
      const int hb=36*h-4*(h&1);
      qf[h]=cat(ld64v(ka+16*A_IROW+hb),ld64v(qb+hb));
      kp[h]=cat(ld64v(ka+144+hb),ld64v(kb+144+hb));
      kc[h]=cat(ld64v(ka+16*A_IROW+144+hb),ld64v(qb+144+hb));
    }
    SB;
    f4 sp[4],sc[4];
    #pragma unroll
    for(int h=0;h<4;h++){
      v4u q=qf[h];
      if(h&1) q.x=(g==0)?0u:q.x; else q.w=0u;
      sp[h]=mfma(kp[h],q,(f4){0,0,0,0}); sc[h]=mfma(kc[h],q,(f4){0,0,0,0});
    }
    SB;
    #pragma unroll
    for(int c=0;c<5;c++) vf[c]=cat(ldtr(va+32*c),ldtr(va+16*A_IROW+32*c));
    SB;
    unsigned a0[4],a1[4];
    #pragma unroll
    for(int h=0;h<4;h++){
      const float c0=sc[h][0],c1=sc[h][1],c2=sc[h][2],c3=sc[h][3],p0=sp[h][0],p1=sp[h][1],p2=sp[h][2],p3=sp[h][3];
      const float ca=par?c1:c0, pa=par?p1:p0, cb=par?c3:c2, pb=par?p3:p2;
      a0[h]=__builtin_bit_cast(unsigned,cur0?ca:pa);
      a1[h]=__builtin_bit_cast(unsigned,cur1?cb:pb);
    }
    if(row0+16*wave==0){
      const unsigned mk=__builtin_bit_cast(unsigned,-1e9f/0.23570226039551584f);
      #pragma unroll
      for(int h=0;h<4;h++){ a0[h]=cur0?a0[h]:mk; a1[h]=cur1?a1[h]:mk; }
    }
    tr4(a0); tr4(a1);
    float l[8];
    #pragma unroll
    for(int i=0;i<4;i++){ l[2*i]=__builtin_bit_cast(float,a0[i]); l[2*i+1]=__builtin_bit_cast(float,a1[i]); }
    constexpr float SC=0.23570226039551584f*1.4426950408889634f;
    const float mx=max3(max3(l[0],l[1],l[2]),max3(l[3],l[4],l[5]),max2(l[6],l[7]));
    #pragma unroll
    for(int i=0;i<8;i++) l[i]=__builtin_amdgcn_exp2f((l[i]-mx)*SC);
    const float inv=__builtin_amdgcn_rcpf(((l[0]+l[1])+(l[2]+l[3]))+((l[4]+l[5])+(l[6]+l[7])));
    unsigned pp[4];
    #pragma unroll
    for(int i=0;i<4;i++) pp[i]=pk2(l[2*i]*inv,l[2*i+1]*inv);
    tr4(pp);
    v4u pf[4];
    #pragma unroll
    for(int h=0;h<4;h++) pf[h]=(v4u){__builtin_amdgcn_perm(0u,pp[h],sel0),__builtin_amdgcn_perm(0u,pp[h],sel1),__builtin_amdgcn_perm(0u,pp[h],sel2),__builtin_amdgcn_perm(0u,pp[h],sel3)};
    const f4 z={0,0,0,0};
    const f4 o0=mfma(vf[0],pf[0],z), a1_=mfma(vf[1],pf[0],z), b1=mfma(vf[1],pf[1],z), a2=mfma(vf[2],pf[1],z),
             b2_=mfma(vf[2],pf[2],z), a3=mfma(vf[3],pf[2],z), b3=mfma(vf[3],pf[3],z), o4=mfma(vf[4],pf[3],z);
    const v4u t01={pk2(o0[0],o0[1]),pk2(o0[2],o0[3]), (g==0)?pk2(a1_[0],a1_[1]):pk2(b1[0],b1[1]), pk2(b1[2],b1[3])};
    const v4u t23={(g==0)?pk2(a2[0],a2[1]):pk2(b2_[0],b2_[1]), (g==0)?pk2(a2[2],a2[3]):pk2(b2_[2],b2_[3]),
                   (g<2)?pk2(a3[0],a3[1]):pk2(b3[0],b3[1]), (g==0)?pk2(a3[2],a3[3]):pk2(b3[2],b3[3])};
    const unsigned u0=pk2(o4[0],o4[1]), u1=pk2(o4[2],o4[3]);
    auto r0=__builtin_amdgcn_permlane16_swap(u0,u0,false,false);
    auto r1=__builtin_amdgcn_permlane16_swap(u1,u1,false,false);
    const unsigned ob=rt*WS_OT+wave*WS_OM;
    __builtin_amdgcn_raw_buffer_store_b128(t01,wsr,ob+2*grp*1024+lane*16,0,16);
    __builtin_amdgcn_raw_buffer_store_b128(t23,wsr,ob+2*grp*1024+1024+lane*16,0,16);
    const v4u t4={(unsigned)r0[0],(unsigned)r1[0],(unsigned)r0[1],(unsigned)r1[1]};
    unsigned hs=sum5(sum5(0,t01),t23);
    { auto x=__builtin_amdgcn_permlane16_swap(hs,hs,false,false); hs=x[0]+x[1];
      auto y=__builtin_amdgcn_permlane32_swap(hs,hs,false,false); hs=y[0]+y[1]; }
    if(g==0){
      __builtin_amdgcn_raw_buffer_store_b128(t4,wsr,ob+16*1024+grp*256+lane*16,0,16);
      __builtin_amdgcn_raw_buffer_store_b64((v2u){sum5(hs,t4)+tag,tag},wsr,ob+18*1024+grp*128+lane*8,0,16);
    }
    WAITV(4);
  }
  STAMP(4);
  BAR;
  STAMP(5);
  {
    const int hf=wave&1, mo=(wave<4)?wave:4+((wave-4)>>1), m0=(wave<4)?(wave^1):mo;
    const unsigned wa=(8*g+2*(i16>>2))*144+(lane&3)*8, wb=wa+hf*8*4608, wc=wa+(16+hf)*4608;
    const v4u so=mksrd(p.ws+WS_O,22*WS_OT);
    const unsigned vo=lane*16;
    const int s0=rt*WS_OT+m0*WS_OM+hf*8192, s1=rt*WS_OT+mo*WS_OM+hf*8192, dl=16384-hf*7168, dr=18432-hf*7680;
    #define WFR(a,ks,c) cat(ldtr((a)+(ks)*4608+(c)*32),ldtr((a)+(ks)*4608+(c)*32+144))
    #define OLD(d,S,k) asm volatile("buffer_load_dwordx4 %0, %1, %2, %3 offen offset:%4 sc1":"=v"(d):"v"(vo),"s"(so),"s"((S)+((k)==8?dl:((k)>>2)*4096)),"i"(((k)&3)*1024):"memory")
    #define RLD(d,S) asm volatile("buffer_load_dwordx2 %0, %1, %2, %3 offen sc1":"=v"(d):"v"(vo>>1),"s"(so),"s"((S)+dr):"memory")
    v4u wf[9][5], of[9]; v2u r0,r1; f4 acc[5];
    #define ATT { RLD(r0,s0); RLD(r1,s1); _Pragma("unroll") for(int k_=0;k_<PNF;k_++) OLD(of[k_],s0,k_); }
    #pragma unroll
    for(int k=0;k<9;k++){
      #pragma unroll
      for(int c=0;c<5;c++) wf[k][c]=(k<8)?WFR(wb,k,c):WFR(wc,0,c);
      if(k==PNISS){ SB; ATT SB; }
    }
    WAITL;
    #pragma unroll
    for(int k=0;k<9;k++){
      #pragma unroll
      for(int c=0;c<5;c++) asm volatile("":"+v"(wf[k][c]));
    }
    STAMP(12);
    const unsigned red=A_IMG+lane*16, rf=A_ZERO+260;
    if(wave>=4) __builtin_amdgcn_s_setprio(1);
    #define UNIT(NEXT,R,OK) { unsigned hs[5]; \
      _Pragma("unroll") for(int k=0;k<9;k++){ \
        asm volatile("s_waitcnt vmcnt(%1)":"+v"(of[k]):"i"((NEXT)?8:8-k):"memory"); \
        _Pragma("unroll") for(int c=0;c<5;c++){ \
          if(k==0) asm volatile("v_mfma_f32_16x16x32_bf16 %0, %1, %2, 0":"=&v"(acc[c]):"v"(wf[k][c]),"v"(of[k])); else AMM(acc[c],wf[k][c],of[k]); \
          if(c==0){ if(k&1) asm volatile("v_add3_u32 %0, %0, %1, %2":"+v"(hs[k>>1]):"v"(of[k].x),"v"(of[k].y)); \
                    else asm volatile("v_add_u32 %0, %1, %2":"=v"(hs[k>>1]):"v"(of[k].x),"v"(of[k].y)); } \
          if(c==1) asm volatile("v_add3_u32 %0, %0, %1, %2":"+v"(hs[k>>1]):"v"(of[k].z),"v"(of[k].w)); } \
        if(NEXT) OLD(of[k],s1,k); } \
      asm volatile("s_nop 7\n\ts_nop 3":::"memory"); SB; \
      OK=__builtin_amdgcn_ballot_w64(rs4(hs[0],hs[1],hs[2],hs[3])+hs[4]+tag!=R.x)==0; }
    #define SEND(m) { _Pragma("unroll") for(int c=0;c<5;c++) st128(red+(m)*5120+c*1024,__builtin_bit_cast(v4u,acc[c])); asm volatile("":::"memory"); *(volatile LAS unsigned*)(rf+4*(m))=1u; }
    #define PWAIT(m) { _Pragma("unroll 1") for(int spin=0;spin<SPIN_L;spin++) if(__builtin_amdgcn_readfirstlane(*(volatile LAS unsigned*)(rf+4*(m)))) break; asm volatile("":::"memory"); }
    #pragma unroll 1
    for(int tries=0;tries<4;tries++){
      #pragma unroll 1
      for(int spin=0;spin<SPIN_G;spin++){
        asm volatile("s_waitcnt vmcnt(%2)":"+v"(r0),"+v"(r1):"i"(PNF):"memory");
        if(__builtin_amdgcn_ballot_w64(r0.y!=tag || r1.y!=tag)==0) break;
        WAITV(0);
        ATT
      }
      #pragma unroll
      for(int k=PNF;k<9;k++) OLD(of[k],s0,k);
      STAMP(6);
      bool ok0=true, ok1;
      if(wave<4){
        UNIT(1,r0,ok0)
        STAMP(10);
        if(ok0) SEND(m0)
      }
      STAMP(11);
      UNIT(0,r1,ok1)
      if(ok0 && ok1) break;
      ATT
    }
    if(wave>=4 && hf) SEND(mo)
    else { PWAIT(mo)
      #pragma unroll
      for(int c=0;c<5;c++) acc[c]+=__builtin_bit_cast(f4,ld128(red+mo*5120+c*1024)); }
    STAMP(7);
    if(wave<4 || !hf){
      const unsigned tb=A_IMG+30720+mo*2560;
      #pragma unroll
      for(int c=0;c<5;c++) st64(tb+i16*160+g*8+c*32,(v2u){pk2(acc[c][0],acc[c][1]),pk2(acc[c][2],acc[c][3])});
      const int l9=(lane*57)>>9, c9=lane-l9*9;
      const rsrc_t orr=__builtin_amdgcn_make_buffer_rsrc(p.out+(size_t)(row0+mo*16)*2304+grp*144,0,__builtin_amdgcn_readfirstlane(imax(imin(nrow-mo*16,16),0)*2304),0x00020000);
      const unsigned oo=(lane<63)? l9*2304+c9*16 : 0x40000000u;
      #pragma unroll
      for(int i=0;i<3;i++) __builtin_amdgcn_raw_buffer_store_b128(ld128(tb+(7*i+l9)*160+c9*16),orr,oo+i*7*2304,0,0);
    }
    asm volatile(".Lcpf1:");
    if(tid==0 && grp==0) __hip_atomic_store(epoch,tag,__ATOMIC_RELAXED,__HIP_MEMORY_SCOPE_AGENT);
  }
  STAMP(8);
}

NOPK __global__ __launch_bounds__(512,2) void fused(Params p){
  STAMPDECL;
  STAMP(0);
  const int b=__builtin_amdgcn_workgroup_id_x(), xcd=b&7, j=b>>3, pr=xcd>>1, half=xcd&1;
  const int ja=(pr==0)? j-4 : j;
  int isb=ja-((pr==0||pr==3)?20:24), cg=(isb<8)? xcd*8+isb : 56+half*4+isb, grp=half*4+(ja&3), rot=((ja>>2)+ja)&1;
  const int tid=__builtin_amdgcn_workitem_id_x(), lane=tid&63, wave=__builtin_amdgcn_readfirstlane(tid>>6);
  {
    u64 pc; unsigned len;
    asm volatile("s_getpc_b64 %0\n.Lcpf0:\n\ts_mov_b32 %1, .Lcpf1-.Lcpf0":"=s"(pc),"=s"(len));
    unsigned o=((j&7)+lane*8)*128; o=o<len?o:len;
    asm volatile("s_cmp_lg_u32 %2, 0\n\ts_cbranch_scc1 1f\n\ts_lshr_b64 exec, exec, 24\n\ts_mov_b32 m0, 0x27f00\n\ts_nop 0\n\tglobal_load_lds_dword %0, %1\n\ts_mov_b64 exec, -1\n1:"
                 ::"v"(o&~3u),"s"(pc),"s"(wave):"memory","scc");
  }
  { const int lw=wave&3, l27=lane>=27? lane-27:lane, seg=(l27>=18)?2:(l27>=9?1:0);
    const unsigned vw=(lane>=27?4*3456:0)+seg*(1152-144)+l27*16+grp*144+lw*3456; const v4u sw=mksrd(p.wqkv,576*3456);
    unsigned so=rot*64*3456;
    asm volatile("s_cmp_lt_i32 %4, 0\n\ts_cbranch_scc1 1f\n\t"
                 "s_lshr_b64 exec, exec, 10\n\ts_mov_b32 m0, %3\n\ts_nop 0\n\tbuffer_load_dwordx4 %1, %2, %0 offen lds\n\t"
                 DW1 DW1 DW1 DW1 DW1 DW1 DW1 "s_mov_b64 exec, -1\n1:"
                 :"+s"(so):"v"(vw),"s"(sw),"s"(lw*864+A_WOFF),"s"(ja|(wave-4)|~isb),"s"(p.ws),"s"(p.out):"memory","scc");
  }
  if(__builtin_expect(ja<0,0)){
    const int t=(half*4+j)*512+tid;
    #pragma unroll 1
    for(int st=2+BS;st<9;st++){
      const char* a=(t<2048)? p.x+(t*1152+st*128) : p.wqkv+(st*1728+imin(t-2048,1727))*128;
      if(t<3776) LTOUCH(a);
    }
    LTOUCH(p.wo+t*128);
    if(t<5184-4096) LTOUCH(p.wo+(4096+t)*128);
    return;
  }
  if(__builtin_expect(isb>=0,1)) role_b(p,cg STAMPPASS);
  else role_a(p, (pr?6*pr-1:0)+(ja>>2), grp, rot STAMPPASS);
}

constexpr int LDS_BYTES=160*1024;
extern "C" void kernel_launch(void* const* d_in, const int* in_sizes, int n_in, void* d_out, int out_size, void* d_ws, size_t ws_size, hipStream_t stream){
  Params p;
  p.x=(const char*)d_in[0]; p.wqkv=(const char*)d_in[1]; p.wo=(const char*)d_in[2];
  p.q=(const char*)d_in[3]; p.k=(const char*)d_in[4]; p.v=(const char*)d_in[5];
  p.sk=(const char*)d_in[6]; p.hk=(const char*)d_in[7]; p.dv=(const char*)d_in[8];
  p.out=(char*)d_out; p.ws=(char*)d_ws;
  (void)hipFuncSetAttribute((const void*)fused,hipFuncAttributeMaxDynamicSharedMemorySize,LDS_BYTES);
  fused<<<256,512,LDS_BYTES,stream>>>(p);
}
```
